```python
import torch

def _softmax(x, axis=-1):
    return torch.softmax(x, dim=axis)
def _gelu(x):
    return torch.nn.functional.gelu(x, approximate="tanh")
def _silu(x):
    return torch.nn.functional.silu(x)
def _sigmoid(x):
    return torch.sigmoid(x)
def _layernorm(x, axis=-1, eps=1e-5):
    m = x.mean(dim=axis, keepdim=True)
    v = x.var(dim=axis, keepdim=True, unbiased=False)
    return (x - m) / torch.sqrt(v + eps)
def _scatter_add(tgt, idx, src, axis=0):
    return tgt.clone().index_add_(axis, idx, src)
def _segment_sum(src, seg, n):
    out = torch.zeros((n,) + src.shape[1:], dtype=src.dtype)
    return out.index_add_(0, seg, src)
def _pad(x, pw):
    flat = tuple(v for pair in reversed(pw) for v in pair)
    return torch.nn.functional.pad(x, flat)

kernel_name = 'concat_heads__ssm_conv_s4__softmax_full_mha__prompt'

def setup_inputs():

    _w = (lambda a: (lambda t: t.to(torch.bfloat16) if t.is_floating_point() else t)(torch.as_tensor(a)))
    def _A_setup():
        B, L, D, N = 1, 2048, 448, 256
        A_re = -(lambda _x: torch.exp(torch.as_tensor(_x)))((lambda *s: torch.randn(*s, dtype=torch.bfloat16) * 3.2)(D, N))
        A_im = torch.pi * (lambda *s: torch.randn(*s, dtype=torch.bfloat16) * 0.05)(D, N)
        B_re = (lambda *s: torch.randn(*s, dtype=torch.bfloat16) * 0.2)(D, N)
        B_im = (lambda *s: torch.randn(*s, dtype=torch.bfloat16) * 0.2)(D, N)
        C_re = (lambda *s: torch.randn(*s, dtype=torch.bfloat16) * 0.2)(D, N)
        C_im = (lambda *s: torch.randn(*s, dtype=torch.bfloat16) * 0.2)(D, N)
        log_dt = (lambda *s: torch.randn(*s, dtype=torch.bfloat16) * 0.05)(D) * 0.5 - 4.0
        Dskip = (lambda *s: torch.randn(*s, dtype=torch.bfloat16) * 0.1)(D)
        return {
            "u": _w((lambda *s: torch.randn(*s, dtype=torch.bfloat16) * 0.05)(B, L, D)),
            "A_re": _w(A_re),
            "A_im": _w(A_im),
            "B_re": _w(B_re),
            "B_im": _w(B_im),
            "C_re": _w(C_re),
            "C_im": _w(C_im),
            "log_dt": _w(log_dt),
            "Dskip": _w(Dskip),
        }
    _A_in = _A_setup()

    def _B_setup():
        B, S, D = 1, 2048, 448
        return {
            "x": _w((lambda *s: torch.randn(*s, dtype=torch.bfloat16) * 0.2)(B, S, D)),
            "wqkv": _w((lambda *s: torch.randn(*s, dtype=torch.bfloat16) * 0.2)(D, 3 * D)),
            "wo": _w((lambda *s: torch.randn(*s, dtype=torch.bfloat16) * 0.05)(D, D)),
        }
    _B_in = _B_setup()

    return {
        "A_u": _A_in["u"],
        "A_A_re": _A_in["A_re"],
        "A_A_im": _A_in["A_im"],
        "A_B_re": _A_in["B_re"],
        "A_B_im": _A_in["B_im"],
        "A_C_re": _A_in["C_re"],
        "A_C_im": _A_in["C_im"],
        "A_log_dt": _A_in["log_dt"],
        "A_Dskip": _A_in["Dskip"],
        "B_x": _B_in["x"],
        "B_wqkv": _B_in["wqkv"],
        "B_wo": _B_in["wo"],
    }

def reference(A_u, A_A_re, A_A_im, A_B_re, A_B_im, A_C_re, A_C_im, A_log_dt, A_Dskip, B_x, B_wqkv, B_wo):
    def _A_ref(u, A_re, A_im, B_re, B_im, C_re, C_im, log_dt, Dskip):
        Bsz, L, D = u.shape
        dt = torch.exp(log_dt)[:, None]
        A = A_re + 1j * A_im
        Bc = B_re + 1j * B_im
        Cc = C_re + 1j * C_im
        logdA = dt * A
        dB = dt * Bc
        l = (lambda x, dt: x.to(dt))(torch.arange(L), A_re.dtype)
        P = torch.exp(logdA[:, :, None] * l[None, None, :])
        K = torch.einsum("dn,dnl,dn->dl", Cc, P, dB).real
        L2 = 2 * L
        Kp = _pad(K, ((0, 0), (0, L)))
        up = (lambda x, *d: x.permute(*d))(u, 0, 2, 1)
        up = _pad(up, ((0, 0), (0, 0), (0, L)))
        Kf = (lambda x, axes=(-2,-1): torch.fft.rfft2(x.float(), dim=axes))(Kp[:, None, :], axes=(-2, -1))
        Uf = (lambda x, axes=(-2,-1): torch.fft.rfft2(x.float(), dim=axes))(up[:, :, None, :], axes=(-2, -1))
        Yf = Uf * Kf[None, :, :, :]
        y = (lambda x, s, axes=(-2,-1): torch.fft.irfft2(x, s=s, dim=axes).to(torch.bfloat16))(Yf, (1, L2), axes=(-2, -1))[:, :, 0, :L]
        y = (lambda x, *d: x.permute(*d))(y, 0, 2, 1) + u * Dskip[None, None, :]
        return y

    def _B_ref(x, wqkv, wo):
        B, S, D = x.shape
        H = 16; Dh = D // H
        qkv = (lambda x, *d: x.permute(*d))((x @ wqkv).reshape(B, S, 3, H, Dh), 2, 0, 3, 1, 4)
        q, k, v = qkv[0], qkv[1], qkv[2]
        scale = Dh ** -0.5
        scores = torch.einsum("bhqd,bhkd->bhqk", q, k) * scale
        p = _softmax(scores, axis=-1)
        o = torch.einsum("bhqk,bhkd->bhqd", p, v)
        return (lambda x, *d: x.permute(*d))(o, 0, 2, 1, 3).reshape(B, S, D) @ wo

    a = _A_ref(A_u, A_A_re, A_A_im, A_B_re, A_B_im, A_C_re, A_C_im, A_log_dt, A_Dskip)
    b = _B_ref(B_x, B_wqkv, B_wo)
    return torch.concatenate([a, b], axis=-1)
```

```cpp
#include <hip/hip_runtime.h>
#include <hip/hip_bf16.h>
#include <stdint.h>
#pragma clang diagnostic ignored "-Wint-to-pointer-cast"

typedef float f32x2 __attribute__((ext_vector_type(2)));
typedef float f32x4 __attribute__((ext_vector_type(4)));
typedef float f32x16 __attribute__((ext_vector_type(16)));
typedef __bf16 bf16x8 __attribute__((ext_vector_type(8)));
typedef short s16x4 __attribute__((ext_vector_type(4)));
typedef unsigned u32x2 __attribute__((ext_vector_type(2)));
typedef unsigned u32x4 __attribute__((ext_vector_type(4)));
#define LAS __attribute__((address_space(3)))
#define GAS __attribute__((address_space(1)))
typedef GAS unsigned gu32;
#define RLX_AGENT __ATOMIC_RELAXED, __HIP_MEMORY_SCOPE_AGENT
#define DEV __device__ __forceinline__

constexpr unsigned WS_FLAG = 0;
constexpr unsigned WS_HB = 32768;
constexpr unsigned WS_KO = 65536;
constexpr unsigned WS_VO = WS_KO + 64 * 28672;
constexpr unsigned WS_OF = WS_VO + 64 * 28672;
constexpr unsigned WS_UT = WS_OF + 64 * 32768;
constexpr unsigned UT_PITCH = 4224;
constexpr unsigned WS_YT = WS_UT + 448 * UT_PITCH;
constexpr unsigned WS_PROF = WS_YT + 448 * 4096;

constexpr unsigned L_SYNC = 64;
constexpr unsigned L_GEN = 256;
constexpr unsigned GEN_W = 8704;
constexpr unsigned L_KX = L_GEN + 4 * GEN_W;
constexpr unsigned KX_W = 4576;
constexpr unsigned L_RED = L_KX + 4 * KX_W + 128;
constexpr unsigned L_XS = L_RED;
constexpr unsigned L_WQ = L_RED + 16384;
constexpr unsigned L_W = L_RED + 49152;
constexpr unsigned L_END = L_W + 57344;
static_assert(L_END <= 163840 && (L_RED % 256) == 0 && (L_KX % 256) == 0, "lds map");
enum { C_W = 0, C_WQ, C_Q, C_KV, C_GO, C_U, C_F0, C_P0, C_P1, C_UC, C_EX, C_Y, C_WO, C_YT };

constexpr unsigned SPIN_CAP = 1u << 18;

struct Params {
    const char *u, *are, *aim, *bre, *bim, *cre, *cim, *logdt, *dskip, *x, *wqkv, *wo;
    char *out, *ws;
};

#define STAMP(k) do {} while (0)
#define PROF_ARG
#define PROF_PASS

typedef __bf16 bf16x2 __attribute__((ext_vector_type(2)));
DEV unsigned cvtpk(float lo, float hi) { return __builtin_bit_cast(unsigned, __builtin_convertvector((f32x2){lo, hi}, bf16x2)); }
DEV float bflo(unsigned v) { return __uint_as_float(v << 16); }
DEV float bfhi(unsigned v) { return __uint_as_float(v & 0xffff0000u); }
DEV bf16x8 asbf(u32x4 v) { return __builtin_bit_cast(bf16x8, v); }
DEV f32x16 mfma32(u32x4 a, u32x4 b, f32x16 c) { return __builtin_amdgcn_mfma_f32_32x32x16_bf16(asbf(a), asbf(b), c, 0, 0, 0); }
DEV f32x4 mfma16(u32x4 a, u32x4 b, f32x4 c) { return __builtin_amdgcn_mfma_f32_16x16x32_bf16(asbf(a), asbf(b), c, 0, 0, 0); }
DEV u32x4 lds_r128(unsigned a) { return *(const LAS u32x4*)a; }
DEV void lds_w128(unsigned a, u32x4 v) { *(LAS u32x4*)a = v; }
DEV u32x2 lds_tr(unsigned a) { return __builtin_bit_cast(u32x2, __builtin_amdgcn_ds_read_tr16_b64_v4i16((LAS s16x4*)a)); }
DEV u32x4 lds_tr2(unsigned a0, unsigned a1) { u32x2 x = lds_tr(a0), y = lds_tr(a1); return (u32x4){x[0], x[1], y[0], y[1]}; }
DEV void fence_c() { asm volatile("" ::: "memory"); }
#define WAIT_VM(n) asm volatile("s_waitcnt vmcnt(" #n ")" ::: "memory")
DEV void st16_sc1(const char* base, unsigned off, u32x4 v) { asm volatile("global_store_dwordx4 %0, %1, %2 sc1\n\ts_nop 1" :: "v"(off), "v"(v), "s"(base) : "memory"); }
DEV void st16(const char* base, unsigned off, u32x4 v) { asm volatile("global_store_dwordx4 %0, %1, %2\n\ts_nop 1" :: "v"(off), "v"(v), "s"(base) : "memory"); }
template <bool SC1> DEV void dma_run(const char* base, unsigned off, unsigned gstep, unsigned lds, unsigned lstep, unsigned n) {
    if (SC1)
        asm volatile("s_mov_b32 m0, %2\n"
                     "1:\n\ts_sub_u32 %1, %1, 1\n\tglobal_load_lds_dwordx4 %0, %3 sc1\n\tv_add_u32 %0, %4, %0\n\ts_add_u32 m0, m0, %5\n\ts_cmp_lg_u32 %1, 0\n\ts_cbranch_scc1 1b"
                     : "+v"(off), "+s"(n) : "s"(lds), "s"(base), "s"(gstep), "s"(lstep) : "memory", "scc");
    else
        asm volatile("s_mov_b32 m0, %2\n"
                     "1:\n\ts_sub_u32 %1, %1, 1\n\tglobal_load_lds_dwordx4 %0, %3\n\tv_add_u32 %0, %4, %0\n\ts_add_u32 m0, m0, %5\n\ts_cmp_lg_u32 %1, 0\n\ts_cbranch_scc1 1b"
                     : "+v"(off), "+s"(n) : "s"(lds), "s"(base), "s"(gstep), "s"(lstep) : "memory", "scc");
}
DEV unsigned arrive(int c, int lane) {
    unsigned old = 0;
    fence_c();
    if (lane == 0) old = __hip_atomic_fetch_add((LAS unsigned*)(L_SYNC + 4 * c), 1u, __ATOMIC_RELAXED, __HIP_MEMORY_SCOPE_WORKGROUP);
    fence_c();
    return __builtin_amdgcn_readfirstlane(old);
}
DEV void await(int c, unsigned n) {
#pragma unroll 1
    for (unsigned s = 0; s < (SPIN_CAP << 4); ++s) {
        fence_c();
        unsigned v = __hip_atomic_load((LAS unsigned*)(L_SYNC + 4 * c), __ATOMIC_RELAXED, __HIP_MEMORY_SCOPE_WORKGROUP);
        if ((unsigned)__builtin_amdgcn_readfirstlane(v) >= n) break;
        __builtin_amdgcn_s_sleep(1);
    }
    fence_c();
}
DEV void meet(int c, unsigned n, int lane) { if (arrive(c, lane) + 1 < n) await(c, n); }
DEV void poll_flags(const char* ws, unsigned off) {
_Pragma("unroll 1") for (unsigned s = 0; s < SPIN_CAP; ++s) {
        unsigned v = __hip_atomic_load((gu32*)(ws + off), RLX_AGENT);
        if (__all(v == 1u)) break;
        __builtin_amdgcn_s_sleep(1);
    }
    fence_c();
}
DEV void set_flag(char* ws, int b, int w, int lane) { if (lane == 0) __hip_atomic_store((gu32*)(ws + WS_FLAG + b * 128 + w * 4), 1u, RLX_AGENT); }
DEV int blk_of(int i, int j) { return ((i >> 3) * 4 + (j >> 2)) + 8 * ((i & 7) * 4 + (~j & 3)); }

#define CM1(t, x, y) "v_pk_mul_f32 " t ", " x ", " y " op_sel:[0,0] op_sel_hi:[1,0]\n\t"
#define CM2(t, x, y) "v_pk_fma_f32 " t ", " x ", " y ", " t " op_sel:[1,1,0] op_sel_hi:[0,1,1] neg_lo:[1,0,0]\n\t"
DEV f32x2 cmul(f32x2 x, f32x2 y) { f32x2 r; asm(CM1("%0", "%1", "%2") CM2("%0", "%1", "%2") : "=&v"(r) : "v"(x), "v"(y)); return r; }
DEV void cmul2(f32x2& r0, f32x2 x0, f32x2 y0, f32x2& r1, f32x2 x1, f32x2 y1) {
    asm(CM1("%0", "%2", "%3") CM1("%1", "%4", "%5") CM2("%0", "%2", "%3") CM2("%1", "%4", "%5") : "=&v"(r0), "=&v"(r1) : "v"(x0), "v"(y0), "v"(x1), "v"(y1)); }
DEV void cmul4(f32x2& r0, f32x2& r1, f32x2& r2, f32x2& r3, f32x2 x0, f32x2 x1, f32x2 x2, f32x2 x3, f32x2 y) {
    asm(CM1("%0", "%4", "%8") CM1("%1", "%5", "%8") CM1("%2", "%6", "%8") CM1("%3", "%7", "%8")
        CM2("%0", "%4", "%8") CM2("%1", "%5", "%8") CM2("%2", "%6", "%8") CM2("%3", "%7", "%8")
        : "=&v"(r0), "=&v"(r1), "=&v"(r2), "=&v"(r3) : "v"(x0), "v"(x1), "v"(x2), "v"(x3), "v"(y)); }
DEV f32x2 cexp2(float lg, float rev) {
    float m = __builtin_amdgcn_exp2f(lg), f = __builtin_amdgcn_fractf(rev);
    return (f32x2){m * __builtin_amdgcn_cosf(f), m * __builtin_amdgcn_sinf(f)}; }
template <int R0, int R1, int R2, int R3> DEV void wrow4(unsigned base, unsigned a, unsigned b, unsigned c, unsigned d) {
    asm volatile("s_mov_b32 m0, %0\n\ts_nop 0\n\tds_write_addtid_b32 %1 offset:%5\n\tds_write_addtid_b32 %2 offset:%6\n\tds_write_addtid_b32 %3 offset:%7\n\tds_write_addtid_b32 %4 offset:%8"
                 :: "s"(base), "v"(a), "v"(b), "v"(c), "v"(d), "i"(R0 * 272), "i"(R1 * 272), "i"(R2 * 272), "i"(R3 * 272) : "memory"); }
DEV unsigned pkE(f32x2 z) { return cvtpk(z[0], z[1]); }

#define EXP4(b, o0, o1, o2, o3) "v_exp_f32 v[" #b "+" #o0 "], v[" #b "+" #o0 "]\n\tv_exp_f32 v[" #b "+" #o1 "], v[" #b "+" #o1 "]\n\tv_exp_f32 v[" #b "+" #o2 "], v[" #b "+" #o2 "]\n\tv_exp_f32 v[" #b "+" #o3 "], v[" #b "+" #o3 "]\n\t"
#define EXP1(b, o) "v_exp_f32 v[" #b "+" #o "], v[" #b "+" #o "]\n\t"
#define QK0(sn, kb, q) "v_mfma_f32_32x32x16_bf16 v[" #sn ":" #sn "+15], v[" #kb ":" #kb "+3], " q ", 0\n\t"
#define QK1(sn, kb, q) "v_mfma_f32_32x32x16_bf16 v[" #sn ":" #sn "+15], v[" #kb "+4:" #kb "+7], " q ", v[" #sn ":" #sn "+15]\n\t"
#define PV0(o, vb, sp) "v_smfmac_f32_32x32x32_bf16 " o ", v[" #vb "+8:" #vb "+11], v[" #sp ":" #sp "+7], %[idx]\n\t"
#define PV1(o, vb, sp) "v_smfmac_f32_32x32x32_bf16 " o ", v[" #vb "+12:" #vb "+15], v[" #sp "+8:" #sp "+15], %[idx]\n\t"
#define STEP(sx, sn, kb, qa, qb, o, vb, sp) QK0(sn, kb, qa) EXP4(sx, 0, 1, 2, 3) QK1(sn, kb, qb) EXP4(sx, 4, 5, 6, 7) PV0(o, vb, sp) EXP4(sx, 8, 9, 10, 11) PV1(o, vb, sp) EXP4(sx, 12, 13, 14, 15)
#define LOADK(b) "global_load_dwordx4 v[" #b ":" #b "+3], %[ko], %[base] sc1\n\tglobal_load_dwordx4 v[" #b "+4:" #b "+7], %[ko], %[base] offset:1024 sc1\n\t"
#define LOADV(b) "s_mov_b64 exec, %[vm]\n\tglobal_load_dwordx4 v[" #b "+8:" #b "+11], %[vo0], %[base] sc1\n\tglobal_load_dwordx4 v[" #b "+12:" #b "+15], %[vo1], %[base] sc1\n\ts_mov_b64 exec, -1\n\t"
#define ADV "v_add_u32 %[ko], %[st], %[ko]\n\tv_add_u32 %[vo0], %[st], %[vo0]\n\tv_add_u32 %[vo1], %[st], %[vo1]\n\t"
#define LOADKV(b) LOADK(b) LOADV(b) ADV
#define STEP0(kb, vprev) STEP(128, 144, kb, "%[q10]", "%[q11]", "%[o3]", vprev, 176)
#define STEP12(kb) STEP(144, 160, kb, "%[q20]", "%[q21]", "%[o0]", kb, 128) STEP(160, 176, kb, "%[q30]", "%[q31]", "%[o1]", kb, 144)
#define STEP3(kb, knext) STEP(176, 128, knext, "%[q00]", "%[q01]", "%[o2]", kb, 160)
#define TILE(kb, vprev, knext, LD, W) STEP0(kb, vprev) LD STEP12(kb) "s_waitcnt vmcnt(" #W ")\n\t" STEP3(kb, knext)

DEV u32x4 pack8(const f32x16& c, int s) { return (u32x4){cvtpk(c[8 * s], c[8 * s + 1]), cvtpk(c[8 * s + 2], c[8 * s + 3]), cvtpk(c[8 * s + 4], c[8 * s + 5]), cvtpk(c[8 * s + 6], c[8 * s + 7])}; }

template <bool VT> DEV void kv_front(const Params& p, u32x4 (&xf)[28], int i, int j, int wid, int lane PROF_ARG) {
    const int w = wid, h = j, r = lane & 31, hb = lane >> 5;
    if (j < 14) dma_run<false>(p.wqkv + 896 + 128 * j, (unsigned)((w * 112 + (lane >> 3)) * 2688 + (((lane & 7) ^ ((lane >> 2) & 4)) << 4)), 8 * 2688, L_W + w * 14336, 1024, 14);
    {
        const char* xb = p.x + (size_t)(128 * i + 32 * w) * 896;
        const unsigned o0 = (lane >> 3) * 896 + (lane & 7) * 16;
#pragma unroll
        for (int c = 0; c < 7; ++c)
#pragma unroll
            for (int e = 0; e < 4; ++e) asm volatile("global_load_dwordx4 %0, %1, %2 offset:%3" : "=v"(xf[4 * c + e]) : "v"(o0 + e * 7168), "s"(xb), "i"(c * 128) : "memory");
    }
    dma_run<false>(p.wqkv + 56 * h - 8 * (h & 1), (unsigned)((w * 112 + (lane >> 2)) * 2688 + (lane & 3) * 16), 16 * 2688, L_WQ + w * 7168, 1024, 7);
    const unsigned xs_w = L_XS + w * 4096 + (lane >> 3) * 128 + (((lane & 7) ^ (lane >> 4)) << 4);
    unsigned xs_r[4];
#pragma unroll
    for (int e = 0; e < 4; ++e) xs_r[e] = L_XS + w * 4096 + r * 128 + (((2 * e + hb) ^ ((r >> 1) & 7)) << 4);
    const int tq = (lane >> 2) & 3, tp = lane & 3, tps = ((tp & 1) << 1) | (tp >> 1), tch = (lane >> 4) & 1;
    const unsigned wa0 = L_W + (8 * hb + tq) * 128 + ((tch * 32 + (VT ? tp : tps) * 8) ^ ((tq >> 1) << 6)), wa1 = wa0 ^ 64;
    STAMP(1);
    WAIT_VM(35);
    meet(C_W, 4, lane);
    STAMP(2);
    f32x16 c0 = {}, c1 = {};
#pragma unroll
    for (int c = 0; c < 7; ++c) {
        asm volatile("s_waitcnt vmcnt(%4)" : "+v"(xf[4 * c]), "+v"(xf[4 * c + 1]), "+v"(xf[4 * c + 2]), "+v"(xf[4 * c + 3]) : "i"(31 - 4 * c) : "memory");
#pragma unroll
        for (int e = 0; e < 4; ++e) lds_w128((xs_w ^ ((e & 1) << 6)) + e * 1024, xf[4 * c + e]);
        fence_c();
#pragma unroll
        for (int e = 0; e < 4; ++e) xf[4 * c + e] = lds_r128(xs_r[e]);
        fence_c();
#pragma unroll
        for (int e = 0; e < 4; ++e) {
            const int k = 4 * c + e;
            const u32x4 w0 = lds_tr2(wa0 + k * 2048, wa0 + k * 2048 + 512), w1 = lds_tr2(wa1 + k * 2048, wa1 + k * 2048 + 512);
            if (VT) { c0 = mfma32(xf[k], w0, c0); c1 = mfma32(xf[k], w1, c1); }
            else    { c0 = mfma32(w0, xf[k], c0); c1 = mfma32(w1, xf[k], c1); }
        }
    }
    const int kt = 4 * i + w;
    if (VT) {
        if (j < 14) {
            const char* base = p.ws + WS_VO + (unsigned)(kt * 28672 + (j - 7) * 1024);
            const unsigned off = hb * 7168 + r * 16;
            st16_sc1(base, off, pack8(c0, 0)); st16_sc1(base, off + 512, pack8(c1, 0));
            st16_sc1(base, off + 14336, pack8(c0, 1)); st16_sc1(base, off + 14336 + 512, pack8(c1, 1));
        }
    } else {
        const char* base = p.ws + WS_KO + (unsigned)(kt * 28672 + j * 4096);
        const unsigned off = hb * 512 + ((r & 0x13) | ((r & 4) << 1) | ((r & 8) >> 1)) * 16;
        st16_sc1(base, off, pack8(c0, 0)); st16_sc1(base, off + 1024, pack8(c0, 1));
        st16_sc1(base, off + 2048, pack8(c1, 0)); st16_sc1(base, off + 3072, pack8(c1, 1));
    }
}

DEV void stage_o(const Params& p, int tt, int wid, int lane) {
    const char* base = p.ws + WS_OF + (unsigned)(tt * 32768 + wid * 4096);
    u32x4 v0, v1, v2, v3;
_Pragma("unroll 1") for (unsigned s = 0; s < SPIN_CAP; ++s) {
        asm volatile("global_load_dwordx4 %0, %4, %5 sc1\n\tglobal_load_dwordx4 %1, %4, %5 offset:1024 sc1\n\tglobal_load_dwordx4 %2, %4, %5 offset:2048 sc1\n\t"
                     "global_load_dwordx4 %3, %4, %5 offset:3072 sc1\n\ts_waitcnt vmcnt(0)"
                     : "=&v"(v0), "=&v"(v1), "=&v"(v2), "=&v"(v3) : "v"(lane * 16), "s"(base) : "memory");
        unsigned m = 0;
#pragma unroll
        for (int e = 0; e < 4; ++e) m = max(max(m, max(v0[e], v1[e])), max(v2[e], v3[e]));
        if (__all(m != 0xffffffffu)) break;
    }
    const unsigned a = L_W + wid * 4096 + lane * 16;
    lds_w128(a, v0); lds_w128(a + 1024, v1); lds_w128(a + 2048, v2); lds_w128(a + 3072, v3);
    asm volatile("s_waitcnt lgkmcnt(0)\n\ts_barrier" ::: "memory");
}

DEV void chain_b(const Params& p, int b, int i, int j, int wid, int lane PROF_ARG) {
    const int w = wid, h = j, r = lane & 31, hb = lane >> 5;
    const int tq = (lane >> 2) & 3, tp = lane & 3, tps = ((tp & 1) << 1) | (tp >> 1), tch = (lane >> 4) & 1, kt = 4 * i + w;
    u32x4 xf[28];
    asm volatile("s_waitcnt lgkmcnt(0)\n\ts_barrier" ::: "memory");
    if (j >= 7) kv_front<true>(p, xf, i, j, wid, lane PROF_PASS); else kv_front<false>(p, xf, i, j, wid, lane PROF_PASS);
    STAMP(3);
    WAIT_VM(0);
    if (arrive(C_KV, lane) == 3) { await(C_GO, 1); set_flag(p.ws, b, 1, lane); } else await(C_KV, 4);
    STAMP(4);
    {
        const unsigned qa = L_WQ + (8 * hb + tq) * 64 + tch * 32 + tps * 8;
        f32x16 cq = {};
#pragma unroll
        for (int k = 0; k < 28; ++k) cq = mfma32(lds_tr2(qa + k * 1024, qa + k * 1024 + 256), xf[k], cq);
        const float sc = 0.27264371327785514f;
        const float slo = ((h & 1) && hb == 0) ? 0.f : sc, shi = (!(h & 1) && hb == 1) ? 0.f : sc;
#pragma unroll
        for (int e = 0; e < 16; ++e) cq[e] *= (e < 4 ? slo : e >= 12 ? shi : sc);
        lds_w128(L_XS + w * 4096 + lane * 16, pack8(cq, 0)); lds_w128(L_XS + w * 4096 + 1024 + lane * 16, pack8(cq, 1));
    }
    meet(C_Q, 4, lane);
    u32x4 q[4][2];
#pragma unroll
    for (int a = 0; a < 4; ++a) { const unsigned qa = L_XS + ((w + 1 + a) & 3) * 4096 + lane * 16; q[a][0] = lds_r128(qa); q[a][1] = lds_r128(qa + 1024); }
    arrive(C_Q, lane);
    STAMP(5);
    const int o0 = (7 * h) >> 1;
    await(C_GO, 1);
    {
        const int g = lane & 3, gj = g == 0 ? o0 >> 3 : g == 1 ? (o0 + 3) >> 3 : g == 2 ? 7 + ((28 * h) >> 6) : 7 + ((28 * h + 27) >> 6);
        poll_flags(p.ws, WS_FLAG + blk_of(4 * w + ((lane >> 2) & 3), gj) * 128 + 4);
    }
    STAMP(6);
    const unsigned ko0 = o0 * 512 + lane * 16;
    const unsigned vo00 = (WS_VO - WS_KO) + hb * 7168 + (28 * h + ((r & 0x13) | ((r & 4) << 1) | ((r & 8) >> 1))) * 16;
    f32x16 oa0 = {}, oa1 = {}, oa2 = {}, oa3 = {};
    {
        const char* base = p.ws + WS_KO + (unsigned)(16 * w * 28672);
        unsigned ko = ko0, vo0 = vo00, vo1 = vo00 + 14336, cnt = 3;
        asm volatile(
            ".irp n,200,201,202,203,204,205,206,207,216,217,218,219,220,221,222,223,232,233,234,235,236,237,238,239,248,249,250,251,252,253,254,255\n\t"
            "v_mov_b32 v[\\n], 0x3f803f80\n\t.endr\n\t"
            LOADKV(192) LOADKV(208) LOADKV(224)
            "s_waitcnt vmcnt(8)\n\t"
            QK0(128, 192, "%[q00]") QK1(128, 192, "%[q01]") QK0(144, 192, "%[q10]") QK1(144, 192, "%[q11]")
            "s_nop 7\n\ts_nop 1\n\t"
            EXP4(128, 0, 1, 2, 3) EXP4(128, 4, 5, 6, 7) EXP4(128, 8, 9, 10, 11) EXP4(128, 12, 13, 14, 15)
            "s_branch 2f\n"
            "1:\n\t"
            STEP0(192, 240)
            "2:\n\t"
            LOADKV(240) STEP12(192) "s_waitcnt vmcnt(8)\n\t" STEP3(192, 208)
            TILE(208, 192, 224, LOADKV(192), 8)
            TILE(224, 208, 240, LOADKV(208), 8)
            TILE(240, 224, 192, LOADKV(224), 8)
            "s_sub_u32 %[cnt], %[cnt], 1\n\ts_cmp_lg_u32 %[cnt], 0\n\ts_cbranch_scc1 1b\n\t"
            TILE(192, 240, 208, LOADKV(240), 8)
            TILE(208, 192, 224, "", 4)
            TILE(224, 208, 240, "", 0)
            STEP0(240, 224) STEP12(240)
            EXP4(176, 0, 1, 2, 3) PV0("%[o2]", 240, 160) EXP4(176, 4, 5, 6, 7) PV1("%[o2]", 240, 160) EXP4(176, 8, 9, 10, 11) PV0("%[o3]", 240, 176) EXP4(176, 12, 13, 14, 15)
            "s_nop 1\n\t" PV1("%[o3]", 240, 176)
            "s_nop 11"
            : [o0] "+v"(oa0), [o1] "+v"(oa1), [o2] "+v"(oa2), [o3] "+v"(oa3), [ko] "+v"(ko), [vo0] "+v"(vo0), [vo1] "+v"(vo1), [cnt] "+s"(cnt)
            : [q00] "v"(q[0][0]), [q01] "v"(q[0][1]), [q10] "v"(q[1][0]), [q11] "v"(q[1][1]), [q20] "v"(q[2][0]), [q21] "v"(q[2][1]), [q30] "v"(q[3][0]), [q31] "v"(q[3][1]),
              [idx] "v"(0xddddddddu), [base] "s"(base), [st] "s"(28672u), [vm] "s"(0x0fffffff0fffffffull)
            : "memory", "scc",
              "v128", "v129", "v130", "v131", "v132", "v133", "v134", "v135", "v136", "v137", "v138", "v139", "v140", "v141", "v142", "v143",
              "v144", "v145", "v146", "v147", "v148", "v149", "v150", "v151", "v152", "v153", "v154", "v155", "v156", "v157", "v158", "v159",
              "v160", "v161", "v162", "v163", "v164", "v165", "v166", "v167", "v168", "v169", "v170", "v171", "v172", "v173", "v174", "v175",
              "v176", "v177", "v178", "v179", "v180", "v181", "v182", "v183", "v184", "v185", "v186", "v187", "v188", "v189", "v190", "v191",
              "v192", "v193", "v194", "v195", "v196", "v197", "v198", "v199", "v200", "v201", "v202", "v203", "v204", "v205", "v206", "v207",
              "v208", "v209", "v210", "v211", "v212", "v213", "v214", "v215", "v216", "v217", "v218", "v219", "v220", "v221", "v222", "v223",
              "v224", "v225", "v226", "v227", "v228", "v229", "v230", "v231", "v232", "v233", "v234", "v235", "v236", "v237", "v238", "v239",
              "v240", "v241", "v242", "v243", "v244", "v245", "v246", "v247", "v248", "v249", "v250", "v251", "v252", "v253", "v254", "v255");
    }
    STAMP(7);
    await(C_Q, 8);
#pragma unroll
    for (int a = 0; a < 3; ++a) {
        const f32x16& o = a == 0 ? oa0 : a == 1 ? oa1 : oa2;
        const unsigned ra = L_RED + (((w + 1 + a) & 3) * 3 + a) * 4096 + lane * 16;
#pragma unroll
        for (int g = 0; g < 4; ++g) lds_w128(ra + g * 1024, (u32x4){__float_as_uint(o[4 * g]), __float_as_uint(o[4 * g + 1]), __float_as_uint(o[4 * g + 2]), __float_as_uint(o[4 * g + 3])});
    }
    asm volatile("s_waitcnt lgkmcnt(0)\n\ts_barrier" ::: "memory");
#pragma unroll
    for (int a = 0; a < 3; ++a)
#pragma unroll
        for (int g = 0; g < 4; ++g) { const u32x4 t = lds_r128(L_RED + (w * 3 + a) * 4096 + g * 1024 + lane * 16);
#pragma unroll
            for (int e = 0; e < 4; ++e) oa3[4 * g + e] += __uint_as_float(t[e]); }
    float l = __uint_as_float(__builtin_amdgcn_permlane32_swap(__float_as_uint(oa3[12]), __float_as_uint(oa3[12]), false, false)[1]);
    {
        float am = 0.f;
#pragma unroll
        for (int e = 0; e < 16; ++e) am = fmaxf(am, fabsf(oa3[e]));
        if (__builtin_expect(!__all(l >= 0x1p-10f && l <= 0x1p60f && am <= 0x1p120f), 0)) {
            poll_flags(p.ws, WS_FLAG + blk_of(lane >> 2, (lane & 3) == 0 ? o0 >> 3 : (lane & 3) == 1 ? (o0 + 3) >> 3 : (lane & 3) == 2 ? 7 + ((28 * h) >> 6) : 7 + ((28 * h + 27) >> 6)) * 128 + 4);
            const char* base = p.ws + WS_KO;
            float m = -INFINITY;
#pragma unroll 1
            for (int pass = 0; pass < 2; ++pass) {
                oa3 = (f32x16){};
#pragma unroll 1
                for (int t = 0; t < 64; ++t) {
                    u32x4 k0, k1, v0 = {0x3f803f80u, 0x3f803f80u, 0x3f803f80u, 0x3f803f80u}, v1 = v0;
                    asm volatile("global_load_dwordx4 %0, %4, %7 sc1\n\tglobal_load_dwordx4 %1, %4, %7 offset:1024 sc1\n\ts_mov_b64 exec, %8\n\t"
                                 "global_load_dwordx4 %2, %5, %7 sc1\n\tglobal_load_dwordx4 %3, %6, %7 sc1\n\ts_mov_b64 exec, -1\n\ts_waitcnt vmcnt(0)"
                                 : "=&v"(k0), "=&v"(k1), "+v"(v0), "+v"(v1) : "v"(ko0 + t * 28672), "v"(vo00 + t * 28672), "v"(vo00 + 14336 + t * 28672), "s"(base), "s"(0x0fffffff0fffffffull) : "memory");
                    f32x16 s = mfma32(k1, q[3][1], mfma32(k0, q[3][0], (f32x16){}));
                    if (pass == 0) {
#pragma unroll
                        for (int e = 0; e < 16; ++e) m = fmaxf(m, s[e]);
                    } else {
                        typedef __bf16 bf16x16 __attribute__((ext_vector_type(16)));
                        typedef float f32x8 __attribute__((ext_vector_type(8)));
                        f32x8 p0, p1;
#pragma unroll
                        for (int e = 0; e < 8; ++e) { p0[e] = __builtin_amdgcn_exp2f(s[e] - m); p1[e] = __builtin_amdgcn_exp2f(s[8 + e] - m); }
                        oa3 = __builtin_amdgcn_smfmac_f32_32x32x32_bf16(asbf(v0), __builtin_bit_cast(bf16x16, p0), oa3, (int)0xddddddddu, 0, 0);
                        oa3 = __builtin_amdgcn_smfmac_f32_32x32x32_bf16(asbf(v1), __builtin_bit_cast(bf16x16, p1), oa3, (int)0xddddddddu, 0, 0);
                    }
                }
                if (pass == 0) { auto sw = __builtin_amdgcn_permlane32_swap(__float_as_uint(m), __float_as_uint(m), false, false); m = fmaxf(__uint_as_float(sw[0]), __uint_as_float(sw[1])); }
            }
            l = __uint_as_float(__builtin_amdgcn_permlane32_swap(__float_as_uint(oa3[12]), __float_as_uint(oa3[12]), false, false)[1]);
        }
    }
    {
        const float rl = 1.0f / l, rh = hb ? 0.f : rl;
#pragma unroll
        for (int e = 0; e < 16; ++e) oa3[e] *= (e >= 12 ? rh : rl);
        u32x4 a0 = pack8(oa3, 0), a1 = pack8(oa3, 1);
#pragma unroll
        for (int e = 0; e < 4; ++e) { a0[e] = min(a0[e], 0xfffffffeu); a1[e] = min(a1[e], 0xfffffffeu); }
        const char* base = p.ws + WS_OF + (unsigned)(kt * 32768 + h * 2048);
        st16_sc1(base, lane * 16, a0); st16_sc1(base, lane * 16 + 1024, a1);
    }
    STAMP(8);
    stage_o(p, 4 * i + (j >> 2), wid, lane);
    STAMP(9);
}

DEV void gen_unit(f32x16& acc, float dt, float are, float aim, float bre, float bim, float cre, float cim, unsigned gb, unsigned gr) {
    const float lg = (dt * are) * 1.4426950408889634f, rv = (dt * aim) * 0.15915494309189535f;
    const f32x2 z1 = cexp2(lg, -rv), z8 = cexp2(8.f * lg, 8.f * rv), z256 = cexp2(256.f * lg, -256.f * rv);
    f32x2 f0 = cmul((f32x2){cre, -cim}, (f32x2){dt * bre, -dt * bim}), f1, f2, f3, f4, f5, f6, f7, g1, g2, g3, g4, g5, g6, g7, g8;
    cmul2(f1, f0, z1, g1, f0, z256); cmul2(f2, f1, z1, g2, g1, z256); cmul2(f3, f2, z1, g3, g2, z256);
    wrow4<0, 1, 2, 3>(gb, pkE(f0), pkE(f1), pkE(f2), pkE(f3));
    cmul2(f4, f3, z1, g4, g3, z256); cmul2(f5, f4, z1, g5, g4, z256); cmul2(f6, f5, z1, g6, g5, z256); cmul2(f7, f6, z1, g7, g6, z256);
    wrow4<4, 5, 6, 7>(gb, pkE(f4), pkE(f5), pkE(f6), pkE(f7));
    wrow4<16, 17, 18, 19>(gb, pkE(g4), pkE(g5), pkE(g6), pkE(g7));
    f32x2 h2, h4, h6, e2, e3, e4, i4, j4;
    cmul4(h2, h4, h6, g8, f2, f4, f6, g7, z256);
    wrow4<8, 9, 10, 11>(gb, pkE(g1), pkE(h2), pkE(h4), pkE(h6));
    cmul2(i4, h4, z256, e2, z8, z8); cmul2(j4, i4, z256, e3, e2, z8); e4 = cmul(e2, e2);
    wrow4<12, 13, 14, 15>(gb, pkE(g2), pkE(i4), pkE(g3), pkE(j4));
    { const unsigned t = pkE(g8); asm volatile("s_mov_b32 m0, %0\n\ts_nop 0\n\tds_write_addtid_b32 %1 offset:%2" :: "s"(gb), "v"(t), "i"(20 * 272) : "memory"); }
    u32x4 ff[8];
#pragma unroll
    for (int k = 0; k < 8; ++k) ff[k] = lds_r128(gr + k * 32);
    fence_c();
    f32x2 a0 = {1.f, 0.f}, a1 = z8, a2 = e2, a3 = e3;
    wrow4<0, 1, 2, 3>(gb, 0x00003f80u, pkE(a1), pkE(a2), pkE(a3));
#define ECH(R) { f32x2 n0, n1, n2, n3; cmul4(n0, n1, n2, n3, a0, a1, a2, a3, e4); a0 = n0; a1 = n1; a2 = n2; a3 = n3; wrow4<R, R + 1, R + 2, R + 3>(gb, pkE(a0), pkE(a1), pkE(a2), pkE(a3)); }
    ECH(4) ECH(8) ECH(12) ECH(16) ECH(20) ECH(24) ECH(28)
#undef ECH
#pragma unroll
    for (int k = 0; k < 8; ++k) acc = mfma32(ff[k], lds_r128(gr + k * 32), acc);
    fence_c();
}

DEV void chain_a(const Params& p, int b, int i, int j, int wid, int lane PROF_ARG) {
    const int aw = wid - 4, hb = lane >> 5, r = lane & 31;
    const int g = b & 7, m = b >> 3;
    const bool ssm = g < 7;
    gu32* hbw = (gu32*)(p.ws + WS_HB);
    const int ch = 64 * g + 2 * m + (aw >> 1), par = aw & 1;
    unsigned hb0, pa[6], ldt;
    asm volatile("global_load_dword %0, %1, %2 sc1" : "=v"(hb0) : "v"(0u), "s"(p.ws + WS_HB) : "memory");
    if (ssm) {
        dma_run<false>(p.u + 128 * g, (unsigned)((64 * m + 16 * aw + (lane >> 3)) * 896 + (((lane & 7) ^ ((lane >> 2) & 6)) << 4)), 8 * 896, L_KX + aw * 2048, 1024, 2);
        asm volatile("global_load_ushort %6, %8, %15\n\tglobal_load_dword %0, %7, %9\n\tglobal_load_dword %1, %7, %10\n\tglobal_load_dword %2, %7, %11\n\t"
                     "global_load_dword %3, %7, %12\n\tglobal_load_dword %4, %7, %13\n\tglobal_load_dword %5, %7, %14"
                     : "=&v"(pa[0]), "=&v"(pa[1]), "=&v"(pa[2]), "=&v"(pa[3]), "=&v"(pa[4]), "=&v"(pa[5]), "=&v"(ldt)
                     : "v"(ch * 512 + par * 256 + lane * 4), "v"(ch * 2), "s"(p.are), "s"(p.aim), "s"(p.bre), "s"(p.bim), "s"(p.cre), "s"(p.cim), "s"(p.logdt) : "memory");
    } else if (b == 7 && aw == 1) {
#pragma unroll
        for (int e = 0; e < 4; ++e) st16_sc1(p.ws + WS_FLAG, (e * 64 + lane) * 128, (u32x4){0u, 0u, 0u, 0u});
        asm volatile("s_waitcnt vmcnt(0)" : "+v"(hb0) :: "memory");
        if (lane == 0) __hip_atomic_store(hbw, hb0 + 1, RLX_AGENT);
    }
    {
        const char* base = p.ws + WS_OF + (unsigned)((4 * i + aw) * 32768 + j * 2048);
        st16_sc1(base, lane * 16, (u32x4){~0u, ~0u, ~0u, ~0u}); st16_sc1(base, lane * 16 + 1024, (u32x4){~0u, ~0u, ~0u, ~0u});
    }
    asm volatile("s_barrier" ::: "memory");
    STAMP(1);
    const unsigned gb = L_GEN + aw * GEN_W;
    const int tq = (lane >> 2) & 3, tp = lane & 3, G = lane >> 4;
    f32x16 acc = {};
    float dt = 0.f;
    if (ssm) {
        asm volatile("s_waitcnt vmcnt(2)" : "+v"(pa[0]), "+v"(pa[1]), "+v"(pa[2]), "+v"(pa[3]), "+v"(pa[4]), "+v"(pa[5]), "+v"(ldt) :: "memory");
        meet(C_U, 4, lane);
        const unsigned ta = L_KX + (16 * G + tq) * 128 + (((2 * aw + (tp >> 1)) ^ (2 * tq)) << 4) + (tp & 1) * 8;
        const u32x4 t0 = lds_tr2(ta, ta + 512), t1 = lds_tr2(ta + 1024, ta + 1536);
        lds_w128(gb + (lane & 15) * 144 + G * 32, t0); lds_w128(gb + (lane & 15) * 144 + G * 32 + 16, t1);
        fence_c();
        const u32x4 s0 = lds_r128(gb + (lane >> 3) * 144 + (lane & 7) * 16), s1 = lds_r128(gb + (8 + (lane >> 3)) * 144 + (lane & 7) * 16);
        const char* base = p.ws + WS_UT + (unsigned)((64 * g + 16 * aw) * UT_PITCH);
        const unsigned off = (lane >> 3) * UT_PITCH + 128 + 128 * m + (lane & 7) * 16;
        st16_sc1(base, off, s0); st16_sc1(base, off + 8 * UT_PITCH, s1);
        if (m == 0 && lane < 16) st16_sc1(base, lane * UT_PITCH + 112, (u32x4){0u, 0u, 0u, 0u});
        arrive(C_U, lane);
        STAMP(2);
        dt = bflo(cvtpk(expf(bflo(ldt)), 0.f));
        gen_unit(acc, dt, bflo(pa[0]), bflo(pa[1]), bflo(pa[2]), bflo(pa[3]), bflo(pa[4]), bflo(pa[5]), gb, gb + r * 272 + hb * 16);
    }
    STAMP(3);
    asm volatile("s_waitcnt vmcnt(0)" : "+v"(hb0) :: "memory");
    arrive(C_F0, lane);
    if (aw == 0) {
    _Pragma("unroll 1") for (unsigned s = 0; s < SPIN_CAP; ++s) { if ((unsigned)__builtin_amdgcn_readfirstlane(__hip_atomic_load(hbw, RLX_AGENT)) != (unsigned)__builtin_amdgcn_readfirstlane(hb0)) break; __builtin_amdgcn_s_sleep(1); }
        arrive(C_GO, lane);
        await(C_F0, 4);
        set_flag(p.ws, b, 0, lane);
    }
    STAMP(4);
    __builtin_amdgcn_s_setprio(2);
    if (b == 7 && aw == 1) {
        unsigned v = __hip_atomic_load(hbw, RLX_AGENT);
    _Pragma("unroll 1") for (unsigned s = 0; s < SPIN_CAP; ++s) {
            unsigned all = 1;
#pragma unroll
            for (int e = 0; e < 4; ++e) all &= __hip_atomic_load((gu32*)(p.ws + WS_FLAG + (e * 64 + lane) * 128), RLX_AGENT);
            if (__all(all == 1u)) break;
            ++v;
            if (lane == 0) __hip_atomic_store(hbw, v, RLX_AGENT);
            __builtin_amdgcn_s_sleep(4);
        }
    }
    if (ssm) {
        gen_unit(acc, dt, bfhi(pa[0]), bfhi(pa[1]), bfhi(pa[2]), bfhi(pa[3]), bfhi(pa[4]), bfhi(pa[5]), gb, gb + r * 272 + hb * 16);
        STAMP(5);
#pragma unroll
        for (int e = 0; e < 3; ++e) lds_w128(gb + e * 1024 + lane * 16, (u32x4){__float_as_uint(acc[4 * e]), __float_as_uint(acc[4 * e + 1]), __float_as_uint(acc[4 * e + 2]), __float_as_uint(acc[4 * e + 3])});
        meet(C_P0 + (aw >> 1), 2, lane);
#pragma unroll
        for (int e = 0; e < 3; ++e) { const u32x4 t = lds_r128(L_GEN + (aw ^ 1) * GEN_W + e * 1024 + lane * 16);
#pragma unroll
            for (int k = 0; k < 4; ++k) acc[4 * e + k] += __uint_as_float(t[k]); }
        const unsigned tb = gb + 4096 + 16;
        *(LAS u32x2*)(tb + (8 * r + 4 * hb) * 2) = (u32x2){cvtpk(acc[0], acc[1]), cvtpk(acc[2], acc[3])};
        *(LAS unsigned*)(tb + (hb ? 384 + 2 * r : 256 + 4 * r) * 2) = cvtpk(acc[4], acc[5]);
        *(LAS unsigned*)(tb + (hb ? 448 + 2 * r : 258 + 4 * r) * 2) = cvtpk(acc[6], acc[7]);
#pragma unroll
        for (int e = 0; e < 4; ++e) *(LAS unsigned short*)(tb + (512 + 128 * hb + 32 * e + r) * 2) = (unsigned short)cvtpk(acc[8 + e], 0.f);
        *(LAS unsigned*)(tb - 4) = 0u;
        await(C_U, 8);
        {
            const unsigned kx = L_KX + aw * KX_W;
            if (lane < 28) lds_w128(kx + lane * 16, (u32x4){0u, 0u, 0u, 0u});
            const unsigned LL = 0x01000100u, HH = 0x03020302u;
            {
                const unsigned ba = tb + (hb ? 256 + 4 * r : 8 * r) * 2;
                const unsigned x0 = *(LAS unsigned*)(ba - 4), x1 = *(LAS unsigned*)(ba), x2 = *(LAS unsigned*)(ba + 4), x3 = *(LAS unsigned*)(ba + 8), x4 = *(LAS unsigned*)(ba + (hb ? 4 : 12));
                const unsigned s0 = par ? 0x05040706u : 0x03020504u;
                const unsigned d0 = __builtin_amdgcn_perm(x4, x3, hb ? (par ? 0x07060100u : 0x05040706u) : s0);
                const unsigned d1 = __builtin_amdgcn_perm(x3, x2, hb ? LL : s0);
                const unsigned d2 = __builtin_amdgcn_perm(x2, x1, hb ? (par ? 0x03020504u : 0x01000302u) : s0);
                const unsigned d3 = __builtin_amdgcn_perm(x1, x0, (hb && (par || r)) ? 0x05040504u : s0);
                lds_w128(kx + (28 + lane) * 16, (u32x4){d0, d1, d2, d3});
            }
            {
                const unsigned ba = tb + (384 + 2 * lane) * 2, x = *(LAS unsigned*)(ba), y = *(LAS unsigned*)(ba + 4);
                lds_w128(kx + (92 + lane) * 16, (u32x4){__builtin_amdgcn_perm(y, x, par ? 0x05040504u : 0x03020504u), __builtin_amdgcn_perm(y, x, HH),
                                                       __builtin_amdgcn_perm(y, x, par ? 0x01000302u : LL), __builtin_amdgcn_perm(y, x, LL)});
            }
#pragma unroll
            for (int e = 0; e < 2; ++e) {
                const unsigned ba = tb + (512 + 64 * e + lane) * 2, x = *(LAS unsigned short*)(ba), y = *(LAS unsigned short*)(ba + 2);
                const unsigned v00 = __builtin_amdgcn_perm(y, x, LL);
                lds_w128(kx + (156 + 64 * e + lane) * 16, (u32x4){__builtin_amdgcn_perm(y, x, 0x05040504u),
                         __builtin_amdgcn_perm(y, x, (par == (lane & 1)) ? (par ? 0x05040504u : LL) : 0x01000504u), v00, v00});
            }
        }
        meet(C_EX, 4, lane);
        STAMP(6);
        await(C_GO, 1);
        poll_flags(p.ws, WS_FLAG + (g + 8 * r) * 128);
        {
            const unsigned uc = L_GEN + aw * 8320;
            if (lane < 6) lds_w128(uc + (lane >= 3 ? 4160 - 48 : 0) + lane * 16, (u32x4){0u, 0u, 0u, 0u});
            const char* base = p.ws + WS_UT + (unsigned)(ch * UT_PITCH + 128 - 12 + 8 * par);
            dma_run<true>(base, lane * 16, 1024, uc + 48, 1024, 4);
            dma_run<true>(base, lane * 16 + 4, 1024, uc + 4160 + 48, 1024, 4);
            WAIT_VM(0);
            meet(C_P0 + (aw >> 1), 4, lane);
        }
        STAMP(7);
        f32x4 y[8];
#pragma unroll
        for (int n = 0; n < 8; ++n) y[n] = (f32x4){};
        {
            const int mm = lane & 15;
            unsigned ua = L_GEN + (aw >> 1) * 16640 + (mm & 3) * 4160 + (G + (mm >> 2)) * 16 + par * 256;
            unsigned ka = L_KX + ((aw & 2) + (mm >> 3)) * KX_W + (28 + 4 * (mm & 7) - G + 3) * 16 - par * 256;
#pragma unroll 1
            for (int cc = 0; cc < 4; ++cc) {
                u32x4 af[8], bf[8];
#pragma unroll
                for (int a = 0; a < 8; ++a) { af[a] = lds_r128(ua + a * 512); bf[a] = lds_r128(ka + a * 512); }
#pragma unroll
                for (int a = 0; a < 8; ++a)
#pragma unroll
                    for (int n = 0; n + a < 8; ++n) y[a + n] = mfma16(af[a], bf[n], y[a + n]);
                ua += 64; ka -= 64;
            }
        }
        meet(C_P0 + (aw >> 1), 6, lane);
        {
            const unsigned yb = L_GEN + (aw >> 1) * 16640;
#pragma unroll
            for (int n = 0; n < 8; ++n) lds_w128(yb + par * 8192 + n * 1024 + (((lane & 7) * 4 + G) * 2 + ((lane >> 3) & 1)) * 16,
                                                 (u32x4){__float_as_uint(y[n][0]), __float_as_uint(y[n][1]), __float_as_uint(y[n][2]), __float_as_uint(y[n][3])});
            meet(C_P0 + (aw >> 1), 8, lane);
            const char* base = p.ws + WS_YT + (unsigned)(ch * 4096 + par * 2048);
#pragma unroll
            for (int e = 0; e < 2; ++e) {
                const unsigned a = yb + par * 4096 + e * 2048 + lane * 32;
                const u32x4 e0 = lds_r128(a), o0 = lds_r128(a + 16), e1 = lds_r128(a + 8192), o1 = lds_r128(a + 8192 + 16);
                u32x4 v;
#pragma unroll
                for (int k = 0; k < 4; ++k) v[k] = cvtpk(__uint_as_float(e0[k]) + __uint_as_float(e1[k]), __uint_as_float(o0[k]) + __uint_as_float(o1[k]));
                st16_sc1(base, e * 1024 + lane * 16, v);
            }
            WAIT_VM(0);
            if (arrive(C_Y, lane) == 3) set_flag(p.ws, b, 2, lane);
        }
        STAMP(8);
    }
    const int cs = j & 3, tt = 4 * i + (j >> 2);
    u32x4 wf[32];
    await(C_KV, 4);
    {
        const int tps = ((tp & 1) << 1) | (tp >> 1);
        const unsigned wa = L_W + (8 * hb + tq) * 256 + ((aw ^ tq) << 6) + (G & 1) * 32 + tps * 8;
        const unsigned wa_last = wa + (hb ? 0 : 1024);
        const unsigned so = (56 * aw + G) * 896 + ((((lane & 15) ^ (G << 2)) & (cs == 3 ? 7 : 15)) << 4);
#pragma unroll
        for (int hf = 0; hf < 2; ++hf) {
            dma_run<false>(p.wo + 256 * cs + hf * 224 * 896, so, 4 * 896, L_W + aw * 14336, 1024, 14);
            WAIT_VM(0);
            meet(C_WO, 4 + 8 * hf, lane);
#pragma unroll
            for (int hh = 0; hh < 8; ++hh) {
                wf[16 * hf + 2 * hh] = lds_tr2(wa + hh * 7168, wa + hh * 7168 + 1024);
                wf[16 * hf + 2 * hh + 1] = lds_tr2(wa + hh * 7168 + 4096, wa_last + hh * 7168 + 4096);
            }
            asm volatile("s_waitcnt lgkmcnt(0)" ::: "memory");
#pragma unroll
            for (int k = 0; k < 16; ++k) asm volatile("" : "+v"(wf[16 * hf + k]));
            meet(C_WO, 8 + 8 * hf, lane);
        }
    }
    STAMP(9);
    await(C_GO, 1);
    if (ssm) {
        const unsigned ro = (64 * m + 16 * aw + (lane >> 3));
        const u32x4 u0 = *(const GAS u32x4*)(p.u + ro * 896 + 128 * g + (lane & 7) * 16), u1 = *(const GAS u32x4*)(p.u + (ro + 8) * 896 + 128 * g + (lane & 7) * 16);
        const u32x4 dk = *(const GAS u32x4*)(p.dskip + 128 * g + (lane & 7) * 16);
        poll_flags(p.ws, WS_FLAG + (g + 8 * r) * 128 + 8);
        dma_run<true>(p.ws + WS_YT + (unsigned)(64 * g * 4096 + 128 * m), (unsigned)((16 * aw + (lane >> 3)) * 4096 + (((lane & 7) ^ ((lane >> 2) & 6)) << 4)), 8 * 4096, L_KX + aw * 2048, 1024, 2);
        WAIT_VM(0);
        meet(C_YT, 4, lane);
        const unsigned ta = L_KX + (16 * G + tq) * 128 + (((2 * aw + (tp >> 1)) ^ (2 * tq)) << 4) + (tp & 1) * 8;
        const u32x4 t0 = lds_tr2(ta, ta + 512), t1 = lds_tr2(ta + 1024, ta + 1536);
        lds_w128(gb + (lane & 15) * 144 + G * 32, t0); lds_w128(gb + (lane & 15) * 144 + G * 32 + 16, t1);
        fence_c();
#pragma unroll
        for (int e = 0; e < 2; ++e) {
            const u32x4 s = lds_r128(gb + (8 * e + (lane >> 3)) * 144 + (lane & 7) * 16), uu = e ? u1 : u0;
            u32x4 v;
#pragma unroll
            for (int k = 0; k < 4; ++k) { const unsigned sk = cvtpk(bflo(uu[k]) * bflo(dk[k]), bfhi(uu[k]) * bfhi(dk[k])); v[k] = cvtpk(bflo(s[k]) + bflo(sk), bfhi(s[k]) + bfhi(sk)); }
            st16(p.out + 128 * g, (ro + 8 * e) * 1792 + (lane & 7) * 16, v);
        }
    }
    poll_flags(p.ws, WS_FLAG + blk_of(i, lane & 15) * 128);
    STAMP(10);
    asm volatile("s_waitcnt lgkmcnt(0)\n\ts_barrier" ::: "memory");
    stage_o(p, tt, wid, lane);
    STAMP(11);
    f32x16 oc = {};
#pragma unroll
    for (int k = 0; k < 32; ++k) oc = mfma32(wf[k], lds_r128(L_W + k * 1024 + lane * 16), oc);
    lds_w128(gb + r * 80 + hb * 16, pack8(oc, 0)); lds_w128(gb + r * 80 + 32 + hb * 16, pack8(oc, 1));
    fence_c();
    if (cs < 3 || aw < 2) {
#pragma unroll
        for (int e = 0; e < 2; ++e)
            st16(p.out + 896 + 256 * cs + 64 * aw, (32 * tt + 16 * e + (lane >> 2)) * 1792 + (lane & 3) * 16, lds_r128(gb + (16 * e + (lane >> 2)) * 80 + (lane & 3) * 16));
    }
    STAMP(12);
}

__global__ void __launch_bounds__(512, 2) fused_s4_mha(Params p) {
    extern __shared__ __attribute__((aligned(256))) char smem[];
    const int lane = threadIdx.x & 63, wid = __builtin_amdgcn_readfirstlane(threadIdx.x >> 6), b = blockIdx.x;
    STAMP(0);
    asm volatile("" :: "s"(p.u), "s"(p.logdt), "s"(p.x), "s"(p.ws));
    if (threadIdx.x < 32) *(LAS unsigned*)(L_SYNC + 4 * threadIdx.x) = 0u;
    const int i = ((b >> 2) & 1) * 8 + (b >> 5), j = (b & 3) * 4 + (~(b >> 3) & 3);
    if (wid < 4) chain_b(p, b, i, j, wid, lane PROF_PASS); else chain_a(p, b, i, j, wid, lane PROF_PASS);
}

extern "C" void kernel_launch(void* const* d_in, const int* in_sizes, int n_in,
                              void* d_out, int out_size, void* d_ws, size_t ws_size,
                              hipStream_t stream) {
    Params p;
    p.u = (const char*)d_in[0]; p.are = (const char*)d_in[1]; p.aim = (const char*)d_in[2]; p.bre = (const char*)d_in[3]; p.bim = (const char*)d_in[4];
    p.cre = (const char*)d_in[5]; p.cim = (const char*)d_in[6]; p.logdt = (const char*)d_in[7]; p.dskip = (const char*)d_in[8];
    p.x = (const char*)d_in[9]; p.wqkv = (const char*)d_in[10]; p.wo = (const char*)d_in[11];
    p.out = (char*)d_out; p.ws = (char*)d_ws;
    (void)hipFuncSetAttribute((const void*)fused_s4_mha, hipFuncAttributeMaxDynamicSharedMemorySize, L_END);
    fused_s4_mha<<<256, 512, L_END, stream>>>(p);
}
```

```cpp
#include <hip/hip_runtime.h>
#include <hip/hip_bf16.h>
#include <stdint.h>
#define PF 1
#pragma clang diagnostic ignored "-Wint-to-pointer-cast"

typedef float f32x2 __attribute__((ext_vector_type(2)));
typedef float f32x4 __attribute__((ext_vector_type(4)));
typedef float f32x16 __attribute__((ext_vector_type(16)));
typedef __bf16 bf16x8 __attribute__((ext_vector_type(8)));
typedef short s16x4 __attribute__((ext_vector_type(4)));
typedef unsigned u32x2 __attribute__((ext_vector_type(2)));
typedef unsigned u32x4 __attribute__((ext_vector_type(4)));
#define LAS __attribute__((address_space(3)))
#define GAS __attribute__((address_space(1)))
typedef GAS unsigned gu32;
#define RLX_AGENT __ATOMIC_RELAXED, __HIP_MEMORY_SCOPE_AGENT
#define DEV __device__ __forceinline__

constexpr unsigned WS_FLAG = 0;
constexpr unsigned WS_HB = 32768;
constexpr unsigned WS_KO = 65536;
constexpr unsigned WS_VO = WS_KO + 64 * 28672;
constexpr unsigned WS_OF = WS_VO + 64 * 28672;
constexpr unsigned WS_UT = WS_OF + 64 * 32768;
constexpr unsigned UT_PITCH = 4224;
constexpr unsigned WS_YT = WS_UT + 448 * UT_PITCH;
constexpr unsigned WS_PROF = WS_YT + 448 * 4096;

constexpr unsigned L_SYNC = 64;
constexpr unsigned L_GEN = 256;
constexpr unsigned GEN_W = 8704;
constexpr unsigned L_KX = L_GEN + 4 * GEN_W;
constexpr unsigned KX_W = 4576;
constexpr unsigned L_RED = L_KX + 4 * KX_W + 128;
constexpr unsigned L_XS = L_RED;
constexpr unsigned L_WQ = L_RED + 16384;
constexpr unsigned L_W = L_RED + 49152;
constexpr unsigned L_END = L_W + 57344;
static_assert(L_END + 256 <= 163840 && (L_RED % 256) == 0 && (L_KX % 256) == 0, "lds map");
enum { C_W = 0, C_WQ, C_Q, C_KV, C_GO, C_U, C_F0, C_P0, C_P1, C_UC, C_EX, C_Y, C_WO, C_YT };

constexpr unsigned SPIN_CAP = 1u << 18;

struct Params {
    const char *u, *are, *aim, *bre, *bim, *cre, *cim, *logdt, *dskip, *x, *wqkv, *wo;
    char *out, *ws;
};

#define STAMP(k) do {} while (0)
#define PROF_ARG
#define PROF_PASS

typedef __bf16 bf16x2 __attribute__((ext_vector_type(2)));
DEV unsigned cvtpk(float lo, float hi) { return __builtin_bit_cast(unsigned, __builtin_convertvector((f32x2){lo, hi}, bf16x2)); }
DEV float bflo(unsigned v) { return __uint_as_float(v << 16); }
DEV float bfhi(unsigned v) { return __uint_as_float(v & 0xffff0000u); }
DEV bf16x8 asbf(u32x4 v) { return __builtin_bit_cast(bf16x8, v); }
DEV f32x16 mfma32(u32x4 a, u32x4 b, f32x16 c) { return __builtin_amdgcn_mfma_f32_32x32x16_bf16(asbf(a), asbf(b), c, 0, 0, 0); }
DEV f32x4 mfma16(u32x4 a, u32x4 b, f32x4 c) { return __builtin_amdgcn_mfma_f32_16x16x32_bf16(asbf(a), asbf(b), c, 0, 0, 0); }
DEV u32x4 lds_r128(unsigned a) { return *(const LAS u32x4*)a; }
DEV void lds_w128(unsigned a, u32x4 v) { *(LAS u32x4*)a = v; }
DEV u32x2 lds_tr(unsigned a) { return __builtin_bit_cast(u32x2, __builtin_amdgcn_ds_read_tr16_b64_v4i16((LAS s16x4*)a)); }
DEV u32x4 lds_tr2(unsigned a0, unsigned a1) { u32x2 x = lds_tr(a0), y = lds_tr(a1); return (u32x4){x[0], x[1], y[0], y[1]}; }
DEV void fence_c() { asm volatile("" ::: "memory"); }
#define WAIT_VM(n) asm volatile("s_waitcnt vmcnt(" #n ")" ::: "memory")
DEV void st16_sc1(const char* base, unsigned off, u32x4 v) { asm volatile("global_store_dwordx4 %0, %1, %2 sc1\n\ts_nop 1" :: "v"(off), "v"(v), "s"(base) : "memory"); }
DEV void st16(const char* base, unsigned off, u32x4 v) { asm volatile("global_store_dwordx4 %0, %1, %2\n\ts_nop 1" :: "v"(off), "v"(v), "s"(base) : "memory"); }
template <bool SC1> DEV void dma_run(const char* base, unsigned off, unsigned gstep, unsigned lds, unsigned lstep, unsigned n) {
    if (SC1)
        asm volatile("s_mov_b32 m0, %2\n"
                     "1:\n\ts_sub_u32 %1, %1, 1\n\tglobal_load_lds_dwordx4 %0, %3 sc1\n\tv_add_u32 %0, %4, %0\n\ts_add_u32 m0, m0, %5\n\ts_cmp_lg_u32 %1, 0\n\ts_cbranch_scc1 1b"
                     : "+v"(off), "+s"(n) : "s"(lds), "s"(base), "s"(gstep), "s"(lstep) : "memory", "scc");
    else
        asm volatile("s_mov_b32 m0, %2\n"
                     "1:\n\ts_sub_u32 %1, %1, 1\n\tglobal_load_lds_dwordx4 %0, %3\n\tv_add_u32 %0, %4, %0\n\ts_add_u32 m0, m0, %5\n\ts_cmp_lg_u32 %1, 0\n\ts_cbranch_scc1 1b"
                     : "+v"(off), "+s"(n) : "s"(lds), "s"(base), "s"(gstep), "s"(lstep) : "memory", "scc");
}
DEV unsigned arrive(int c, int lane) {
    unsigned old = 0;
    fence_c();
    if (lane == 0) old = __hip_atomic_fetch_add((LAS unsigned*)(L_SYNC + 4 * c), 1u, __ATOMIC_RELAXED, __HIP_MEMORY_SCOPE_WORKGROUP);
    fence_c();
    return __builtin_amdgcn_readfirstlane(old);
}
DEV void await(int c, unsigned n) {
#pragma unroll 1
    for (unsigned s = 0; s < (SPIN_CAP << 4); ++s) {
        fence_c();
        unsigned v = __hip_atomic_load((LAS unsigned*)(L_SYNC + 4 * c), __ATOMIC_RELAXED, __HIP_MEMORY_SCOPE_WORKGROUP);
        if ((unsigned)__builtin_amdgcn_readfirstlane(v) >= n) break;
        __builtin_amdgcn_s_sleep(1);
    }
    fence_c();
}
DEV void meet(int c, unsigned n, int lane) { if (arrive(c, lane) + 1 < n) await(c, n); }
DEV void poll_flags(const char* ws, unsigned off) {
_Pragma("unroll 1") for (unsigned s = 0; s < SPIN_CAP; ++s) {
        unsigned v = __hip_atomic_load((gu32*)(ws + off), RLX_AGENT);
        if (__all(v == 1u)) break;
        __builtin_amdgcn_s_sleep(1);
    }
    fence_c();
}
DEV void set_flag(char* ws, int b, int w, int lane) { if (lane == 0) __hip_atomic_store((gu32*)(ws + WS_FLAG + b * 128 + w * 4), 1u, RLX_AGENT); }
DEV int blk_of(int i, int j) { return ((i >> 3) * 4 + (j >> 2)) + 8 * ((i & 7) * 4 + (~j & 3)); }

#define CM1(t, x, y) "v_pk_mul_f32 " t ", " x ", " y " op_sel:[0,0] op_sel_hi:[1,0]\n\t"
#define CM2(t, x, y) "v_pk_fma_f32 " t ", " x ", " y ", " t " op_sel:[1,1,0] op_sel_hi:[0,1,1] neg_lo:[1,0,0]\n\t"
DEV f32x2 cmul(f32x2 x, f32x2 y) { f32x2 r; asm(CM1("%0", "%1", "%2") CM2("%0", "%1", "%2") : "=&v"(r) : "v"(x), "v"(y)); return r; }
DEV void cmul2(f32x2& r0, f32x2 x0, f32x2 y0, f32x2& r1, f32x2 x1, f32x2 y1) {
    asm(CM1("%0", "%2", "%3") CM1("%1", "%4", "%5") CM2("%0", "%2", "%3") CM2("%1", "%4", "%5") : "=&v"(r0), "=&v"(r1) : "v"(x0), "v"(y0), "v"(x1), "v"(y1)); }
DEV void cmul4(f32x2& r0, f32x2& r1, f32x2& r2, f32x2& r3, f32x2 x0, f32x2 x1, f32x2 x2, f32x2 x3, f32x2 y) {
    asm(CM1("%0", "%4", "%8") CM1("%1", "%5", "%8") CM1("%2", "%6", "%8") CM1("%3", "%7", "%8")
        CM2("%0", "%4", "%8") CM2("%1", "%5", "%8") CM2("%2", "%6", "%8") CM2("%3", "%7", "%8")
        : "=&v"(r0), "=&v"(r1), "=&v"(r2), "=&v"(r3) : "v"(x0), "v"(x1), "v"(x2), "v"(x3), "v"(y)); }
DEV f32x2 cexp2(float lg, float rev) {
    float m = __builtin_amdgcn_exp2f(lg), f = __builtin_amdgcn_fractf(rev);
    return (f32x2){m * __builtin_amdgcn_cosf(f), m * __builtin_amdgcn_sinf(f)}; }
template <int R0, int R1, int R2, int R3> DEV void wrow4(unsigned base, unsigned a, unsigned b, unsigned c, unsigned d) {
    asm volatile("s_mov_b32 m0, %0\n\ts_nop 0\n\tds_write_addtid_b32 %1 offset:%5\n\tds_write_addtid_b32 %2 offset:%6\n\tds_write_addtid_b32 %3 offset:%7\n\tds_write_addtid_b32 %4 offset:%8"
                 :: "s"(base), "v"(a), "v"(b), "v"(c), "v"(d), "i"(R0 * 272), "i"(R1 * 272), "i"(R2 * 272), "i"(R3 * 272) : "memory"); }
DEV unsigned pkE(f32x2 z) { return cvtpk(z[0], z[1]); }

#define EXP4(b, o0, o1, o2, o3) "v_exp_f32 v[" #b "+" #o0 "], v[" #b "+" #o0 "]\n\tv_exp_f32 v[" #b "+" #o1 "], v[" #b "+" #o1 "]\n\tv_exp_f32 v[" #b "+" #o2 "], v[" #b "+" #o2 "]\n\tv_exp_f32 v[" #b "+" #o3 "], v[" #b "+" #o3 "]\n\t"
#define EXP1(b, o) "v_exp_f32 v[" #b "+" #o "], v[" #b "+" #o "]\n\t"
#define QK0(sn, kb, q) "v_mfma_f32_32x32x16_bf16 v[" #sn ":" #sn "+15], v[" #kb ":" #kb "+3], " q ", 0\n\t"
#define QK1(sn, kb, q) "v_mfma_f32_32x32x16_bf16 v[" #sn ":" #sn "+15], v[" #kb "+4:" #kb "+7], " q ", v[" #sn ":" #sn "+15]\n\t"
#define PV0(o, vb, sp) "v_smfmac_f32_32x32x32_bf16 " o ", v[" #vb "+8:" #vb "+11], v[" #sp ":" #sp "+7], %[idx]\n\t"
#define PV1(o, vb, sp) "v_smfmac_f32_32x32x32_bf16 " o ", v[" #vb "+12:" #vb "+15], v[" #sp "+8:" #sp "+15], %[idx]\n\t"
#define STEP(sx, sn, kb, qa, qb, o, vb, sp) QK0(sn, kb, qa) EXP4(sx, 0, 1, 2, 3) QK1(sn, kb, qb) EXP4(sx, 4, 5, 6, 7) PV0(o, vb, sp) EXP4(sx, 8, 9, 10, 11) PV1(o, vb, sp) EXP4(sx, 12, 13, 14, 15)
#define LOADK(b) "global_load_dwordx4 v[" #b ":" #b "+3], %[ko], %[base] sc1\n\tglobal_load_dwordx4 v[" #b "+4:" #b "+7], %[ko], %[base] offset:1024 sc1\n\t"
#define LOADV(b) "s_mov_b64 exec, %[vm]\n\tglobal_load_dwordx4 v[" #b "+8:" #b "+11], %[vo0], %[base] sc1\n\tglobal_load_dwordx4 v[" #b "+12:" #b "+15], %[vo1], %[base] sc1\n\ts_mov_b64 exec, -1\n\t"
#define ADV "v_add_u32 %[ko], %[st], %[ko]\n\tv_add_u32 %[vo0], %[st], %[vo0]\n\tv_add_u32 %[vo1], %[st], %[vo1]\n\t"
#define LOADKV(b) LOADK(b) LOADV(b) ADV
#define STEP0(kb, vprev) STEP(128, 144, kb, "%[q10]", "%[q11]", "%[o3]", vprev, 176)
#define STEP12(kb) STEP(144, 160, kb, "%[q20]", "%[q21]", "%[o0]", kb, 128) STEP(160, 176, kb, "%[q30]", "%[q31]", "%[o1]", kb, 144)
#define STEP3(kb, knext) STEP(176, 128, knext, "%[q00]", "%[q01]", "%[o2]", kb, 160)
#define TILE(kb, vprev, knext, LD, W) STEP0(kb, vprev) LD STEP12(kb) "s_waitcnt vmcnt(" #W ")\n\t" STEP3(kb, knext)

DEV u32x4 pack8(const f32x16& c, int s) { return (u32x4){cvtpk(c[8 * s], c[8 * s + 1]), cvtpk(c[8 * s + 2], c[8 * s + 3]), cvtpk(c[8 * s + 4], c[8 * s + 5]), cvtpk(c[8 * s + 6], c[8 * s + 7])}; }

template <bool VT> DEV void kv_front(const Params& p, u32x4 (&xf)[28], int i, int j, int wid, int lane PROF_ARG) {
    const int w = wid, h = j, r = lane & 31, hb = lane >> 5;
    if (j < 14) dma_run<false>(p.wqkv + 896 + 128 * j, (unsigned)((w * 112 + (lane >> 3)) * 2688 + (((lane & 7) ^ ((lane >> 2) & 4)) << 4)), 8 * 2688, L_W + w * 14336, 1024, 14);
    {
        const char* xb = p.x + (size_t)(128 * i + 32 * w) * 896;
        const unsigned o0 = (lane >> 3) * 896 + (lane & 7) * 16;
#pragma unroll
        for (int c = 0; c < 7; ++c)
#pragma unroll
            for (int e = 0; e < 4; ++e) asm volatile("global_load_dwordx4 %0, %1, %2 offset:%3" : "=v"(xf[4 * c + e]) : "v"(o0 + e * 7168), "s"(xb), "i"(c * 128) : "memory");
    }
    dma_run<false>(p.wqkv + 56 * h - 8 * (h & 1), (unsigned)((w * 112 + (lane >> 2)) * 2688 + (lane & 3) * 16), 16 * 2688, L_WQ + w * 7168, 1024, 7);
    const unsigned xs_w = L_XS + w * 4096 + (lane >> 3) * 128 + (((lane & 7) ^ (lane >> 4)) << 4);
    unsigned xs_r[4];
#pragma unroll
    for (int e = 0; e < 4; ++e) xs_r[e] = L_XS + w * 4096 + r * 128 + (((2 * e + hb) ^ ((r >> 1) & 7)) << 4);
    const int tq = (lane >> 2) & 3, tp = lane & 3, tps = ((tp & 1) << 1) | (tp >> 1), tch = (lane >> 4) & 1;
    const unsigned wa0 = L_W + (8 * hb + tq) * 128 + ((tch * 32 + (VT ? tp : tps) * 8) ^ ((tq >> 1) << 6)), wa1 = wa0 ^ 64;
    STAMP(1);
    WAIT_VM(35);
    meet(C_W, 4, lane);
    STAMP(2);
    f32x16 c0 = {}, c1 = {};
#pragma unroll
    for (int c = 0; c < 7; ++c) {
        asm volatile("s_waitcnt vmcnt(%4)" : "+v"(xf[4 * c]), "+v"(xf[4 * c + 1]), "+v"(xf[4 * c + 2]), "+v"(xf[4 * c + 3]) : "i"(31 - 4 * c) : "memory");
#pragma unroll
        for (int e = 0; e < 4; ++e) lds_w128((xs_w ^ ((e & 1) << 6)) + e * 1024, xf[4 * c + e]);
        fence_c();
#pragma unroll
        for (int e = 0; e < 4; ++e) xf[4 * c + e] = lds_r128(xs_r[e]);
        fence_c();
#pragma unroll
        for (int e = 0; e < 4; ++e) {
            const int k = 4 * c + e;
            const u32x4 w0 = lds_tr2(wa0 + k * 2048, wa0 + k * 2048 + 512), w1 = lds_tr2(wa1 + k * 2048, wa1 + k * 2048 + 512);
            if (VT) { c0 = mfma32(xf[k], w0, c0); c1 = mfma32(xf[k], w1, c1); }
            else    { c0 = mfma32(w0, xf[k], c0); c1 = mfma32(w1, xf[k], c1); }
        }
    }
    const int kt = 4 * i + w;
    if (VT) {
        if (j < 14) {
            const char* base = p.ws + WS_VO + (unsigned)(kt * 28672 + (j - 7) * 1024);
            const unsigned off = hb * 7168 + r * 16;
            st16_sc1(base, off, pack8(c0, 0)); st16_sc1(base, off + 512, pack8(c1, 0));
            st16_sc1(base, off + 14336, pack8(c0, 1)); st16_sc1(base, off + 14336 + 512, pack8(c1, 1));
        }
    } else {
        const char* base = p.ws + WS_KO + (unsigned)(kt * 28672 + j * 4096);
        const unsigned off = hb * 512 + ((r & 0x13) | ((r & 4) << 1) | ((r & 8) >> 1)) * 16;
        st16_sc1(base, off, pack8(c0, 0)); st16_sc1(base, off + 1024, pack8(c0, 1));
        st16_sc1(base, off + 2048, pack8(c1, 0)); st16_sc1(base, off + 3072, pack8(c1, 1));
    }
}

DEV void stage_o(const Params& p, int tt, int wid, int lane) {
    const char* base = p.ws + WS_OF + (unsigned)(tt * 32768 + wid * 4096);
    u32x4 v0, v1, v2, v3;
_Pragma("unroll 1") for (unsigned s = 0; s < SPIN_CAP; ++s) {
        asm volatile("global_load_dwordx4 %0, %4, %5 sc1\n\tglobal_load_dwordx4 %1, %4, %5 offset:1024 sc1\n\tglobal_load_dwordx4 %2, %4, %5 offset:2048 sc1\n\t"
                     "global_load_dwordx4 %3, %4, %5 offset:3072 sc1\n\ts_waitcnt vmcnt(0)"
                     : "=&v"(v0), "=&v"(v1), "=&v"(v2), "=&v"(v3) : "v"(lane * 16), "s"(base) : "memory");
        unsigned m = 0;
#pragma unroll
        for (int e = 0; e < 4; ++e) m = max(max(m, max(v0[e], v1[e])), max(v2[e], v3[e]));
        if (__all(m != 0xffffffffu)) break;
    }
    const unsigned a = L_W + wid * 4096 + lane * 16;
    lds_w128(a, v0); lds_w128(a + 1024, v1); lds_w128(a + 2048, v2); lds_w128(a + 3072, v3);
    asm volatile("s_waitcnt lgkmcnt(0)\n\ts_barrier" ::: "memory");
}

DEV void chain_b(const Params& p, int b, int i, int j, int wid, int lane PROF_ARG) {
    const int w = wid, h = j, r = lane & 31, hb = lane >> 5;
    const int tq = (lane >> 2) & 3, tp = lane & 3, tps = ((tp & 1) << 1) | (tp >> 1), tch = (lane >> 4) & 1, kt = 4 * i + w;
    u32x4 xf[28];
    asm volatile("s_waitcnt lgkmcnt(0)\n\ts_barrier" ::: "memory");
    if (j >= 7) kv_front<true>(p, xf, i, j, wid, lane PROF_PASS); else kv_front<false>(p, xf, i, j, wid, lane PROF_PASS);
    STAMP(3);
    WAIT_VM(0);
    if (arrive(C_KV, lane) == 3) { await(C_GO, 1); set_flag(p.ws, b, 1, lane); } else await(C_KV, 4);
    STAMP(4);
    {
        const unsigned qa = L_WQ + (8 * hb + tq) * 64 + tch * 32 + tps * 8;
        f32x16 cq = {};
#pragma unroll
        for (int k = 0; k < 28; ++k) cq = mfma32(lds_tr2(qa + k * 1024, qa + k * 1024 + 256), xf[k], cq);
        const float sc = 0.27264371327785514f;
        const float slo = ((h & 1) && hb == 0) ? 0.f : sc, shi = (!(h & 1) && hb == 1) ? 0.f : sc;
#pragma unroll
        for (int e = 0; e < 16; ++e) cq[e] *= (e < 4 ? slo : e >= 12 ? shi : sc);
        lds_w128(L_XS + w * 4096 + lane * 16, pack8(cq, 0)); lds_w128(L_XS + w * 4096 + 1024 + lane * 16, pack8(cq, 1));
    }
    meet(C_Q, 4, lane);
    u32x4 q[4][2];
#pragma unroll
    for (int a = 0; a < 4; ++a) { const unsigned qa = L_XS + ((w + 1 + a) & 3) * 4096 + lane * 16; q[a][0] = lds_r128(qa); q[a][1] = lds_r128(qa + 1024); }
    arrive(C_Q, lane);
    STAMP(5);
    const int o0 = (7 * h) >> 1;
    await(C_GO, 1);
    {
        const int g = lane & 3, gj = g == 0 ? o0 >> 3 : g == 1 ? (o0 + 3) >> 3 : g == 2 ? 7 + ((28 * h) >> 6) : 7 + ((28 * h + 27) >> 6);
        poll_flags(p.ws, WS_FLAG + blk_of(4 * w + ((lane >> 2) & 3), gj) * 128 + 4);
    }
    STAMP(6);
    const unsigned ko0 = o0 * 512 + lane * 16;
    const unsigned vo00 = (WS_VO - WS_KO) + hb * 7168 + (28 * h + ((r & 0x13) | ((r & 4) << 1) | ((r & 8) >> 1))) * 16;
    f32x16 oa0 = {}, oa1 = {}, oa2 = {}, oa3 = {};
    {
        const char* base = p.ws + WS_KO + (unsigned)(16 * w * 28672);
        unsigned ko = ko0, vo0 = vo00, vo1 = vo00 + 14336, cnt = 3;
        asm volatile(
            ".irp n,200,201,202,203,204,205,206,207,216,217,218,219,220,221,222,223,232,233,234,235,236,237,238,239,248,249,250,251,252,253,254,255\n\t"
            "v_mov_b32 v[\\n], 0x3f803f80\n\t.endr\n\t"
            LOADKV(192) LOADKV(208) LOADKV(224)
            "s_waitcnt vmcnt(8)\n\t"
            QK0(128, 192, "%[q00]") QK1(128, 192, "%[q01]") QK0(144, 192, "%[q10]") QK1(144, 192, "%[q11]")
            "s_nop 7\n\ts_nop 1\n\t"
            EXP4(128, 0, 1, 2, 3) EXP4(128, 4, 5, 6, 7) EXP4(128, 8, 9, 10, 11) EXP4(128, 12, 13, 14, 15)
            "s_branch 2f\n"
            "1:\n\t"
            STEP0(192, 240)
            "2:\n\t"
            LOADKV(240) STEP12(192) "s_waitcnt vmcnt(8)\n\t" STEP3(192, 208)
            TILE(208, 192, 224, LOADKV(192), 8)
            TILE(224, 208, 240, LOADKV(208), 8)
            TILE(240, 224, 192, LOADKV(224), 8)
            "s_sub_u32 %[cnt], %[cnt], 1\n\ts_cmp_lg_u32 %[cnt], 0\n\ts_cbranch_scc1 1b\n\t"
            TILE(192, 240, 208, LOADKV(240), 8)
            TILE(208, 192, 224, "", 4)
            TILE(224, 208, 240, "", 0)
            STEP0(240, 224) STEP12(240)
            EXP4(176, 0, 1, 2, 3) PV0("%[o2]", 240, 160) EXP4(176, 4, 5, 6, 7) PV1("%[o2]", 240, 160) EXP4(176, 8, 9, 10, 11) PV0("%[o3]", 240, 176) EXP4(176, 12, 13, 14, 15)
            "s_nop 1\n\t" PV1("%[o3]", 240, 176)
            "s_nop 11"
            : [o0] "+v"(oa0), [o1] "+v"(oa1), [o2] "+v"(oa2), [o3] "+v"(oa3), [ko] "+v"(ko), [vo0] "+v"(vo0), [vo1] "+v"(vo1), [cnt] "+s"(cnt)
            : [q00] "v"(q[0][0]), [q01] "v"(q[0][1]), [q10] "v"(q[1][0]), [q11] "v"(q[1][1]), [q20] "v"(q[2][0]), [q21] "v"(q[2][1]), [q30] "v"(q[3][0]), [q31] "v"(q[3][1]),
              [idx] "v"(0xddddddddu), [base] "s"(base), [st] "s"(28672u), [vm] "s"(0x0fffffff0fffffffull)
            : "memory", "scc",
              "v128", "v129", "v130", "v131", "v132", "v133", "v134", "v135", "v136", "v137", "v138", "v139", "v140", "v141", "v142", "v143",
              "v144", "v145", "v146", "v147", "v148", "v149", "v150", "v151", "v152", "v153", "v154", "v155", "v156", "v157", "v158", "v159",
              "v160", "v161", "v162", "v163", "v164", "v165", "v166", "v167", "v168", "v169", "v170", "v171", "v172", "v173", "v174", "v175",
              "v176", "v177", "v178", "v179", "v180", "v181", "v182", "v183", "v184", "v185", "v186", "v187", "v188", "v189", "v190", "v191",
              "v192", "v193", "v194", "v195", "v196", "v197", "v198", "v199", "v200", "v201", "v202", "v203", "v204", "v205", "v206", "v207",
              "v208", "v209", "v210", "v211", "v212", "v213", "v214", "v215", "v216", "v217", "v218", "v219", "v220", "v221", "v222", "v223",
              "v224", "v225", "v226", "v227", "v228", "v229", "v230", "v231", "v232", "v233", "v234", "v235", "v236", "v237", "v238", "v239",
              "v240", "v241", "v242", "v243", "v244", "v245", "v246", "v247", "v248", "v249", "v250", "v251", "v252", "v253", "v254", "v255");
    }
    STAMP(7);
    await(C_Q, 8);
#pragma unroll
    for (int a = 0; a < 3; ++a) {
        const f32x16& o = a == 0 ? oa0 : a == 1 ? oa1 : oa2;
        const unsigned ra = L_RED + (((w + 1 + a) & 3) * 3 + a) * 4096 + lane * 16;
#pragma unroll
        for (int g = 0; g < 4; ++g) lds_w128(ra + g * 1024, (u32x4){__float_as_uint(o[4 * g]), __float_as_uint(o[4 * g + 1]), __float_as_uint(o[4 * g + 2]), __float_as_uint(o[4 * g + 3])});
    }
    asm volatile("s_waitcnt lgkmcnt(0)\n\ts_barrier" ::: "memory");
#pragma unroll
    for (int a = 0; a < 3; ++a)
#pragma unroll
        for (int g = 0; g < 4; ++g) { const u32x4 t = lds_r128(L_RED + (w * 3 + a) * 4096 + g * 1024 + lane * 16);
#pragma unroll
            for (int e = 0; e < 4; ++e) oa3[4 * g + e] += __uint_as_float(t[e]); }
    float l = __uint_as_float(__builtin_amdgcn_permlane32_swap(__float_as_uint(oa3[12]), __float_as_uint(oa3[12]), false, false)[1]);
    {
        float am = 0.f;
#pragma unroll
        for (int e = 0; e < 16; ++e) am = fmaxf(am, fabsf(oa3[e]));
        if (__builtin_expect(!__all(l >= 0x1p-10f && l <= 0x1p60f && am <= 0x1p120f), 0)) {
            poll_flags(p.ws, WS_FLAG + blk_of(lane >> 2, (lane & 3) == 0 ? o0 >> 3 : (lane & 3) == 1 ? (o0 + 3) >> 3 : (lane & 3) == 2 ? 7 + ((28 * h) >> 6) : 7 + ((28 * h + 27) >> 6)) * 128 + 4);
            const char* base = p.ws + WS_KO;
            float m = -INFINITY;
#pragma unroll 1
            for (int pass = 0; pass < 2; ++pass) {
                oa3 = (f32x16){};
#pragma unroll 1
                for (int t = 0; t < 64; ++t) {
                    u32x4 k0, k1, v0 = {0x3f803f80u, 0x3f803f80u, 0x3f803f80u, 0x3f803f80u}, v1 = v0;
                    asm volatile("global_load_dwordx4 %0, %4, %7 sc1\n\tglobal_load_dwordx4 %1, %4, %7 offset:1024 sc1\n\ts_mov_b64 exec, %8\n\t"
                                 "global_load_dwordx4 %2, %5, %7 sc1\n\tglobal_load_dwordx4 %3, %6, %7 sc1\n\ts_mov_b64 exec, -1\n\ts_waitcnt vmcnt(0)"
                                 : "=&v"(k0), "=&v"(k1), "+v"(v0), "+v"(v1) : "v"(ko0 + t * 28672), "v"(vo00 + t * 28672), "v"(vo00 + 14336 + t * 28672), "s"(base), "s"(0x0fffffff0fffffffull) : "memory");
                    f32x16 s = mfma32(k1, q[3][1], mfma32(k0, q[3][0], (f32x16){}));
                    if (pass == 0) {
#pragma unroll
                        for (int e = 0; e < 16; ++e) m = fmaxf(m, s[e]);
                    } else {
                        typedef __bf16 bf16x16 __attribute__((ext_vector_type(16)));
                        typedef float f32x8 __attribute__((ext_vector_type(8)));
                        f32x8 p0, p1;
#pragma unroll
                        for (int e = 0; e < 8; ++e) { p0[e] = __builtin_amdgcn_exp2f(s[e] - m); p1[e] = __builtin_amdgcn_exp2f(s[8 + e] - m); }
                        oa3 = __builtin_amdgcn_smfmac_f32_32x32x32_bf16(asbf(v0), __builtin_bit_cast(bf16x16, p0), oa3, (int)0xddddddddu, 0, 0);
                        oa3 = __builtin_amdgcn_smfmac_f32_32x32x32_bf16(asbf(v1), __builtin_bit_cast(bf16x16, p1), oa3, (int)0xddddddddu, 0, 0);
                    }
                }
                if (pass == 0) { auto sw = __builtin_amdgcn_permlane32_swap(__float_as_uint(m), __float_as_uint(m), false, false); m = fmaxf(__uint_as_float(sw[0]), __uint_as_float(sw[1])); }
            }
            l = __uint_as_float(__builtin_amdgcn_permlane32_swap(__float_as_uint(oa3[12]), __float_as_uint(oa3[12]), false, false)[1]);
        }
    }
    {
        const float rl = 1.0f / l, rh = hb ? 0.f : rl;
#pragma unroll
        for (int e = 0; e < 16; ++e) oa3[e] *= (e >= 12 ? rh : rl);
        u32x4 a0 = pack8(oa3, 0), a1 = pack8(oa3, 1);
#pragma unroll
        for (int e = 0; e < 4; ++e) { a0[e] = min(a0[e], 0xfffffffeu); a1[e] = min(a1[e], 0xfffffffeu); }
        const char* base = p.ws + WS_OF + (unsigned)(kt * 32768 + h * 2048);
        st16_sc1(base, lane * 16, a0); st16_sc1(base, lane * 16 + 1024, a1);
    }
    STAMP(8);
    stage_o(p, 4 * i + (j >> 2), wid, lane);
    STAMP(9);
}

DEV void gen_unit(f32x16& acc, float dt, float are, float aim, float bre, float bim, float cre, float cim, unsigned gb, unsigned gr) {
    const float lg = (dt * are) * 1.4426950408889634f, rv = (dt * aim) * 0.15915494309189535f;
    const f32x2 z1 = cexp2(lg, -rv), z8 = cexp2(8.f * lg, 8.f * rv), z256 = cexp2(256.f * lg, -256.f * rv);
    f32x2 f0 = cmul((f32x2){cre, -cim}, (f32x2){dt * bre, -dt * bim}), f1, f2, f3, f4, f5, f6, f7, g1, g2, g3, g4, g5, g6, g7, g8;
    cmul2(f1, f0, z1, g1, f0, z256); cmul2(f2, f1, z1, g2, g1, z256); cmul2(f3, f2, z1, g3, g2, z256);
    wrow4<0, 1, 2, 3>(gb, pkE(f0), pkE(f1), pkE(f2), pkE(f3));
    cmul2(f4, f3, z1, g4, g3, z256); cmul2(f5, f4, z1, g5, g4, z256); cmul2(f6, f5, z1, g6, g5, z256); cmul2(f7, f6, z1, g7, g6, z256);
    wrow4<4, 5, 6, 7>(gb, pkE(f4), pkE(f5), pkE(f6), pkE(f7));
    wrow4<16, 17, 18, 19>(gb, pkE(g4), pkE(g5), pkE(g6), pkE(g7));
    f32x2 h2, h4, h6, e2, e3, e4, i4, j4;
    cmul4(h2, h4, h6, g8, f2, f4, f6, g7, z256);
    wrow4<8, 9, 10, 11>(gb, pkE(g1), pkE(h2), pkE(h4), pkE(h6));
    cmul2(i4, h4, z256, e2, z8, z8); cmul2(j4, i4, z256, e3, e2, z8); e4 = cmul(e2, e2);
    wrow4<12, 13, 14, 15>(gb, pkE(g2), pkE(i4), pkE(g3), pkE(j4));
    { const unsigned t = pkE(g8); asm volatile("s_mov_b32 m0, %0\n\ts_nop 0\n\tds_write_addtid_b32 %1 offset:%2" :: "s"(gb), "v"(t), "i"(20 * 272) : "memory"); }
    u32x4 ff[8];
#pragma unroll
    for (int k = 0; k < 8; ++k) ff[k] = lds_r128(gr + k * 32);
    fence_c();
    f32x2 a0 = {1.f, 0.f}, a1 = z8, a2 = e2, a3 = e3;
    wrow4<0, 1, 2, 3>(gb, 0x00003f80u, pkE(a1), pkE(a2), pkE(a3));
#define ECH(R) { f32x2 n0, n1, n2, n3; cmul4(n0, n1, n2, n3, a0, a1, a2, a3, e4); a0 = n0; a1 = n1; a2 = n2; a3 = n3; wrow4<R, R + 1, R + 2, R + 3>(gb, pkE(a0), pkE(a1), pkE(a2), pkE(a3)); }
    ECH(4) ECH(8) ECH(12) ECH(16) ECH(20) ECH(24) ECH(28)
#undef ECH
#pragma unroll
    for (int k = 0; k < 8; ++k) acc = mfma32(ff[k], lds_r128(gr + k * 32), acc);
    fence_c();
}

DEV void chain_a(const Params& p, int b, int i, int j, int wid, int lane PROF_ARG) {
    const int aw = wid - 4, hb = lane >> 5, r = lane & 31;
    const int g = b & 7, m = b >> 3;
    const bool ssm = g < 7;
    gu32* hbw = (gu32*)(p.ws + WS_HB);
    const int ch = 64 * g + 2 * m + (aw >> 1), par = aw & 1;
    unsigned hb0, pa[6], ldt;
    asm volatile("global_load_dword %0, %1, %2 sc1" : "=v"(hb0) : "v"(0u), "s"(p.ws + WS_HB) : "memory");
    if (ssm) {
        dma_run<false>(p.u + 128 * g, (unsigned)((64 * m + 16 * aw + (lane >> 3)) * 896 + (((lane & 7) ^ ((lane >> 2) & 6)) << 4)), 8 * 896, L_KX + aw * 2048, 1024, 2);
        asm volatile("global_load_ushort %6, %8, %15\n\tglobal_load_dword %0, %7, %9\n\tglobal_load_dword %1, %7, %10\n\tglobal_load_dword %2, %7, %11\n\t"
                     "global_load_dword %3, %7, %12\n\tglobal_load_dword %4, %7, %13\n\tglobal_load_dword %5, %7, %14"
                     : "=&v"(pa[0]), "=&v"(pa[1]), "=&v"(pa[2]), "=&v"(pa[3]), "=&v"(pa[4]), "=&v"(pa[5]), "=&v"(ldt)
                     : "v"(ch * 512 + par * 256 + lane * 4), "v"(ch * 2), "s"(p.are), "s"(p.aim), "s"(p.bre), "s"(p.bim), "s"(p.cre), "s"(p.cim), "s"(p.logdt) : "memory");
    } else if (b == 7 && aw == 1) {
#pragma unroll
        for (int e = 0; e < 4; ++e) st16_sc1(p.ws + WS_FLAG, (e * 64 + lane) * 128, (u32x4){0u, 0u, 0u, 0u});
        asm volatile("s_waitcnt vmcnt(0)" : "+v"(hb0) :: "memory");
        if (lane == 0) __hip_atomic_store(hbw, hb0 + 1, RLX_AGENT);
    }
    {
        const char* base = p.ws + WS_OF + (unsigned)((4 * i + aw) * 32768 + j * 2048);
        st16_sc1(base, lane * 16, (u32x4){~0u, ~0u, ~0u, ~0u}); st16_sc1(base, lane * 16 + 1024, (u32x4){~0u, ~0u, ~0u, ~0u});
    }
    asm volatile("s_barrier" ::: "memory");
    STAMP(1);
    const unsigned gb = L_GEN + aw * GEN_W;
    const int tq = (lane >> 2) & 3, tp = lane & 3, G = lane >> 4;
    f32x16 acc = {};
    float dt = 0.f;
    if (ssm) {
        asm volatile("s_waitcnt vmcnt(2)" : "+v"(pa[0]), "+v"(pa[1]), "+v"(pa[2]), "+v"(pa[3]), "+v"(pa[4]), "+v"(pa[5]), "+v"(ldt) :: "memory");
        meet(C_U, 4, lane);
        const unsigned ta = L_KX + (16 * G + tq) * 128 + (((2 * aw + (tp >> 1)) ^ (2 * tq)) << 4) + (tp & 1) * 8;
        const u32x4 t0 = lds_tr2(ta, ta + 512), t1 = lds_tr2(ta + 1024, ta + 1536);
        lds_w128(gb + (lane & 15) * 144 + G * 32, t0); lds_w128(gb + (lane & 15) * 144 + G * 32 + 16, t1);
        fence_c();
        const u32x4 s0 = lds_r128(gb + (lane >> 3) * 144 + (lane & 7) * 16), s1 = lds_r128(gb + (8 + (lane >> 3)) * 144 + (lane & 7) * 16);
        const char* base = p.ws + WS_UT + (unsigned)((64 * g + 16 * aw) * UT_PITCH);
        const unsigned off = (lane >> 3) * UT_PITCH + 128 + 128 * m + (lane & 7) * 16;
        st16_sc1(base, off, s0); st16_sc1(base, off + 8 * UT_PITCH, s1);
        if (m == 0 && lane < 16) st16_sc1(base, lane * UT_PITCH + 112, (u32x4){0u, 0u, 0u, 0u});
        arrive(C_U, lane);
        STAMP(2);
        dt = bflo(cvtpk(expf(bflo(ldt)), 0.f));
        gen_unit(acc, dt, bflo(pa[0]), bflo(pa[1]), bflo(pa[2]), bflo(pa[3]), bflo(pa[4]), bflo(pa[5]), gb, gb + r * 272 + hb * 16);
    }
    STAMP(3);
    asm volatile("s_waitcnt vmcnt(0)" : "+v"(hb0) :: "memory");
    arrive(C_F0, lane);
    if (aw == 0) {
    _Pragma("unroll 1") for (unsigned s = 0; s < SPIN_CAP; ++s) { if ((unsigned)__builtin_amdgcn_readfirstlane(__hip_atomic_load(hbw, RLX_AGENT)) != (unsigned)__builtin_amdgcn_readfirstlane(hb0)) break; __builtin_amdgcn_s_sleep(1); }
        arrive(C_GO, lane);
        await(C_F0, 4);
        set_flag(p.ws, b, 0, lane);
    }
    STAMP(4);
    __builtin_amdgcn_s_setprio(2);
    if (b == 7 && aw == 1) {
        unsigned v = __hip_atomic_load(hbw, RLX_AGENT);
    _Pragma("unroll 1") for (unsigned s = 0; s < SPIN_CAP; ++s) {
            unsigned all = 1;
#pragma unroll
            for (int e = 0; e < 4; ++e) all &= __hip_atomic_load((gu32*)(p.ws + WS_FLAG + (e * 64 + lane) * 128), RLX_AGENT);
            if (__all(all == 1u)) break;
            ++v;
            if (lane == 0) __hip_atomic_store(hbw, v, RLX_AGENT);
            __builtin_amdgcn_s_sleep(4);
        }
    }
    if (ssm) {
        gen_unit(acc, dt, bfhi(pa[0]), bfhi(pa[1]), bfhi(pa[2]), bfhi(pa[3]), bfhi(pa[4]), bfhi(pa[5]), gb, gb + r * 272 + hb * 16);
        STAMP(5);
#pragma unroll
        for (int e = 0; e < 3; ++e) lds_w128(gb + e * 1024 + lane * 16, (u32x4){__float_as_uint(acc[4 * e]), __float_as_uint(acc[4 * e + 1]), __float_as_uint(acc[4 * e + 2]), __float_as_uint(acc[4 * e + 3])});
        meet(C_P0 + (aw >> 1), 2, lane);
#pragma unroll
        for (int e = 0; e < 3; ++e) { const u32x4 t = lds_r128(L_GEN + (aw ^ 1) * GEN_W + e * 1024 + lane * 16);
#pragma unroll
            for (int k = 0; k < 4; ++k) acc[4 * e + k] += __uint_as_float(t[k]); }
        const unsigned tb = gb + 4096 + 16;
        *(LAS u32x2*)(tb + (8 * r + 4 * hb) * 2) = (u32x2){cvtpk(acc[0], acc[1]), cvtpk(acc[2], acc[3])};
        *(LAS unsigned*)(tb + (hb ? 384 + 2 * r : 256 + 4 * r) * 2) = cvtpk(acc[4], acc[5]);
        *(LAS unsigned*)(tb + (hb ? 448 + 2 * r : 258 + 4 * r) * 2) = cvtpk(acc[6], acc[7]);
#pragma unroll
        for (int e = 0; e < 4; ++e) *(LAS unsigned short*)(tb + (512 + 128 * hb + 32 * e + r) * 2) = (unsigned short)cvtpk(acc[8 + e], 0.f);
        *(LAS unsigned*)(tb - 4) = 0u;
        await(C_U, 8);
        {
            const unsigned kx = L_KX + aw * KX_W;
            if (lane < 28) lds_w128(kx + lane * 16, (u32x4){0u, 0u, 0u, 0u});
            const unsigned LL = 0x01000100u, HH = 0x03020302u;
            {
                const unsigned ba = tb + (hb ? 256 + 4 * r : 8 * r) * 2;
                const unsigned x0 = *(LAS unsigned*)(ba - 4), x1 = *(LAS unsigned*)(ba), x2 = *(LAS unsigned*)(ba + 4), x3 = *(LAS unsigned*)(ba + 8), x4 = *(LAS unsigned*)(ba + (hb ? 4 : 12));
                const unsigned s0 = par ? 0x05040706u : 0x03020504u;
                const unsigned d0 = __builtin_amdgcn_perm(x4, x3, hb ? (par ? 0x07060100u : 0x05040706u) : s0);
                const unsigned d1 = __builtin_amdgcn_perm(x3, x2, hb ? LL : s0);
                const unsigned d2 = __builtin_amdgcn_perm(x2, x1, hb ? (par ? 0x03020504u : 0x01000302u) : s0);
                const unsigned d3 = __builtin_amdgcn_perm(x1, x0, (hb && (par || r)) ? 0x05040504u : s0);
                lds_w128(kx + (28 + lane) * 16, (u32x4){d0, d1, d2, d3});
            }
            {
                const unsigned ba = tb + (384 + 2 * lane) * 2, x = *(LAS unsigned*)(ba), y = *(LAS unsigned*)(ba + 4);
                lds_w128(kx + (92 + lane) * 16, (u32x4){__builtin_amdgcn_perm(y, x, par ? 0x05040504u : 0x03020504u), __builtin_amdgcn_perm(y, x, HH),
                                                       __builtin_amdgcn_perm(y, x, par ? 0x01000302u : LL), __builtin_amdgcn_perm(y, x, LL)});
            }
#pragma unroll
            for (int e = 0; e < 2; ++e) {
                const unsigned ba = tb + (512 + 64 * e + lane) * 2, x = *(LAS unsigned short*)(ba), y = *(LAS unsigned short*)(ba + 2);
                const unsigned v00 = __builtin_amdgcn_perm(y, x, LL);
                lds_w128(kx + (156 + 64 * e + lane) * 16, (u32x4){__builtin_amdgcn_perm(y, x, 0x05040504u),
                         __builtin_amdgcn_perm(y, x, (par == (lane & 1)) ? (par ? 0x05040504u : LL) : 0x01000504u), v00, v00});
            }
        }
        meet(C_EX, 4, lane);
        STAMP(6);
        await(C_GO, 1);
        poll_flags(p.ws, WS_FLAG + (g + 8 * r) * 128);
        {
            const unsigned uc = L_GEN + aw * 8320;
            if (lane < 6) lds_w128(uc + (lane >= 3 ? 4160 - 48 : 0) + lane * 16, (u32x4){0u, 0u, 0u, 0u});
            const char* base = p.ws + WS_UT + (unsigned)(ch * UT_PITCH + 128 - 12 + 8 * par);
            dma_run<true>(base, lane * 16, 1024, uc + 48, 1024, 4);
            dma_run<true>(base, lane * 16 + 4, 1024, uc + 4160 + 48, 1024, 4);
            WAIT_VM(0);
            meet(C_P0 + (aw >> 1), 4, lane);
        }
        STAMP(7);
        f32x4 y[8];
#pragma unroll
        for (int n = 0; n < 8; ++n) y[n] = (f32x4){};
        {
            const int mm = lane & 15;
            unsigned ua = L_GEN + (aw >> 1) * 16640 + (mm & 3) * 4160 + (G + (mm >> 2)) * 16 + par * 256;
            unsigned ka = L_KX + ((aw & 2) + (mm >> 3)) * KX_W + (28 + 4 * (mm & 7) - G + 3) * 16 - par * 256;
#pragma unroll 1
            for (int cc = 0; cc < 4; ++cc) {
                u32x4 af[8], bf[8];
#pragma unroll
                for (int a = 0; a < 8; ++a) { af[a] = lds_r128(ua + a * 512); bf[a] = lds_r128(ka + a * 512); }
#pragma unroll
                for (int a = 0; a < 8; ++a)
#pragma unroll
                    for (int n = 0; n + a < 8; ++n) y[a + n] = mfma16(af[a], bf[n], y[a + n]);
                ua += 64; ka -= 64;
            }
        }
        meet(C_P0 + (aw >> 1), 6, lane);
        {
            const unsigned yb = L_GEN + (aw >> 1) * 16640;
#pragma unroll
            for (int n = 0; n < 8; ++n) lds_w128(yb + par * 8192 + n * 1024 + (((lane & 7) * 4 + G) * 2 + ((lane >> 3) & 1)) * 16,
                                                 (u32x4){__float_as_uint(y[n][0]), __float_as_uint(y[n][1]), __float_as_uint(y[n][2]), __float_as_uint(y[n][3])});
            meet(C_P0 + (aw >> 1), 8, lane);
            const char* base = p.ws + WS_YT + (unsigned)(ch * 4096 + par * 2048);
#pragma unroll
            for (int e = 0; e < 2; ++e) {
                const unsigned a = yb + par * 4096 + e * 2048 + lane * 32;
                const u32x4 e0 = lds_r128(a), o0 = lds_r128(a + 16), e1 = lds_r128(a + 8192), o1 = lds_r128(a + 8192 + 16);
                u32x4 v;
#pragma unroll
                for (int k = 0; k < 4; ++k) v[k] = cvtpk(__uint_as_float(e0[k]) + __uint_as_float(e1[k]), __uint_as_float(o0[k]) + __uint_as_float(o1[k]));
                st16_sc1(base, e * 1024 + lane * 16, v);
            }
            WAIT_VM(0);
            if (arrive(C_Y, lane) == 3) set_flag(p.ws, b, 2, lane);
        }
        STAMP(8);
    }
    const int cs = j & 3, tt = 4 * i + (j >> 2);
    u32x4 wf[32];
    await(C_KV, 4);
    {
        const int tps = ((tp & 1) << 1) | (tp >> 1);
        const unsigned wa = L_W + (8 * hb + tq) * 256 + ((aw ^ tq) << 6) + (G & 1) * 32 + tps * 8;
        const unsigned wa_last = wa + (hb ? 0 : 1024);
        const unsigned so = (56 * aw + G) * 896 + ((((lane & 15) ^ (G << 2)) & (cs == 3 ? 7 : 15)) << 4);
#pragma unroll
        for (int hf = 0; hf < 2; ++hf) {
            dma_run<false>(p.wo + 256 * cs + hf * 224 * 896, so, 4 * 896, L_W + aw * 14336, 1024, 14);
            WAIT_VM(0);
            meet(C_WO, 4 + 8 * hf, lane);
#pragma unroll
            for (int hh = 0; hh < 8; ++hh) {
                wf[16 * hf + 2 * hh] = lds_tr2(wa + hh * 7168, wa + hh * 7168 + 1024);
                wf[16 * hf + 2 * hh + 1] = lds_tr2(wa + hh * 7168 + 4096, wa_last + hh * 7168 + 4096);
            }
            asm volatile("s_waitcnt lgkmcnt(0)" ::: "memory");
#pragma unroll
            for (int k = 0; k < 16; ++k) asm volatile("" : "+v"(wf[16 * hf + k]));
            meet(C_WO, 8 + 8 * hf, lane);
        }
    }
    STAMP(9);
    await(C_GO, 1);
    if (ssm) {
        const unsigned ro = (64 * m + 16 * aw + (lane >> 3));
        const u32x4 u0 = *(const GAS u32x4*)(p.u + ro * 896 + 128 * g + (lane & 7) * 16), u1 = *(const GAS u32x4*)(p.u + (ro + 8) * 896 + 128 * g + (lane & 7) * 16);
        const u32x4 dk = *(const GAS u32x4*)(p.dskip + 128 * g + (lane & 7) * 16);
        poll_flags(p.ws, WS_FLAG + (g + 8 * r) * 128 + 8);
        dma_run<true>(p.ws + WS_YT + (unsigned)(64 * g * 4096 + 128 * m), (unsigned)((16 * aw + (lane >> 3)) * 4096 + (((lane & 7) ^ ((lane >> 2) & 6)) << 4)), 8 * 4096, L_KX + aw * 2048, 1024, 2);
        WAIT_VM(0);
        meet(C_YT, 4, lane);
        const unsigned ta = L_KX + (16 * G + tq) * 128 + (((2 * aw + (tp >> 1)) ^ (2 * tq)) << 4) + (tp & 1) * 8;
        const u32x4 t0 = lds_tr2(ta, ta + 512), t1 = lds_tr2(ta + 1024, ta + 1536);
        lds_w128(gb + (lane & 15) * 144 + G * 32, t0); lds_w128(gb + (lane & 15) * 144 + G * 32 + 16, t1);
        fence_c();
#pragma unroll
        for (int e = 0; e < 2; ++e) {
            const u32x4 s = lds_r128(gb + (8 * e + (lane >> 3)) * 144 + (lane & 7) * 16), uu = e ? u1 : u0;
            u32x4 v;
#pragma unroll
            for (int k = 0; k < 4; ++k) { const unsigned sk = cvtpk(bflo(uu[k]) * bflo(dk[k]), bfhi(uu[k]) * bfhi(dk[k])); v[k] = cvtpk(bflo(s[k]) + bflo(sk), bfhi(s[k]) + bfhi(sk)); }
            st16(p.out + 128 * g, (ro + 8 * e) * 1792 + (lane & 7) * 16, v);
        }
    }
    poll_flags(p.ws, WS_FLAG + blk_of(i, lane & 15) * 128);
    STAMP(10);
    asm volatile("s_waitcnt lgkmcnt(0)\n\ts_barrier" ::: "memory");
    stage_o(p, tt, wid, lane);
    STAMP(11);
    f32x16 oc = {};
#pragma unroll
    for (int k = 0; k < 32; ++k) oc = mfma32(wf[k], lds_r128(L_W + k * 1024 + lane * 16), oc);
    lds_w128(gb + r * 80 + hb * 16, pack8(oc, 0)); lds_w128(gb + r * 80 + 32 + hb * 16, pack8(oc, 1));
    fence_c();
    if (cs < 3 || aw < 2) {
#pragma unroll
        for (int e = 0; e < 2; ++e)
            st16(p.out + 896 + 256 * cs + 64 * aw, (32 * tt + 16 * e + (lane >> 2)) * 1792 + (lane & 3) * 16, lds_r128(gb + (16 * e + (lane >> 2)) * 80 + (lane & 3) * 16));
    }
    STAMP(12);
}

__global__ void __launch_bounds__(512, 2) fused_s4_mha(Params p) {
    extern __shared__ __attribute__((aligned(256))) char smem[];
    const int lane = threadIdx.x & 63, wid = __builtin_amdgcn_readfirstlane(threadIdx.x >> 6), b = blockIdx.x;
    STAMP(0);
    {
        unsigned long long pc;
        asm volatile("s_getpc_b64 %0\n\ts_mov_b32 m0, %2\n\ts_nop 0\n\tglobal_load_lds_dword %1, %0" : "=&s"(pc) : "v"(min((threadIdx.x >> 1), 240u) * 128u), "s"(L_END) : "memory");
    }
    asm volatile("" :: "s"(p.u), "s"(p.logdt), "s"(p.x), "s"(p.ws));
    if (threadIdx.x < 32) *(LAS unsigned*)(L_SYNC + 4 * threadIdx.x) = 0u;
    const int i = ((b >> 2) & 1) * 8 + (b >> 5), j = (b & 3) * 4 + (~(b >> 3) & 3);
    if (wid < 4) chain_b(p, b, i, j, wid, lane PROF_PASS); else chain_a(p, b, i, j, wid, lane PROF_PASS);
}

extern "C" void kernel_launch(void* const* d_in, const int* in_sizes, int n_in,
                              void* d_out, int out_size, void* d_ws, size_t ws_size,
                              hipStream_t stream) {
    Params p;
    p.u = (const char*)d_in[0]; p.are = (const char*)d_in[1]; p.aim = (const char*)d_in[2]; p.bre = (const char*)d_in[3]; p.bim = (const char*)d_in[4];
    p.cre = (const char*)d_in[5]; p.cim = (const char*)d_in[6]; p.logdt = (const char*)d_in[7]; p.dskip = (const char*)d_in[8];
    p.x = (const char*)d_in[9]; p.wqkv = (const char*)d_in[10]; p.wo = (const char*)d_in[11];
    p.out = (char*)d_out; p.ws = (char*)d_ws;
    (void)hipFuncSetAttribute((const void*)fused_s4_mha, hipFuncAttributeMaxDynamicSharedMemorySize, L_END + 256);
    fused_s4_mha<<<256, 512, L_END + 256, stream>>>(p);
}
```

```cpp
#define DIAG 101
#define PROFILE 0xcb
#include <hip/hip_runtime.h>
#include <hip/hip_bf16.h>
#include <stdint.h>
#define PF 1
#pragma clang diagnostic ignored "-Wint-to-pointer-cast"

typedef float f32x2 __attribute__((ext_vector_type(2)));
typedef float f32x4 __attribute__((ext_vector_type(4)));
typedef float f32x16 __attribute__((ext_vector_type(16)));
typedef __bf16 bf16x8 __attribute__((ext_vector_type(8)));
typedef short s16x4 __attribute__((ext_vector_type(4)));
typedef unsigned u32x2 __attribute__((ext_vector_type(2)));
typedef unsigned u32x4 __attribute__((ext_vector_type(4)));
#define LAS __attribute__((address_space(3)))
#define GAS __attribute__((address_space(1)))
typedef GAS unsigned gu32;
#define RLX_AGENT __ATOMIC_RELAXED, __HIP_MEMORY_SCOPE_AGENT
#define DEV __device__ __forceinline__

constexpr unsigned WS_FLAG = 0;
constexpr unsigned WS_HB = 32768;
constexpr unsigned WS_KO = 65536;
constexpr unsigned WS_VO = WS_KO + 64 * 28672;
constexpr unsigned WS_OF = WS_VO + 64 * 28672;
constexpr unsigned WS_UT = WS_OF + 64 * 32768;
constexpr unsigned UT_PITCH = 4224;
constexpr unsigned WS_YT = WS_UT + 448 * UT_PITCH;
constexpr unsigned WS_PROF = WS_YT + 448 * 4096;

constexpr unsigned L_SYNC = 64;
constexpr unsigned L_GEN = 256;
constexpr unsigned GEN_W = 8704;
constexpr unsigned L_KX = L_GEN + 4 * GEN_W;
constexpr unsigned KX_W = 4576;
constexpr unsigned L_RED = L_KX + 4 * KX_W + 128;
constexpr unsigned L_XS = L_RED;
constexpr unsigned L_WQ = L_RED + 16384;
constexpr unsigned L_W = L_RED + 49152;
constexpr unsigned L_END = L_W + 57344;
static_assert(L_END + 256 <= 163840 && (L_RED % 256) == 0 && (L_KX % 256) == 0, "lds map");
enum { C_W = 0, C_WQ, C_Q, C_KV, C_GO, C_U, C_F0, C_P0, C_P1, C_UC, C_EX, C_Y, C_WO, C_YT };

constexpr unsigned SPIN_CAP = 1u << 18;

struct Params {
    const char *u, *are, *aim, *bre, *bim, *cre, *cim, *logdt, *dskip, *x, *wqkv, *wo;
    char *out, *ws;
};

#define STAMP(k) do { if (!((PROFILE >> (k)) & 1)) break; unsigned long long t_, c_; asm volatile("s_memrealtime %1\n\ts_memtime %2\n\ts_waitcnt lgkmcnt(0)" : "+v"(prof), "=s"(t_), "=s"(c_) :: "memory"); \
    asm volatile("v_writelane_b32 %0, %1, %3\n\tv_writelane_b32 %0, %2, %4" : "+v"(prof) : "s"((unsigned)t_), "s"((unsigned)c_), "i"(k), "i"(32 + (k))); } while (0)
#define PROF_ARG , unsigned& prof
#define PROF_PASS , prof

typedef __bf16 bf16x2 __attribute__((ext_vector_type(2)));
DEV unsigned cvtpk(float lo, float hi) { return __builtin_bit_cast(unsigned, __builtin_convertvector((f32x2){lo, hi}, bf16x2)); }
DEV float bflo(unsigned v) { return __uint_as_float(v << 16); }
DEV float bfhi(unsigned v) { return __uint_as_float(v & 0xffff0000u); }
DEV bf16x8 asbf(u32x4 v) { return __builtin_bit_cast(bf16x8, v); }
DEV f32x16 mfma32(u32x4 a, u32x4 b, f32x16 c) { return __builtin_amdgcn_mfma_f32_32x32x16_bf16(asbf(a), asbf(b), c, 0, 0, 0); }
DEV f32x4 mfma16(u32x4 a, u32x4 b, f32x4 c) { return __builtin_amdgcn_mfma_f32_16x16x32_bf16(asbf(a), asbf(b), c, 0, 0, 0); }
DEV u32x4 lds_r128(unsigned a) { return *(const LAS u32x4*)a; }
DEV void lds_w128(unsigned a, u32x4 v) { *(LAS u32x4*)a = v; }
DEV u32x2 lds_tr(unsigned a) { return __builtin_bit_cast(u32x2, __builtin_amdgcn_ds_read_tr16_b64_v4i16((LAS s16x4*)a)); }
DEV u32x4 lds_tr2(unsigned a0, unsigned a1) { u32x2 x = lds_tr(a0), y = lds_tr(a1); return (u32x4){x[0], x[1], y[0], y[1]}; }
DEV void fence_c() { asm volatile("" ::: "memory"); }
#define WAIT_VM(n) asm volatile("s_waitcnt vmcnt(" #n ")" ::: "memory")
DEV void st16_sc1(const char* base, unsigned off, u32x4 v) { asm volatile("global_store_dwordx4 %0, %1, %2 sc1\n\ts_nop 1" :: "v"(off), "v"(v), "s"(base) : "memory"); }
DEV void st16(const char* base, unsigned off, u32x4 v) { asm volatile("global_store_dwordx4 %0, %1, %2\n\ts_nop 1" :: "v"(off), "v"(v), "s"(base) : "memory"); }
template <bool SC1> DEV void dma_run(const char* base, unsigned off, unsigned gstep, unsigned lds, unsigned lstep, unsigned n) {
    if (SC1)
        asm volatile("s_mov_b32 m0, %2\n"
                     "1:\n\ts_sub_u32 %1, %1, 1\n\tglobal_load_lds_dwordx4 %0, %3 sc1\n\tv_add_u32 %0, %4, %0\n\ts_add_u32 m0, m0, %5\n\ts_cmp_lg_u32 %1, 0\n\ts_cbranch_scc1 1b"
                     : "+v"(off), "+s"(n) : "s"(lds), "s"(base), "s"(gstep), "s"(lstep) : "memory", "scc");
    else
        asm volatile("s_mov_b32 m0, %2\n"
                     "1:\n\ts_sub_u32 %1, %1, 1\n\tglobal_load_lds_dwordx4 %0, %3\n\tv_add_u32 %0, %4, %0\n\ts_add_u32 m0, m0, %5\n\ts_cmp_lg_u32 %1, 0\n\ts_cbranch_scc1 1b"
                     : "+v"(off), "+s"(n) : "s"(lds), "s"(base), "s"(gstep), "s"(lstep) : "memory", "scc");
}
DEV unsigned arrive(int c, int lane) {
    unsigned old = 0;
    fence_c();
    if (lane == 0) old = __hip_atomic_fetch_add((LAS unsigned*)(L_SYNC + 4 * c), 1u, __ATOMIC_RELAXED, __HIP_MEMORY_SCOPE_WORKGROUP);
    fence_c();
    return __builtin_amdgcn_readfirstlane(old);
}
DEV void await(int c, unsigned n) {
#pragma unroll 1
    for (unsigned s = 0; s < (SPIN_CAP << 4); ++s) {
        fence_c();
        unsigned v = __hip_atomic_load((LAS unsigned*)(L_SYNC + 4 * c), __ATOMIC_RELAXED, __HIP_MEMORY_SCOPE_WORKGROUP);
        if ((unsigned)__builtin_amdgcn_readfirstlane(v) >= n) break;
        __builtin_amdgcn_s_sleep(1);
    }
    fence_c();
}
DEV void meet(int c, unsigned n, int lane) { if (arrive(c, lane) + 1 < n) await(c, n); }
DEV void poll_flags(const char* ws, unsigned off) {
_Pragma("unroll 1") for (unsigned s = 0; s < SPIN_CAP; ++s) {
        unsigned v = __hip_atomic_load((gu32*)(ws + off), RLX_AGENT);
        if (__all(v == 1u)) break;
        __builtin_amdgcn_s_sleep(1);
    }
    fence_c();
}
DEV void set_flag(char* ws, int b, int w, int lane) { if (lane == 0) __hip_atomic_store((gu32*)(ws + WS_FLAG + b * 128 + w * 4), 1u, RLX_AGENT); }
DEV int blk_of(int i, int j) { return ((i >> 3) * 4 + (j >> 2)) + 8 * ((i & 7) * 4 + (~j & 3)); }

#define CM1(t, x, y) "v_pk_mul_f32 " t ", " x ", " y " op_sel:[0,0] op_sel_hi:[1,0]\n\t"
#define CM2(t, x, y) "v_pk_fma_f32 " t ", " x ", " y ", " t " op_sel:[1,1,0] op_sel_hi:[0,1,1] neg_lo:[1,0,0]\n\t"
DEV f32x2 cmul(f32x2 x, f32x2 y) { f32x2 r; asm(CM1("%0", "%1", "%2") CM2("%0", "%1", "%2") : "=&v"(r) : "v"(x), "v"(y)); return r; }
DEV void cmul2(f32x2& r0, f32x2 x0, f32x2 y0, f32x2& r1, f32x2 x1, f32x2 y1) {
    asm(CM1("%0", "%2", "%3") CM1("%1", "%4", "%5") CM2("%0", "%2", "%3") CM2("%1", "%4", "%5") : "=&v"(r0), "=&v"(r1) : "v"(x0), "v"(y0), "v"(x1), "v"(y1)); }
DEV void cmul4(f32x2& r0, f32x2& r1, f32x2& r2, f32x2& r3, f32x2 x0, f32x2 x1, f32x2 x2, f32x2 x3, f32x2 y) {
    asm(CM1("%0", "%4", "%8") CM1("%1", "%5", "%8") CM1("%2", "%6", "%8") CM1("%3", "%7", "%8")
        CM2("%0", "%4", "%8") CM2("%1", "%5", "%8") CM2("%2", "%6", "%8") CM2("%3", "%7", "%8")
        : "=&v"(r0), "=&v"(r1), "=&v"(r2), "=&v"(r3) : "v"(x0), "v"(x1), "v"(x2), "v"(x3), "v"(y)); }
DEV f32x2 cexp2(float lg, float rev) {
    float m = __builtin_amdgcn_exp2f(lg), f = __builtin_amdgcn_fractf(rev);
    return (f32x2){m * __builtin_amdgcn_cosf(f), m * __builtin_amdgcn_sinf(f)}; }
template <int R0, int R1, int R2, int R3> DEV void wrow4(unsigned base, unsigned a, unsigned b, unsigned c, unsigned d) {
    asm volatile("s_mov_b32 m0, %0\n\ts_nop 0\n\tds_write_addtid_b32 %1 offset:%5\n\tds_write_addtid_b32 %2 offset:%6\n\tds_write_addtid_b32 %3 offset:%7\n\tds_write_addtid_b32 %4 offset:%8"
                 :: "s"(base), "v"(a), "v"(b), "v"(c), "v"(d), "i"(R0 * 272), "i"(R1 * 272), "i"(R2 * 272), "i"(R3 * 272) : "memory"); }
DEV unsigned pkE(f32x2 z) { return cvtpk(z[0], z[1]); }

#define EXP4(b, o0, o1, o2, o3) "v_exp_f32 v[" #b "+" #o0 "], v[" #b "+" #o0 "]\n\tv_exp_f32 v[" #b "+" #o1 "], v[" #b "+" #o1 "]\n\tv_exp_f32 v[" #b "+" #o2 "], v[" #b "+" #o2 "]\n\tv_exp_f32 v[" #b "+" #o3 "], v[" #b "+" #o3 "]\n\t"
#define EXP1(b, o) "v_exp_f32 v[" #b "+" #o "], v[" #b "+" #o "]\n\t"
#define QK0(sn, kb, q) "v_mfma_f32_32x32x16_bf16 v[" #sn ":" #sn "+15], v[" #kb ":" #kb "+3], " q ", 0\n\t"
#define QK1(sn, kb, q) "v_mfma_f32_32x32x16_bf16 v[" #sn ":" #sn "+15], v[" #kb "+4:" #kb "+7], " q ", v[" #sn ":" #sn "+15]\n\t"
#define PV0(o, vb, sp) "v_smfmac_f32_32x32x32_bf16 " o ", v[" #vb "+8:" #vb "+11], v[" #sp ":" #sp "+7], %[idx]\n\t"
#define PV1(o, vb, sp) "v_smfmac_f32_32x32x32_bf16 " o ", v[" #vb "+12:" #vb "+15], v[" #sp "+8:" #sp "+15], %[idx]\n\t"
#define STEP(sx, sn, kb, qa, qb, o, vb, sp) QK0(sn, kb, qa) EXP4(sx, 0, 1, 2, 3) QK1(sn, kb, qb) EXP4(sx, 4, 5, 6, 7) PV0(o, vb, sp) EXP4(sx, 8, 9, 10, 11) PV1(o, vb, sp) EXP4(sx, 12, 13, 14, 15)
#define LOADK(b) "global_load_dwordx4 v[" #b ":" #b "+3], %[ko], %[base] sc1\n\tglobal_load_dwordx4 v[" #b "+4:" #b "+7], %[ko], %[base] offset:1024 sc1\n\t"
#define LOADV(b) "s_mov_b64 exec, %[vm]\n\tglobal_load_dwordx4 v[" #b "+8:" #b "+11], %[vo0], %[base] sc1\n\tglobal_load_dwordx4 v[" #b "+12:" #b "+15], %[vo1], %[base] sc1\n\ts_mov_b64 exec, -1\n\t"
#define ADV "v_add_u32 %[ko], %[st], %[ko]\n\tv_add_u32 %[vo0], %[st], %[vo0]\n\tv_add_u32 %[vo1], %[st], %[vo1]\n\t"
#define LOADKV(b) LOADK(b) LOADV(b) ADV
#define STEP0(kb, vprev) STEP(128, 144, kb, "%[q10]", "%[q11]", "%[o3]", vprev, 176)
#define STEP12(kb) STEP(144, 160, kb, "%[q20]", "%[q21]", "%[o0]", kb, 128) STEP(160, 176, kb, "%[q30]", "%[q31]", "%[o1]", kb, 144)
#define STEP3(kb, knext) STEP(176, 128, knext, "%[q00]", "%[q01]", "%[o2]", kb, 160)
#define TILE(kb, vprev, knext, LD, W) STEP0(kb, vprev) LD STEP12(kb) "s_waitcnt vmcnt(" #W ")\n\t" STEP3(kb, knext)

DEV u32x4 pack8(const f32x16& c, int s) { return (u32x4){cvtpk(c[8 * s], c[8 * s + 1]), cvtpk(c[8 * s + 2], c[8 * s + 3]), cvtpk(c[8 * s + 4], c[8 * s + 5]), cvtpk(c[8 * s + 6], c[8 * s + 7])}; }

template <bool VT> DEV void kv_front(const Params& p, u32x4 (&xf)[28], int i, int j, int wid, int lane PROF_ARG) {
    const int w = wid, h = j, r = lane & 31, hb = lane >> 5;
    if (j < 14) dma_run<false>(p.wqkv + 896 + 128 * j, (unsigned)((w * 112 + (lane >> 3)) * 2688 + (((lane & 7) ^ ((lane >> 2) & 4)) << 4)), 8 * 2688, L_W + w * 14336, 1024, 14);
    {
        const char* xb = p.x + (size_t)(128 * i + 32 * w) * 896;
        const unsigned o0 = (lane >> 3) * 896 + (lane & 7) * 16;
#pragma unroll
        for (int c = 0; c < 7; ++c)
#pragma unroll
            for (int e = 0; e < 4; ++e) asm volatile("global_load_dwordx4 %0, %1, %2 offset:%3" : "=v"(xf[4 * c + e]) : "v"(o0 + e * 7168), "s"(xb), "i"(c * 128) : "memory");
    }
    dma_run<false>(p.wqkv + 56 * h - 8 * (h & 1), (unsigned)((w * 112 + (lane >> 2)) * 2688 + (lane & 3) * 16), 16 * 2688, L_WQ + w * 7168, 1024, 7);
    const unsigned xs_w = L_XS + w * 4096 + (lane >> 3) * 128 + (((lane & 7) ^ (lane >> 4)) << 4);
    unsigned xs_r[4];
#pragma unroll
    for (int e = 0; e < 4; ++e) xs_r[e] = L_XS + w * 4096 + r * 128 + (((2 * e + hb) ^ ((r >> 1) & 7)) << 4);
    const int tq = (lane >> 2) & 3, tp = lane & 3, tps = ((tp & 1) << 1) | (tp >> 1), tch = (lane >> 4) & 1;
    const unsigned wa0 = L_W + (8 * hb + tq) * 128 + ((tch * 32 + (VT ? tp : tps) * 8) ^ ((tq >> 1) << 6)), wa1 = wa0 ^ 64;
    STAMP(1);
    WAIT_VM(35);
    meet(C_W, 4, lane);
    STAMP(2);
    f32x16 c0 = {}, c1 = {};
#pragma unroll
    for (int c = 0; c < 7; ++c) {
        asm volatile("s_waitcnt vmcnt(%4)" : "+v"(xf[4 * c]), "+v"(xf[4 * c + 1]), "+v"(xf[4 * c + 2]), "+v"(xf[4 * c + 3]) : "i"(31 - 4 * c) : "memory");
#pragma unroll
        for (int e = 0; e < 4; ++e) lds_w128((xs_w ^ ((e & 1) << 6)) + e * 1024, xf[4 * c + e]);
        fence_c();
#pragma unroll
        for (int e = 0; e < 4; ++e) xf[4 * c + e] = lds_r128(xs_r[e]);
        fence_c();
#pragma unroll
        for (int e = 0; e < 4; ++e) {
            const int k = 4 * c + e;
            const u32x4 w0 = lds_tr2(wa0 + k * 2048, wa0 + k * 2048 + 512), w1 = lds_tr2(wa1 + k * 2048, wa1 + k * 2048 + 512);
            if (VT) { c0 = mfma32(xf[k], w0, c0); c1 = mfma32(xf[k], w1, c1); }
            else    { c0 = mfma32(w0, xf[k], c0); c1 = mfma32(w1, xf[k], c1); }
        }
    }
    const int kt = 4 * i + w;
    if (VT) {
        if (j < 14) {
            const char* base = p.ws + WS_VO + (unsigned)(kt * 28672 + (j - 7) * 1024);
            const unsigned off = hb * 7168 + r * 16;
            st16_sc1(base, off, pack8(c0, 0)); st16_sc1(base, off + 512, pack8(c1, 0));
            st16_sc1(base, off + 14336, pack8(c0, 1)); st16_sc1(base, off + 14336 + 512, pack8(c1, 1));
        }
    } else {
        const char* base = p.ws + WS_KO + (unsigned)(kt * 28672 + j * 4096);
        const unsigned off = hb * 512 + ((r & 0x13) | ((r & 4) << 1) | ((r & 8) >> 1)) * 16;
        st16_sc1(base, off, pack8(c0, 0)); st16_sc1(base, off + 1024, pack8(c0, 1));
        st16_sc1(base, off + 2048, pack8(c1, 0)); st16_sc1(base, off + 3072, pack8(c1, 1));
    }
}

DEV void stage_o(const Params& p, int tt, int wid, int lane) {
    const char* base = p.ws + WS_OF + (unsigned)(tt * 32768 + wid * 4096);
    u32x4 v0, v1, v2, v3;
_Pragma("unroll 1") for (unsigned s = 0; s < SPIN_CAP; ++s) {
        asm volatile("global_load_dwordx4 %0, %4, %5 sc1\n\tglobal_load_dwordx4 %1, %4, %5 offset:1024 sc1\n\tglobal_load_dwordx4 %2, %4, %5 offset:2048 sc1\n\t"
                     "global_load_dwordx4 %3, %4, %5 offset:3072 sc1\n\ts_waitcnt vmcnt(0)"
                     : "=&v"(v0), "=&v"(v1), "=&v"(v2), "=&v"(v3) : "v"(lane * 16), "s"(base) : "memory");
        unsigned m = 0;
#pragma unroll
        for (int e = 0; e < 4; ++e) m = max(max(m, max(v0[e], v1[e])), max(v2[e], v3[e]));
        if (__all(m != 0xffffffffu)) break;
    }
    const unsigned a = L_W + wid * 4096 + lane * 16;
    lds_w128(a, v0); lds_w128(a + 1024, v1); lds_w128(a + 2048, v2); lds_w128(a + 3072, v3);
    asm volatile("s_waitcnt lgkmcnt(0)\n\ts_barrier" ::: "memory");
}

DEV void chain_b(const Params& p, int b, int i, int j, int wid, int lane PROF_ARG) {
    const int w = wid, h = j, r = lane & 31, hb = lane >> 5;
    const int tq = (lane >> 2) & 3, tp = lane & 3, tps = ((tp & 1) << 1) | (tp >> 1), tch = (lane >> 4) & 1, kt = 4 * i + w;
    u32x4 xf[28];
    asm volatile("s_waitcnt lgkmcnt(0)\n\ts_barrier" ::: "memory");
    if (j >= 7) kv_front<true>(p, xf, i, j, wid, lane PROF_PASS); else kv_front<false>(p, xf, i, j, wid, lane PROF_PASS);
    STAMP(3);
    WAIT_VM(0);
    if (arrive(C_KV, lane) == 3) { await(C_GO, 1); set_flag(p.ws, b, 1, lane); } else await(C_KV, 4);
    STAMP(4);
    {
        const unsigned qa = L_WQ + (8 * hb + tq) * 64 + tch * 32 + tps * 8;
        f32x16 cq = {};
#pragma unroll
        for (int k = 0; k < 28; ++k) cq = mfma32(lds_tr2(qa + k * 1024, qa + k * 1024 + 256), xf[k], cq);
        const float sc = 0.27264371327785514f;
        const float slo = ((h & 1) && hb == 0) ? 0.f : sc, shi = (!(h & 1) && hb == 1) ? 0.f : sc;
#pragma unroll
        for (int e = 0; e < 16; ++e) cq[e] *= (e < 4 ? slo : e >= 12 ? shi : sc);
        lds_w128(L_XS + w * 4096 + lane * 16, pack8(cq, 0)); lds_w128(L_XS + w * 4096 + 1024 + lane * 16, pack8(cq, 1));
    }
    meet(C_Q, 4, lane);
    u32x4 q[4][2];
#pragma unroll
    for (int a = 0; a < 4; ++a) { const unsigned qa = L_XS + ((w + 1 + a) & 3) * 4096 + lane * 16; q[a][0] = lds_r128(qa); q[a][1] = lds_r128(qa + 1024); }
    arrive(C_Q, lane);
    STAMP(5);
    const int o0 = (7 * h) >> 1;
    await(C_GO, 1);
    {
        const int g = lane & 3, gj = g == 0 ? o0 >> 3 : g == 1 ? (o0 + 3) >> 3 : g == 2 ? 7 + ((28 * h) >> 6) : 7 + ((28 * h + 27) >> 6);
        poll_flags(p.ws, WS_FLAG + blk_of(4 * w + ((lane >> 2) & 3), gj) * 128 + 4);
    }
    STAMP(6);
    const unsigned ko0 = o0 * 512 + lane * 16;
    const unsigned vo00 = (WS_VO - WS_KO) + hb * 7168 + (28 * h + ((r & 0x13) | ((r & 4) << 1) | ((r & 8) >> 1))) * 16;
    f32x16 oa0 = {}, oa1 = {}, oa2 = {}, oa3 = {};
    {
        const char* base = p.ws + WS_KO + (unsigned)(16 * w * 28672);
        unsigned ko = ko0, vo0 = vo00, vo1 = vo00 + 14336, cnt = 3;
        asm volatile(
            ".irp n,200,201,202,203,204,205,206,207,216,217,218,219,220,221,222,223,232,233,234,235,236,237,238,239,248,249,250,251,252,253,254,255\n\t"
            "v_mov_b32 v[\\n], 0x3f803f80\n\t.endr\n\t"
            LOADKV(192) LOADKV(208) LOADKV(224)
            "s_waitcnt vmcnt(8)\n\t"
            QK0(128, 192, "%[q00]") QK1(128, 192, "%[q01]") QK0(144, 192, "%[q10]") QK1(144, 192, "%[q11]")
            "s_nop 7\n\ts_nop 1\n\t"
            EXP4(128, 0, 1, 2, 3) EXP4(128, 4, 5, 6, 7) EXP4(128, 8, 9, 10, 11) EXP4(128, 12, 13, 14, 15)
            "s_branch 2f\n"
            "1:\n\t"
            STEP0(192, 240)
            "2:\n\t"
            LOADKV(240) STEP12(192) "s_waitcnt vmcnt(8)\n\t" STEP3(192, 208)
            TILE(208, 192, 224, LOADKV(192), 8)
            TILE(224, 208, 240, LOADKV(208), 8)
            TILE(240, 224, 192, LOADKV(224), 8)
            "s_sub_u32 %[cnt], %[cnt], 1\n\ts_cmp_lg_u32 %[cnt], 0\n\ts_cbranch_scc1 1b\n\t"
            TILE(192, 240, 208, LOADKV(240), 8)
            TILE(208, 192, 224, "", 4)
            TILE(224, 208, 240, "", 0)
            STEP0(240, 224) STEP12(240)
            EXP4(176, 0, 1, 2, 3) PV0("%[o2]", 240, 160) EXP4(176, 4, 5, 6, 7) PV1("%[o2]", 240, 160) EXP4(176, 8, 9, 10, 11) PV0("%[o3]", 240, 176) EXP4(176, 12, 13, 14, 15)
            "s_nop 1\n\t" PV1("%[o3]", 240, 176)
            "s_nop 11"
            : [o0] "+v"(oa0), [o1] "+v"(oa1), [o2] "+v"(oa2), [o3] "+v"(oa3), [ko] "+v"(ko), [vo0] "+v"(vo0), [vo1] "+v"(vo1), [cnt] "+s"(cnt)
            : [q00] "v"(q[0][0]), [q01] "v"(q[0][1]), [q10] "v"(q[1][0]), [q11] "v"(q[1][1]), [q20] "v"(q[2][0]), [q21] "v"(q[2][1]), [q30] "v"(q[3][0]), [q31] "v"(q[3][1]),
              [idx] "v"(0xddddddddu), [base] "s"(base), [st] "s"(28672u), [vm] "s"(0x0fffffff0fffffffull)
            : "memory", "scc",
              "v128", "v129", "v130", "v131", "v132", "v133", "v134", "v135", "v136", "v137", "v138", "v139", "v140", "v141", "v142", "v143",
              "v144", "v145", "v146", "v147", "v148", "v149", "v150", "v151", "v152", "v153", "v154", "v155", "v156", "v157", "v158", "v159",
              "v160", "v161", "v162", "v163", "v164", "v165", "v166", "v167", "v168", "v169", "v170", "v171", "v172", "v173", "v174", "v175",
              "v176", "v177", "v178", "v179", "v180", "v181", "v182", "v183", "v184", "v185", "v186", "v187", "v188", "v189", "v190", "v191",
              "v192", "v193", "v194", "v195", "v196", "v197", "v198", "v199", "v200", "v201", "v202", "v203", "v204", "v205", "v206", "v207",
              "v208", "v209", "v210", "v211", "v212", "v213", "v214", "v215", "v216", "v217", "v218", "v219", "v220", "v221", "v222", "v223",
              "v224", "v225", "v226", "v227", "v228", "v229", "v230", "v231", "v232", "v233", "v234", "v235", "v236", "v237", "v238", "v239",
              "v240", "v241", "v242", "v243", "v244", "v245", "v246", "v247", "v248", "v249", "v250", "v251", "v252", "v253", "v254", "v255");
    }
    STAMP(7);
    await(C_Q, 8);
#pragma unroll
    for (int a = 0; a < 3; ++a) {
        const f32x16& o = a == 0 ? oa0 : a == 1 ? oa1 : oa2;
        const unsigned ra = L_RED + (((w + 1 + a) & 3) * 3 + a) * 4096 + lane * 16;
#pragma unroll
        for (int g = 0; g < 4; ++g) lds_w128(ra + g * 1024, (u32x4){__float_as_uint(o[4 * g]), __float_as_uint(o[4 * g + 1]), __float_as_uint(o[4 * g + 2]), __float_as_uint(o[4 * g + 3])});
    }
    asm volatile("s_waitcnt lgkmcnt(0)\n\ts_barrier" ::: "memory");
#pragma unroll
    for (int a = 0; a < 3; ++a)
#pragma unroll
        for (int g = 0; g < 4; ++g) { const u32x4 t = lds_r128(L_RED + (w * 3 + a) * 4096 + g * 1024 + lane * 16);
#pragma unroll
            for (int e = 0; e < 4; ++e) oa3[4 * g + e] += __uint_as_float(t[e]); }
    float l = __uint_as_float(__builtin_amdgcn_permlane32_swap(__float_as_uint(oa3[12]), __float_as_uint(oa3[12]), false, false)[1]);
    {
        float am = 0.f;
#pragma unroll
        for (int e = 0; e < 16; ++e) am = fmaxf(am, fabsf(oa3[e]));
        if (__builtin_expect(!__all(l >= 0x1p-10f && l <= 0x1p60f && am <= 0x1p120f), 0)) {
            poll_flags(p.ws, WS_FLAG + blk_of(lane >> 2, (lane & 3) == 0 ? o0 >> 3 : (lane & 3) == 1 ? (o0 + 3) >> 3 : (lane & 3) == 2 ? 7 + ((28 * h) >> 6) : 7 + ((28 * h + 27) >> 6)) * 128 + 4);
            const char* base = p.ws + WS_KO;
            float m = -INFINITY;
#pragma unroll 1
            for (int pass = 0; pass < 2; ++pass) {
                oa3 = (f32x16){};
#pragma unroll 1
                for (int t = 0; t < 64; ++t) {
                    u32x4 k0, k1, v0 = {0x3f803f80u, 0x3f803f80u, 0x3f803f80u, 0x3f803f80u}, v1 = v0;
                    asm volatile("global_load_dwordx4 %0, %4, %7 sc1\n\tglobal_load_dwordx4 %1, %4, %7 offset:1024 sc1\n\ts_mov_b64 exec, %8\n\t"
                                 "global_load_dwordx4 %2, %5, %7 sc1\n\tglobal_load_dwordx4 %3, %6, %7 sc1\n\ts_mov_b64 exec, -1\n\ts_waitcnt vmcnt(0)"
                                 : "=&v"(k0), "=&v"(k1), "+v"(v0), "+v"(v1) : "v"(ko0 + t * 28672), "v"(vo00 + t * 28672), "v"(vo00 + 14336 + t * 28672), "s"(base), "s"(0x0fffffff0fffffffull) : "memory");
                    f32x16 s = mfma32(k1, q[3][1], mfma32(k0, q[3][0], (f32x16){}));
                    if (pass == 0) {
#pragma unroll
                        for (int e = 0; e < 16; ++e) m = fmaxf(m, s[e]);
                    } else {
                        typedef __bf16 bf16x16 __attribute__((ext_vector_type(16)));
                        typedef float f32x8 __attribute__((ext_vector_type(8)));
                        f32x8 p0, p1;
#pragma unroll
                        for (int e = 0; e < 8; ++e) { p0[e] = __builtin_amdgcn_exp2f(s[e] - m); p1[e] = __builtin_amdgcn_exp2f(s[8 + e] - m); }
                        oa3 = __builtin_amdgcn_smfmac_f32_32x32x32_bf16(asbf(v0), __builtin_bit_cast(bf16x16, p0), oa3, (int)0xddddddddu, 0, 0);
                        oa3 = __builtin_amdgcn_smfmac_f32_32x32x32_bf16(asbf(v1), __builtin_bit_cast(bf16x16, p1), oa3, (int)0xddddddddu, 0, 0);
                    }
                }
                if (pass == 0) { auto sw = __builtin_amdgcn_permlane32_swap(__float_as_uint(m), __float_as_uint(m), false, false); m = fmaxf(__uint_as_float(sw[0]), __uint_as_float(sw[1])); }
            }
            l = __uint_as_float(__builtin_amdgcn_permlane32_swap(__float_as_uint(oa3[12]), __float_as_uint(oa3[12]), false, false)[1]);
        }
    }
    {
        const float rl = 1.0f / l, rh = hb ? 0.f : rl;
#pragma unroll
        for (int e = 0; e < 16; ++e) oa3[e] *= (e >= 12 ? rh : rl);
        u32x4 a0 = pack8(oa3, 0), a1 = pack8(oa3, 1);
#pragma unroll
        for (int e = 0; e < 4; ++e) { a0[e] = min(a0[e], 0xfffffffeu); a1[e] = min(a1[e], 0xfffffffeu); }
        const char* base = p.ws + WS_OF + (unsigned)(kt * 32768 + h * 2048);
        st16_sc1(base, lane * 16, a0); st16_sc1(base, lane * 16 + 1024, a1);
    }
    STAMP(8);
    stage_o(p, 4 * i + (j >> 2), wid, lane);
    STAMP(9);
}

DEV void gen_unit(f32x16& acc, float dt, float are, float aim, float bre, float bim, float cre, float cim, unsigned gb, unsigned gr) {
    const float lg = (dt * are) * 1.4426950408889634f, rv = (dt * aim) * 0.15915494309189535f;
    const f32x2 z1 = cexp2(lg, -rv), z8 = cexp2(8.f * lg, 8.f * rv), z256 = cexp2(256.f * lg, -256.f * rv);
    f32x2 f0 = cmul((f32x2){cre, -cim}, (f32x2){dt * bre, -dt * bim}), f1, f2, f3, f4, f5, f6, f7, g1, g2, g3, g4, g5, g6, g7, g8;
    cmul2(f1, f0, z1, g1, f0, z256); cmul2(f2, f1, z1, g2, g1, z256); cmul2(f3, f2, z1, g3, g2, z256);
    wrow4<0, 1, 2, 3>(gb, pkE(f0), pkE(f1), pkE(f2), pkE(f3));
    cmul2(f4, f3, z1, g4, g3, z256); cmul2(f5, f4, z1, g5, g4, z256); cmul2(f6, f5, z1, g6, g5, z256); cmul2(f7, f6, z1, g7, g6, z256);
    wrow4<4, 5, 6, 7>(gb, pkE(f4), pkE(f5), pkE(f6), pkE(f7));
    wrow4<16, 17, 18, 19>(gb, pkE(g4), pkE(g5), pkE(g6), pkE(g7));
    f32x2 h2, h4, h6, e2, e3, e4, i4, j4;
    cmul4(h2, h4, h6, g8, f2, f4, f6, g7, z256);
    wrow4<8, 9, 10, 11>(gb, pkE(g1), pkE(h2), pkE(h4), pkE(h6));
    cmul2(i4, h4, z256, e2, z8, z8); cmul2(j4, i4, z256, e3, e2, z8); e4 = cmul(e2, e2);
    wrow4<12, 13, 14, 15>(gb, pkE(g2), pkE(i4), pkE(g3), pkE(j4));
    { const unsigned t = pkE(g8); asm volatile("s_mov_b32 m0, %0\n\ts_nop 0\n\tds_write_addtid_b32 %1 offset:%2" :: "s"(gb), "v"(t), "i"(20 * 272) : "memory"); }
    u32x4 ff[8];
#pragma unroll
    for (int k = 0; k < 8; ++k) ff[k] = lds_r128(gr + k * 32);
    fence_c();
    f32x2 a0 = {1.f, 0.f}, a1 = z8, a2 = e2, a3 = e3;
    wrow4<0, 1, 2, 3>(gb, 0x00003f80u, pkE(a1), pkE(a2), pkE(a3));
#define ECH(R) { f32x2 n0, n1, n2, n3; cmul4(n0, n1, n2, n3, a0, a1, a2, a3, e4); a0 = n0; a1 = n1; a2 = n2; a3 = n3; wrow4<R, R + 1, R + 2, R + 3>(gb, pkE(a0), pkE(a1), pkE(a2), pkE(a3)); }
    ECH(4) ECH(8) ECH(12) ECH(16) ECH(20) ECH(24) ECH(28)
#undef ECH
#pragma unroll
    for (int k = 0; k < 8; ++k) acc = mfma32(ff[k], lds_r128(gr + k * 32), acc);
    fence_c();
}

DEV void chain_a(const Params& p, int b, int i, int j, int wid, int lane PROF_ARG) {
    const int aw = wid - 4, hb = lane >> 5, r = lane & 31;
    const int g = b & 7, m = b >> 3;
    const bool ssm = g < 7;
    gu32* hbw = (gu32*)(p.ws + WS_HB);
    const int ch = 64 * g + 2 * m + (aw >> 1), par = aw & 1;
    unsigned hb0, pa[6], ldt;
    asm volatile("global_load_dword %0, %1, %2 sc1" : "=v"(hb0) : "v"(0u), "s"(p.ws + WS_HB) : "memory");
    if (ssm) {
        dma_run<false>(p.u + 128 * g, (unsigned)((64 * m + 16 * aw + (lane >> 3)) * 896 + (((lane & 7) ^ ((lane >> 2) & 6)) << 4)), 8 * 896, L_KX + aw * 2048, 1024, 2);
        asm volatile("global_load_ushort %6, %8, %15\n\tglobal_load_dword %0, %7, %9\n\tglobal_load_dword %1, %7, %10\n\tglobal_load_dword %2, %7, %11\n\t"
                     "global_load_dword %3, %7, %12\n\tglobal_load_dword %4, %7, %13\n\tglobal_load_dword %5, %7, %14"
                     : "=&v"(pa[0]), "=&v"(pa[1]), "=&v"(pa[2]), "=&v"(pa[3]), "=&v"(pa[4]), "=&v"(pa[5]), "=&v"(ldt)
                     : "v"(ch * 512 + par * 256 + lane * 4), "v"(ch * 2), "s"(p.are), "s"(p.aim), "s"(p.bre), "s"(p.bim), "s"(p.cre), "s"(p.cim), "s"(p.logdt) : "memory");
    } else if (b == 7 && aw == 1) {
#pragma unroll
        for (int e = 0; e < 4; ++e) st16_sc1(p.ws + WS_FLAG, (e * 64 + lane) * 128, (u32x4){0u, 0u, 0u, 0u});
        asm volatile("s_waitcnt vmcnt(0)" : "+v"(hb0) :: "memory");
        if (lane == 0) __hip_atomic_store(hbw, hb0 + 1, RLX_AGENT);
    }
    {
        const char* base = p.ws + WS_OF + (unsigned)((4 * i + aw) * 32768 + j * 2048);
        st16_sc1(base, lane * 16, (u32x4){~0u, ~0u, ~0u, ~0u}); st16_sc1(base, lane * 16 + 1024, (u32x4){~0u, ~0u, ~0u, ~0u});
    }
    asm volatile("s_barrier" ::: "memory");
    STAMP(1);
    const unsigned gb = L_GEN + aw * GEN_W;
    const int tq = (lane >> 2) & 3, tp = lane & 3, G = lane >> 4;
    f32x16 acc = {};
    float dt = 0.f;
    if (ssm) {
        asm volatile("s_waitcnt vmcnt(2)" : "+v"(pa[0]), "+v"(pa[1]), "+v"(pa[2]), "+v"(pa[3]), "+v"(pa[4]), "+v"(pa[5]), "+v"(ldt) :: "memory");
        meet(C_U, 4, lane);
        const unsigned ta = L_KX + (16 * G + tq) * 128 + (((2 * aw + (tp >> 1)) ^ (2 * tq)) << 4) + (tp & 1) * 8;
        const u32x4 t0 = lds_tr2(ta, ta + 512), t1 = lds_tr2(ta + 1024, ta + 1536);
        lds_w128(gb + (lane & 15) * 144 + G * 32, t0); lds_w128(gb + (lane & 15) * 144 + G * 32 + 16, t1);
        fence_c();
        const u32x4 s0 = lds_r128(gb + (lane >> 3) * 144 + (lane & 7) * 16), s1 = lds_r128(gb + (8 + (lane >> 3)) * 144 + (lane & 7) * 16);
        const char* base = p.ws + WS_UT + (unsigned)((64 * g + 16 * aw) * UT_PITCH);
        const unsigned off = (lane >> 3) * UT_PITCH + 128 + 128 * m + (lane & 7) * 16;
        st16_sc1(base, off, s0); st16_sc1(base, off + 8 * UT_PITCH, s1);
        if (m == 0 && lane < 16) st16_sc1(base, lane * UT_PITCH + 112, (u32x4){0u, 0u, 0u, 0u});
        arrive(C_U, lane);
        STAMP(2);
        dt = bflo(cvtpk(expf(bflo(ldt)), 0.f));
        gen_unit(acc, dt, bflo(pa[0]), bflo(pa[1]), bflo(pa[2]), bflo(pa[3]), bflo(pa[4]), bflo(pa[5]), gb, gb + r * 272 + hb * 16);
    }
    STAMP(3);
    asm volatile("s_waitcnt vmcnt(0)" : "+v"(hb0) :: "memory");
    arrive(C_F0, lane);
    if (aw == 0) {
    _Pragma("unroll 1") for (unsigned s = 0; s < SPIN_CAP; ++s) { if ((unsigned)__builtin_amdgcn_readfirstlane(__hip_atomic_load(hbw, RLX_AGENT)) != (unsigned)__builtin_amdgcn_readfirstlane(hb0)) break; __builtin_amdgcn_s_sleep(1); }
        arrive(C_GO, lane);
        await(C_F0, 4);
        set_flag(p.ws, b, 0, lane);
    }
    STAMP(4);
    __builtin_amdgcn_s_setprio(2);
    if (b == 7 && aw == 1) {
        unsigned v = __hip_atomic_load(hbw, RLX_AGENT);
    _Pragma("unroll 1") for (unsigned s = 0; s < SPIN_CAP; ++s) {
            unsigned all = 1;
#pragma unroll
            for (int e = 0; e < 4; ++e) all &= __hip_atomic_load((gu32*)(p.ws + WS_FLAG + (e * 64 + lane) * 128), RLX_AGENT);
            if (__all(all == 1u)) break;
            ++v;
            if (lane == 0) __hip_atomic_store(hbw, v, RLX_AGENT);
            __builtin_amdgcn_s_sleep(4);
        }
    }
    if (ssm) {
        gen_unit(acc, dt, bfhi(pa[0]), bfhi(pa[1]), bfhi(pa[2]), bfhi(pa[3]), bfhi(pa[4]), bfhi(pa[5]), gb, gb + r * 272 + hb * 16);
        STAMP(5);
#pragma unroll
        for (int e = 0; e < 3; ++e) lds_w128(gb + e * 1024 + lane * 16, (u32x4){__float_as_uint(acc[4 * e]), __float_as_uint(acc[4 * e + 1]), __float_as_uint(acc[4 * e + 2]), __float_as_uint(acc[4 * e + 3])});
        meet(C_P0 + (aw >> 1), 2, lane);
#pragma unroll
        for (int e = 0; e < 3; ++e) { const u32x4 t = lds_r128(L_GEN + (aw ^ 1) * GEN_W + e * 1024 + lane * 16);
#pragma unroll
            for (int k = 0; k < 4; ++k) acc[4 * e + k] += __uint_as_float(t[k]); }
        const unsigned tb = gb + 4096 + 16;
        *(LAS u32x2*)(tb + (8 * r + 4 * hb) * 2) = (u32x2){cvtpk(acc[0], acc[1]), cvtpk(acc[2], acc[3])};
        *(LAS unsigned*)(tb + (hb ? 384 + 2 * r : 256 + 4 * r) * 2) = cvtpk(acc[4], acc[5]);
        *(LAS unsigned*)(tb + (hb ? 448 + 2 * r : 258 + 4 * r) * 2) = cvtpk(acc[6], acc[7]);
#pragma unroll
        for (int e = 0; e < 4; ++e) *(LAS unsigned short*)(tb + (512 + 128 * hb + 32 * e + r) * 2) = (unsigned short)cvtpk(acc[8 + e], 0.f);
        *(LAS unsigned*)(tb - 4) = 0u;
        await(C_U, 8);
        {
            const unsigned kx = L_KX + aw * KX_W;
            if (lane < 28) lds_w128(kx + lane * 16, (u32x4){0u, 0u, 0u, 0u});
            const unsigned LL = 0x01000100u, HH = 0x03020302u;
            {
                const unsigned ba = tb + (hb ? 256 + 4 * r : 8 * r) * 2;
                const unsigned x0 = *(LAS unsigned*)(ba - 4), x1 = *(LAS unsigned*)(ba), x2 = *(LAS unsigned*)(ba + 4), x3 = *(LAS unsigned*)(ba + 8), x4 = *(LAS unsigned*)(ba + (hb ? 4 : 12));
                const unsigned s0 = par ? 0x05040706u : 0x03020504u;
                const unsigned d0 = __builtin_amdgcn_perm(x4, x3, hb ? (par ? 0x07060100u : 0x05040706u) : s0);
                const unsigned d1 = __builtin_amdgcn_perm(x3, x2, hb ? LL : s0);
                const unsigned d2 = __builtin_amdgcn_perm(x2, x1, hb ? (par ? 0x03020504u : 0x01000302u) : s0);
                const unsigned d3 = __builtin_amdgcn_perm(x1, x0, (hb && (par || r)) ? 0x05040504u : s0);
                lds_w128(kx + (28 + lane) * 16, (u32x4){d0, d1, d2, d3});
            }
            {
                const unsigned ba = tb + (384 + 2 * lane) * 2, x = *(LAS unsigned*)(ba), y = *(LAS unsigned*)(ba + 4);
                lds_w128(kx + (92 + lane) * 16, (u32x4){__builtin_amdgcn_perm(y, x, par ? 0x05040504u : 0x03020504u), __builtin_amdgcn_perm(y, x, HH),
                                                       __builtin_amdgcn_perm(y, x, par ? 0x01000302u : LL), __builtin_amdgcn_perm(y, x, LL)});
            }
#pragma unroll
            for (int e = 0; e < 2; ++e) {
                const unsigned ba = tb + (512 + 64 * e + lane) * 2, x = *(LAS unsigned short*)(ba), y = *(LAS unsigned short*)(ba + 2);
                const unsigned v00 = __builtin_amdgcn_perm(y, x, LL);
                lds_w128(kx + (156 + 64 * e + lane) * 16, (u32x4){__builtin_amdgcn_perm(y, x, 0x05040504u),
                         __builtin_amdgcn_perm(y, x, (par == (lane & 1)) ? (par ? 0x05040504u : LL) : 0x01000504u), v00, v00});
            }
        }
        meet(C_EX, 4, lane);
        STAMP(6);
        await(C_GO, 1);
        poll_flags(p.ws, WS_FLAG + (g + 8 * r) * 128);
        {
            const unsigned uc = L_GEN + aw * 8320;
            if (lane < 6) lds_w128(uc + (lane >= 3 ? 4160 - 48 : 0) + lane * 16, (u32x4){0u, 0u, 0u, 0u});
            const char* base = p.ws + WS_UT + (unsigned)(ch * UT_PITCH + 128 - 12 + 8 * par);
            dma_run<true>(base, lane * 16, 1024, uc + 48, 1024, 4);
            dma_run<true>(base, lane * 16 + 4, 1024, uc + 4160 + 48, 1024, 4);
            WAIT_VM(0);
            meet(C_P0 + (aw >> 1), 4, lane);
        }
        STAMP(7);
        f32x4 y[8];
#pragma unroll
        for (int n = 0; n < 8; ++n) y[n] = (f32x4){};
        {
            const int mm = lane & 15;
            unsigned ua = L_GEN + (aw >> 1) * 16640 + (mm & 3) * 4160 + (G + (mm >> 2)) * 16 + par * 256;
            unsigned ka = L_KX + ((aw & 2) + (mm >> 3)) * KX_W + (28 + 4 * (mm & 7) - G + 3) * 16 - par * 256;
#pragma unroll 1
            for (int cc = 0; cc < 4; ++cc) {
                u32x4 af[8], bf[8];
#pragma unroll
                for (int a = 0; a < 8; ++a) { af[a] = lds_r128(ua + a * 512); bf[a] = lds_r128(ka + a * 512); }
#pragma unroll
                for (int a = 0; a < 8; ++a)
#pragma unroll
                    for (int n = 0; n + a < 8; ++n) y[a + n] = mfma16(af[a], bf[n], y[a + n]);
                ua += 64; ka -= 64;
            }
        }
        meet(C_P0 + (aw >> 1), 6, lane);
        {
            const unsigned yb = L_GEN + (aw >> 1) * 16640;
#pragma unroll
            for (int n = 0; n < 8; ++n) lds_w128(yb + par * 8192 + n * 1024 + (((lane & 7) * 4 + G) * 2 + ((lane >> 3) & 1)) * 16,
                                                 (u32x4){__float_as_uint(y[n][0]), __float_as_uint(y[n][1]), __float_as_uint(y[n][2]), __float_as_uint(y[n][3])});
            meet(C_P0 + (aw >> 1), 8, lane);
            const char* base = p.ws + WS_YT + (unsigned)(ch * 4096 + par * 2048);
#pragma unroll
            for (int e = 0; e < 2; ++e) {
                const unsigned a = yb + par * 4096 + e * 2048 + lane * 32;
                const u32x4 e0 = lds_r128(a), o0 = lds_r128(a + 16), e1 = lds_r128(a + 8192), o1 = lds_r128(a + 8192 + 16);
                u32x4 v;
#pragma unroll
                for (int k = 0; k < 4; ++k) v[k] = cvtpk(__uint_as_float(e0[k]) + __uint_as_float(e1[k]), __uint_as_float(o0[k]) + __uint_as_float(o1[k]));
                st16_sc1(base, e * 1024 + lane * 16, v);
            }
            WAIT_VM(0);
            if (arrive(C_Y, lane) == 3) set_flag(p.ws, b, 2, lane);
        }
        STAMP(8);
    }
    const int cs = j & 3, tt = 4 * i + (j >> 2);
    u32x4 wf[32];
    await(C_KV, 4);
    {
        const int tps = ((tp & 1) << 1) | (tp >> 1);
        const unsigned wa = L_W + (8 * hb + tq) * 256 + ((aw ^ tq) << 6) + (G & 1) * 32 + tps * 8;
        const unsigned wa_last = wa + (hb ? 0 : 1024);
        const unsigned so = (56 * aw + G) * 896 + ((((lane & 15) ^ (G << 2)) & (cs == 3 ? 7 : 15)) << 4);
#pragma unroll
        for (int hf = 0; hf < 2; ++hf) {
            dma_run<false>(p.wo + 256 * cs + hf * 224 * 896, so, 4 * 896, L_W + aw * 14336, 1024, 14);
            WAIT_VM(0);
            meet(C_WO, 4 + 8 * hf, lane);
#pragma unroll
            for (int hh = 0; hh < 8; ++hh) {
                wf[16 * hf + 2 * hh] = lds_tr2(wa + hh * 7168, wa + hh * 7168 + 1024);
                wf[16 * hf + 2 * hh + 1] = lds_tr2(wa + hh * 7168 + 4096, wa_last + hh * 7168 + 4096);
            }
            asm volatile("s_waitcnt lgkmcnt(0)" ::: "memory");
#pragma unroll
            for (int k = 0; k < 16; ++k) asm volatile("" : "+v"(wf[16 * hf + k]));
            meet(C_WO, 8 + 8 * hf, lane);
        }
    }
    STAMP(9);
    await(C_GO, 1);
    if (ssm) {
        const unsigned ro = (64 * m + 16 * aw + (lane >> 3));
        const u32x4 u0 = *(const GAS u32x4*)(p.u + ro * 896 + 128 * g + (lane & 7) * 16), u1 = *(const GAS u32x4*)(p.u + (ro + 8) * 896 + 128 * g + (lane & 7) * 16);
        const u32x4 dk = *(const GAS u32x4*)(p.dskip + 128 * g + (lane & 7) * 16);
        poll_flags(p.ws, WS_FLAG + (g + 8 * r) * 128 + 8);
        dma_run<true>(p.ws + WS_YT + (unsigned)(64 * g * 4096 + 128 * m), (unsigned)((16 * aw + (lane >> 3)) * 4096 + (((lane & 7) ^ ((lane >> 2) & 6)) << 4)), 8 * 4096, L_KX + aw * 2048, 1024, 2);
        WAIT_VM(0);
        meet(C_YT, 4, lane);
        const unsigned ta = L_KX + (16 * G + tq) * 128 + (((2 * aw + (tp >> 1)) ^ (2 * tq)) << 4) + (tp & 1) * 8;
        const u32x4 t0 = lds_tr2(ta, ta + 512), t1 = lds_tr2(ta + 1024, ta + 1536);
        lds_w128(gb + (lane & 15) * 144 + G * 32, t0); lds_w128(gb + (lane & 15) * 144 + G * 32 + 16, t1);
        fence_c();
#pragma unroll
        for (int e = 0; e < 2; ++e) {
            const u32x4 s = lds_r128(gb + (8 * e + (lane >> 3)) * 144 + (lane & 7) * 16), uu = e ? u1 : u0;
            u32x4 v;
#pragma unroll
            for (int k = 0; k < 4; ++k) { const unsigned sk = cvtpk(bflo(uu[k]) * bflo(dk[k]), bfhi(uu[k]) * bfhi(dk[k])); v[k] = cvtpk(bflo(s[k]) + bflo(sk), bfhi(s[k]) + bfhi(sk)); }
            st16(p.out + 128 * g, (ro + 8 * e) * 1792 + (lane & 7) * 16, v);
        }
    }
    poll_flags(p.ws, WS_FLAG + blk_of(i, lane & 15) * 128);
    STAMP(10);
    asm volatile("s_waitcnt lgkmcnt(0)\n\ts_barrier" ::: "memory");
    stage_o(p, tt, wid, lane);
    STAMP(11);
    f32x16 oc = {};
#pragma unroll
    for (int k = 0; k < 32; ++k) oc = mfma32(wf[k], lds_r128(L_W + k * 1024 + lane * 16), oc);
    lds_w128(gb + r * 80 + hb * 16, pack8(oc, 0)); lds_w128(gb + r * 80 + 32 + hb * 16, pack8(oc, 1));
    fence_c();
    if (cs < 3 || aw < 2) {
#pragma unroll
        for (int e = 0; e < 2; ++e)
            st16(p.out + 896 + 256 * cs + 64 * aw, (32 * tt + 16 * e + (lane >> 2)) * 1792 + (lane & 3) * 16, lds_r128(gb + (16 * e + (lane >> 2)) * 80 + (lane & 3) * 16));
    }
    STAMP(12);
}

__global__ void __launch_bounds__(512, 2) fused_s4_mha(Params p) {
    extern __shared__ __attribute__((aligned(256))) char smem[];
    const int lane = threadIdx.x & 63, wid = __builtin_amdgcn_readfirstlane(threadIdx.x >> 6), b = blockIdx.x;
    unsigned prof = 0;
    STAMP(0);
    {
        unsigned long long pc;
        asm volatile("s_getpc_b64 %0\n\ts_mov_b32 m0, %2\n\ts_nop 0\n\tglobal_load_lds_dword %1, %0" : "=&s"(pc) : "v"(min((threadIdx.x >> 1), 240u) * 128u), "s"(L_END) : "memory");
    }
    asm volatile("" :: "s"(p.u), "s"(p.logdt), "s"(p.x), "s"(p.ws));
    if (threadIdx.x < 32) *(LAS unsigned*)(L_SYNC + 4 * threadIdx.x) = 0u;
    const int i = ((b >> 2) & 1) * 8 + (b >> 5), j = (b & 3) * 4 + (~(b >> 3) & 3);
    if (wid < 4) chain_b(p, b, i, j, wid, lane PROF_PASS); else chain_a(p, b, i, j, wid, lane PROF_PASS);
    if ((wid >= 4) == (DIAG >= 100)) {
        const unsigned d = __builtin_amdgcn_readlane(prof, DIAG % 100) - __builtin_amdgcn_readlane(prof, 0);
        const unsigned long long e0 = __builtin_amdgcn_s_memrealtime();
        if (d < 10000u) while (__builtin_amdgcn_s_memrealtime() - e0 < 8ull * d) __builtin_amdgcn_s_sleep(4);
    }
}

extern "C" void kernel_launch(void* const* d_in, const int* in_sizes, int n_in,
                              void* d_out, int out_size, void* d_ws, size_t ws_size,
                              hipStream_t stream) {
    Params p;
    p.u = (const char*)d_in[0]; p.are = (const char*)d_in[1]; p.aim = (const char*)d_in[2]; p.bre = (const char*)d_in[3]; p.bim = (const char*)d_in[4];
    p.cre = (const char*)d_in[5]; p.cim = (const char*)d_in[6]; p.logdt = (const char*)d_in[7]; p.dskip = (const char*)d_in[8];
    p.x = (const char*)d_in[9]; p.wqkv = (const char*)d_in[10]; p.wo = (const char*)d_in[11];
    p.out = (char*)d_out; p.ws = (char*)d_ws;
    (void)hipFuncSetAttribute((const void*)fused_s4_mha, hipFuncAttributeMaxDynamicSharedMemorySize, L_END + 256);
    fused_s4_mha<<<256, 512, L_END + 256, stream>>>(p);
}
```

```cpp
#include <hip/hip_runtime.h>
#include <hip/hip_bf16.h>
#include <stdint.h>

struct Params {
    const void *u, *are, *aim, *bre, *bim, *cre, *cim, *logdt, *dskip, *x, *wqkv, *wo;
    void *out, *ws;
};

__global__ void __launch_bounds__(512, 2) fused_s4_mha(Params p) {
    asm volatile(
    "s_load_dwordx2 s[12:13], s[0:1], 0x50\n"
    "s_load_dwordx2 s[8:9], s[0:1], 0x48\n"
    "s_load_dwordx2 s[16:17], s[0:1], 0x68\n"
    "s_getpc_b64 s[48:49]\n"
    "s_mov_b32 m0, 160000\n"
    "s_bfe_u32 s4, s2, 0x20003\n"
    "v_lshrrev_b32 v1, 3, v0\n"
    "v_lshl_add_u32 v1, v1, 2, s4\n"
    "v_min_u32 v1, 264, v1\n"
    "v_lshlrev_b32 v1, 7, v1\n"
    "global_load_lds_dword v1, s[48:49]\n"
    "v_readfirstlane_b32 s3, v0\n"
    "s_mov_b32 s14, 1204224\n"
    "s_mov_b32 s15, 0x20000\n"
    "s_mov_b32 s10, 1835008\n"
    "s_mov_b32 s11, 0x20000\n"
    "s_mov_b32 s18, 9617408\n"
    "s_mov_b32 s19, 0x20000\n"
    "s_lshr_b32 s3, s3, 6\n"
    "s_cmp_gt_u32 s3, 3\n"
    "s_cbranch_scc1 .La1_%=\n"
    "v_and_b32 v1, 31, v0\n"
    "v_lshlrev_b32 v1, 2, v1\n"
    "v_mov_b32 v2, 0\n"
    "ds_write_b32 v1, v2 offset:64\n"
    "s_setprio 2\n"
    "v_and_b32 v79, 63, v0\n"
    "v_lshrrev_b32 v2, 3, v79\n"
    "v_and_b32 v3, 7, v0\n"
    "v_lshrrev_b32 v4, 4, v79\n"
    "v_lshrrev_b32 v5, 2, v79\n"
    "s_and_b32 s25, s2, 1\n"
    "s_lshr_b32 s48, s2, 3\n"
    "s_andn2_b32 s32, 7, s48\n"
    "s_lshr_b32 s48, s2, 6\n"
    "s_lshl_b32 s49, s2, 3\n"
    "s_and_b32 s49, s49, 48\n"
    "s_lshl2_add_u32 s33, s48, s49\n"
    "s_add_u32 s33, s33, s3\n"
    "s_mul_i32 s20, s33, 28672\n"
    "v_xor_b32 v4, v3, v4\n"
    "v_mul_u32_u24 v6, 896, v2\n"
    "v_lshl_or_b32 v240, v4, 4, v6\n"
    "v_and_b32 v4, 4, v5\n"
    "v_xor_b32 v4, v3, v4\n"
    "s_cmp_lt_u32 s32, 7\n"
    "s_cselect_b64 s[38:39], -1, 1\n"
    "s_cselect_b32 s48, s32, 0\n"
    "s_mul_i32 s49, s25, 7\n"
    "s_add_u32 s48, s48, s49\n"
    "s_lshl_b32 s48, s48, 7\n"
    "s_and_b32 s51, s3, 1\n"
    "s_mul_i32 s50, s51, 86016\n"
    "s_add_u32 s48, s48, s50\n"
    "s_add_u32 s21, s48, 896\n"
    "v_mul_u32_u24 v6, 2688, v2\n"
    "v_lshl_add_u32 v56, v4, 4, v6\n"
    "v_add_u32 v57, 20480, v56\n"
    "v_add_u32 v58, 40960, v56\n"
    "v_add_u32 v59, 61440, v56\n"
    "s_lshl_b32 s24, s51, 12\n"
    "s_add_u32 s24, s24, 102656\n"
    "s_mul_i32 s23, s3, 12288\n"
    "s_add_u32 s23, s23, 53504\n"
    "v_xor_b32 v4, 64, v240\n"
    "v_add_u32 v241, 6144, v4\n"
    "v_add_u32 v242, 12288, v240\n"
    "v_add_u32 v243, 18432, v4\n"
    "s_cmp_gt_u32 s3, 1\n"
    "s_waitcnt lgkmcnt(0)\n"
    "s_cbranch_scc1 .Lb_lw1_%=\n"
    "s_add_u32 m0, s23, 0\n"
    "s_nop 0\n"
    "buffer_load_dwordx4 v240, s[8:11], s20 offen offset:0 lds\n"
    "buffer_load_dwordx4 v241, s[8:11], s20 offen offset:1024 lds\n"
    "buffer_load_dwordx4 v242, s[8:11], s20 offen offset:2048 lds\n"
    "buffer_load_dwordx4 v243, s[8:11], s20 offen offset:3072 lds\n"
    "s_add_u32 m0, s23, 3968\n"
    "s_nop 0\n"
    "buffer_load_dwordx4 v240, s[8:11], s20 offen offset:128 lds\n"
    "buffer_load_dwordx4 v241, s[8:11], s20 offen offset:1152 lds\n"
    "buffer_load_dwordx4 v242, s[8:11], s20 offen offset:2176 lds\n"
    "buffer_load_dwordx4 v243, s[8:11], s20 offen offset:3200 lds\n"
    "s_barrier\n"
    "s_add_u32 m0, s23, 7936\n"
    "s_nop 0\n"
    "buffer_load_dwordx4 v240, s[8:11], s20 offen offset:256 lds\n"
    "buffer_load_dwordx4 v241, s[8:11], s20 offen offset:1280 lds\n"
    "buffer_load_dwordx4 v242, s[8:11], s20 offen offset:2304 lds\n"
    "buffer_load_dwordx4 v243, s[8:11], s20 offen offset:3328 lds\n"
    "s_branch .Lb_lj2_%=\n"
    ".Lb_lw1_%=:\n"
    "s_add_u32 m0, s24, 0\n"
    "s_mov_b64 exec, s[38:39]\n"
    "buffer_load_dwordx4 v56, s[12:15], s21 offen offset:0 lds\n"
    "buffer_load_dwordx4 v57, s[12:15], s21 offen offset:1024 lds\n"
    "buffer_load_dwordx4 v58, s[12:15], s21 offen offset:2048 lds\n"
    "buffer_load_dwordx4 v59, s[12:15], s21 offen offset:3072 lds\n"
    "s_mov_b64 exec, -1\n"
    "s_add_u32 s21, s21, 172032\n"
    "s_add_u32 m0, s24, 8192\n"
    "s_mov_b64 exec, s[38:39]\n"
    "buffer_load_dwordx4 v56, s[12:15], s21 offen offset:0 lds\n"
    "buffer_load_dwordx4 v57, s[12:15], s21 offen offset:1024 lds\n"
    "buffer_load_dwordx4 v58, s[12:15], s21 offen offset:2048 lds\n"
    "buffer_load_dwordx4 v59, s[12:15], s21 offen offset:3072 lds\n"
    "s_mov_b64 exec, -1\n"
    "s_add_u32 s21, s21, 172032\n"
    "s_barrier\n"
    "s_add_u32 m0, s24, 16384\n"
    "s_mov_b64 exec, s[38:39]\n"
    "buffer_load_dwordx4 v56, s[12:15], s21 offen offset:0 lds\n"
    "buffer_load_dwordx4 v57, s[12:15], s21 offen offset:1024 lds\n"
    "buffer_load_dwordx4 v58, s[12:15], s21 offen offset:2048 lds\n"
    "buffer_load_dwordx4 v59, s[12:15], s21 offen offset:3072 lds\n"
    "s_mov_b64 exec, -1\n"
    "s_add_u32 s21, s21, 172032\n"
    "s_add_u32 m0, s24, 24576\n"
    "s_mov_b64 exec, s[38:39]\n"
    "buffer_load_dwordx4 v56, s[12:15], s21 offen offset:0 lds\n"
    "buffer_load_dwordx4 v57, s[12:15], s21 offen offset:1024 lds\n"
    "buffer_load_dwordx4 v58, s[12:15], s21 offen offset:2048 lds\n"
    "buffer_load_dwordx4 v59, s[12:15], s21 offen offset:3072 lds\n"
    "s_mov_b64 exec, -1\n"
    "s_add_u32 s21, s21, 172032\n"
    ".Lb_lj2_%=:\n"
    "s_branch .Lb2_%=\n"
    ".La1_%=:\n"
    "s_load_dwordx16 s[20:35], s[0:1], 0x0\n"
    "s_load_dwordx2 s[36:37], s[0:1], 0x58\n"
    "s_load_dwordx2 s[40:41], s[0:1], 0x60\n"
    "s_setprio 3\n"
    "v_and_b32 v1, 63, v0\n"
    "s_sub_u32 s5, s3, 4\n"
    "s_and_b32 s6, s2, 7\n"
    "s_lshr_b32 s7, s2, 3\n"
    "s_lshl_b32 s48, s6, 7\n"
    "s_lshl2_add_u32 s48, s7, s48\n"
    "s_add_u32 s48, s48, s5\n"
    "s_lshl_b32 s49, s48, 8\n"
    "v_lshl_add_u32 v32, v1, 2, s49\n"
    "s_andn2_b32 s48, s48, 1\n"
    "v_mov_b32 v33, s48\n"
    "s_mov_b32 s50, 49152\n"
    "v_mov_b32 v222, 0\n"
    "s_waitcnt lgkmcnt(0)\n"
    "buffer_load_dword v7, off, s[16:19], s50 sc1\n"
    "s_cmp_eq_u32 s6, 7\n"
    "s_cselect_b32 s44, 0, 1\n"
    "s_cbranch_scc1 .La_ns1_%=\n"
    "global_load_short_d16_hi v222, v33, s[34:35]\n"
    "global_load_dword v216, v32, s[22:23]\n"
    "global_load_dword v217, v32, s[24:25]\n"
    "global_load_dword v218, v32, s[26:27]\n"
    "global_load_dword v219, v32, s[28:29]\n"
    "global_load_dword v220, v32, s[30:31]\n"
    "global_load_dword v221, v32, s[32:33]\n"
    ".La_bar2_%=:\n"
    "s_barrier\n"
    "s_branch .La2_%=\n"
    ".La_ns1_%=:\n"
    "s_cmp_eq_u32 s2, 7\n"
    "s_cbranch_scc0 .La_bar2_%=\n"
    "v_mov_b64 v[40:41], 0\n"
    "v_mov_b64 v[42:43], 0\n"
    "v_lshlrev_b32 v38, 4, v1\n"
    "s_mul_i32 s48, s5, 10240\n"
    "s_add_u32 s49, s48, 4096\n"
    "s_add_u32 s55, s48, 8192\n"
    "buffer_store_dwordx4 v[40:43], v38, s[16:19], s48 offen offset:0 sc1\n"
    "buffer_store_dwordx4 v[40:43], v38, s[16:19], s48 offen offset:1024 sc1\n"
    "buffer_store_dwordx4 v[40:43], v38, s[16:19], s48 offen offset:2048 sc1\n"
    "buffer_store_dwordx4 v[40:43], v38, s[16:19], s48 offen offset:3072 sc1\n"
    "buffer_store_dwordx4 v[40:43], v38, s[16:19], s49 offen offset:0 sc1\n"
    "buffer_store_dwordx4 v[40:43], v38, s[16:19], s49 offen offset:1024 sc1\n"
    "buffer_store_dwordx4 v[40:43], v38, s[16:19], s49 offen offset:2048 sc1\n"
    "buffer_store_dwordx4 v[40:43], v38, s[16:19], s49 offen offset:3072 sc1\n"
    "buffer_store_dwordx4 v[40:43], v38, s[16:19], s55 offen offset:0 sc1\n"
    "buffer_store_dwordx4 v[40:43], v38, s[16:19], s55 offen offset:1024 sc1\n"
    "s_branch .La_bar2_%=\n"
    ".Lb2_%=:\n"
    "s_sub_u32 s48, s32, s25\n"
    "s_add_u32 s48, s48, 7\n"
    "s_and_b32 s48, s48, 7\n"
    "s_lshl3_add_u32 s31, s25, s48\n"
    "s_mul_i32 s36, s31, 7\n"
    "s_lshr_b32 s36, s36, 1\n"
    "s_lshl4_add_u32 s22, s36, s50\n"
    "v_and_b32 v6, 3, v0\n"
    "v_mul_u32_u24 v7, 2688, v5\n"
    "v_lshl_add_u32 v245, v6, 4, v7\n"
    "v_add_u32 v246, 41984, v245\n"
    "v_lshlrev_b32 v74, 4, v79\n"
    "v_mov_b32 v75, 0\n"
    "v_mov_b32 v78, 1\n"
    "v_and_b32 v8, 31, v0\n"
    "v_lshrrev_b32 v9, 5, v79\n"
    "v_and_b32 v10, 0x13, v0\n"
    "v_and_b32 v11, 4, v0\n"
    "v_lshl_or_b32 v10, v11, 1, v10\n"
    "v_and_b32 v15, 8, v0\n"
    "v_lshrrev_b32 v11, 1, v15\n"
    "v_or_b32 v10, v10, v11\n"
    "v_bfe_u32 v11, v0, 1, 3\n"
    "v_xor_b32 v11, v11, v9\n"
    "v_lshlrev_b32 v11, 4, v11\n"
    "v_lshl_or_b32 v11, v8, 7, v11\n"
    "v_add_u32 v64, s23, v11\n"
    "v_xor_b32 v65, 32, v64\n"
    "v_xor_b32 v66, 64, v64\n"
    "v_xor_b32 v67, 0x60, v64\n"
    "v_bfrev_b32 v11, v0\n"
    "v_lshrrev_b32 v11, 30, v11\n"
    "v_and_b32 v12, 3, v5\n"
    "v_lshl_or_b32 v12, v9, 3, v12\n"
    "v_and_b32 v13, 16, v0\n"
    "v_lshlrev_b32 v13, 1, v13\n"
    "v_lshl_or_b32 v14, v11, 3, v13\n"
    "v_lshl_or_b32 v14, v12, 6, v14\n"
    "v_add_u32 v70, 53504, v14\n"
    "s_cmp_lg_u32 s25, 0\n"
    "s_cselect_b64 vcc, -1, 0\n"
    "v_cndmask_b32 v14, v11, v6, vcc\n"
    "v_lshl_or_b32 v14, v14, 3, v13\n"
    "v_lshl_or_b32 v14, v12, 7, v14\n"
    "v_lshl_or_b32 v14, v15, 3, v14\n"
    "v_add_u32 v68, 102656, v14\n"
    "v_xor_b32 v69, 64, v68\n"
    "v_mul_u32_u24 v14, 7168, v9\n"
    "v_lshl_add_u32 v15, v8, 4, v14\n"
    "v_lshlrev_b32 v16, 9, v9\n"
    "v_lshl_or_b32 v16, v10, 4, v16\n"
    "v_cndmask_b32 v247, v16, v15, vcc\n"
    "v_lshl_add_u32 v72, v10, 4, v14\n"
    "s_cselect_b32 s48, 10, 12\n"
    "s_cselect_b32 s49, 1835008, 0\n"
    "s_lshl_b32 s48, s32, s48\n"
    "s_add_u32 s48, s48, s49\n"
    "s_add_u32 s48, s48, s20\n"
    "s_add_u32 s26, s48, 65536\n"
    "s_mul_i32 s48, s25, 13312\n"
    "s_add_u32 s48, s48, 1024\n"
    "s_add_u32 s37, s26, s48\n"
    "s_mul_i32 s49, s25, 4294965760\n"
    "s_add_u32 s49, s49, 2048\n"
    "s_add_u32 s47, s26, s49\n"
    "s_add_u32 s99, s47, s48\n"
    "s_andn2_b32 s48, 0x40000000, s38\n"
    "v_or_b32 v247, s48, v247\n"
    "s_cmp_gt_u32 s3, 1\n"
    "s_cselect_b64 s[6:7], 1, -1\n"
    "s_mov_b32 s5, s22\n"
    "s_cbranch_scc0 .Lb_le3_%=\n"
    "s_add_u32 s5, s5, 1032192\n"
    "s_waitcnt vmcnt(12)\n"
    "s_mov_b64 exec, 1\n"
    "ds_add_u32 v75, v78 offset:64\n"
    "s_mov_b64 exec, -1\n"
    "s_add_u32 m0, s24, 32768\n"
    "s_mov_b64 exec, s[38:39]\n"
    "buffer_load_dwordx4 v56, s[12:15], s21 offen offset:0 lds\n"
    "buffer_load_dwordx4 v57, s[12:15], s21 offen offset:1024 lds\n"
    "buffer_load_dwordx4 v58, s[12:15], s21 offen offset:2048 lds\n"
    "buffer_load_dwordx4 v59, s[12:15], s21 offen offset:3072 lds\n"
    "s_mov_b64 exec, -1\n"
    "s_add_u32 s21, s21, 172032\n"
    "s_mul_i32 s48, s31, 448\n"
    "s_add_u32 s48, s48, 1835008\n"
    "v_add_u32 v72, s48, v72\n"
    "s_lshl_b32 s48, s36, 9\n"
    "v_add_u32 v71, s48, v74\n"
    "s_lshl_b32 s48, s2, 7\n"
    "s_add_u32 s27, s48, 8\n"
    "s_mul_i32 s48, s3, 114688\n"
    "s_add_u32 s28, s48, 65536\n"
    "s_add_u32 s29, s48, 79872\n"
    "s_bitcmp1_b32 s31, 0\n"
    "s_cselect_b32 s40, -1, 0\n"
    "s_cselect_b32 s43, 0, -1\n"
    "s_mov_b32 s41, 0\n"
    "s_mov_b32 s42, 0\n"
    "s_mov_b32 s44, 0x0fffffff\n"
    "s_mov_b32 s45, 0x0fffffff\n"
    "s_mov_b32 s46, 0x3e8b97f6\n"
    "s_waitcnt vmcnt(12)\n"
    "s_mov_b64 exec, 1\n"
    "ds_add_u32 v75, v78 offset:68\n"
    "s_mov_b64 exec, -1\n"
    "s_add_u32 m0, s24, 40960\n"
    "s_mov_b64 exec, s[38:39]\n"
    "buffer_load_dwordx4 v56, s[12:15], s21 offen offset:0 lds\n"
    "buffer_load_dwordx4 v57, s[12:15], s21 offen offset:1024 lds\n"
    "buffer_load_dwordx4 v58, s[12:15], s21 offen offset:2048 lds\n"
    "buffer_load_dwordx4 v59, s[12:15], s21 offen offset:3072 lds\n"
    "s_mov_b64 exec, -1\n"
    "s_add_u32 s21, s21, 172032\n"
    "s_lshr_b32 s52, s36, 3\n"
    "s_add_u32 s48, s36, 3\n"
    "s_lshr_b32 s48, s48, 3\n"
    "s_lshl4_add_u32 s52, s48, s52\n"
    "s_mul_i32 s49, s31, 28\n"
    "s_lshr_b32 s48, s49, 6\n"
    "s_lshl_b32 s48, s48, 8\n"
    "s_add_u32 s52, s52, s48\n"
    "s_add_u32 s48, s49, 27\n"
    "s_lshr_b32 s48, s48, 6\n"
    "s_lshl_b32 s48, s48, 12\n"
    "s_add_u32 s52, s52, s48\n"
    "s_xor_b32 s52, s52, 0x7777\n"
    "v_and_b32 v192, 3, v79\n"
    "v_lshlrev_b32 v192, 2, v192\n"
    "v_bfe_u32 v192, s52, v192, 3\n"
    "v_bfe_u32 v193, v79, 1, 1\n"
    "v_lshl_or_b32 v192, v192, 3, v193\n"
    "v_lshrrev_b32 v193, 4, v79\n"
    "v_lshl_or_b32 v192, v193, 1, v192\n"
    "v_lshlrev_b32 v192, 7, v192\n"
    "s_lshl_b32 s48, s3, 13\n"
    "s_add_u32 s48, s48, 8\n"
    "v_add_u32 v73, s48, v192\n"
    "s_waitcnt vmcnt(12)\n"
    "s_mov_b64 exec, 1\n"
    "ds_add_u32 v75, v78 offset:72\n"
    "s_mov_b64 exec, -1\n"
    "s_add_u32 m0, s24, 49152\n"
    "s_mov_b64 exec, s[38:39]\n"
    "buffer_load_dwordx4 v56, s[12:15], s21 offen offset:0 lds\n"
    "buffer_load_dwordx4 v57, s[12:15], s21 offen offset:1024 lds\n"
    "buffer_load_dwordx4 v58, s[12:15], s21 offen offset:2048 lds\n"
    "buffer_load_dwordx4 v59, s[12:15], s21 offen offset:3072 lds\n"
    "s_mov_b64 exec, -1\n"
    "s_add_u32 s21, s21, 172032\n"
    "s_add_u32 s48, s3, 1\n"
    "s_and_b32 s48, s48, 3\n"
    "s_mul_i32 s48, s48, 12288\n"
    "s_add_u32 s94, s48, 53504\n"
    "s_add_u32 s48, s3, 2\n"
    "s_and_b32 s48, s48, 3\n"
    "s_mul_i32 s48, s48, 12288\n"
    "s_add_u32 s95, s48, 53504\n"
    "s_add_u32 s48, s3, 3\n"
    "s_and_b32 s48, s48, 3\n"
    "s_mul_i32 s48, s48, 12288\n"
    "s_add_u32 s96, s48, 53504\n"
    "s_add_u32 s48, s3, 4\n"
    "s_and_b32 s48, s48, 3\n"
    "s_mul_i32 s48, s48, 12288\n"
    "s_add_u32 s97, s48, 53504\n"
    "s_waitcnt vmcnt(12)\n"
    "s_mov_b64 exec, 1\n"
    "ds_add_u32 v75, v78 offset:76\n"
    "s_mov_b64 exec, -1\n"
    "s_add_u32 m0, s23, 0\n"
    "s_nop 0\n"
    "buffer_load_dwordx4 v240, s[8:11], s20 offen offset:0 lds\n"
    "buffer_load_dwordx4 v241, s[8:11], s20 offen offset:1024 lds\n"
    "buffer_load_dwordx4 v242, s[8:11], s20 offen offset:2048 lds\n"
    "buffer_load_dwordx4 v243, s[8:11], s20 offen offset:3072 lds\n"
    "s_lshl_b32 s48, s33, 15\n"
    "s_lshl_b32 s49, s31, 11\n"
    "s_add_u32 s48, s48, s49\n"
    "s_add_u32 s34, s48, 3735552\n"
    "s_andn2_b32 s48, s33, 3\n"
    "s_lshl1_add_u32 s48, s25, s48\n"
    "s_lshr_b32 s49, s32, 2\n"
    "s_add_u32 s48, s48, s49\n"
    "s_lshl2_add_u32 s48, s48, s3\n"
    "s_lshl_b32 s48, s48, 13\n"
    "s_add_u32 s35, s48, 3735552\n"
    "s_load_dwordx2 s[88:89], s[0:1], 0x0\n"
    "s_load_dwordx2 s[90:91], s[0:1], 0x40\n"
    "s_load_dwordx2 s[92:93], s[0:1], 0x60\n"
    "s_lshl_b32 s48, s3, 13\n"
    "s_add_u32 s98, s48, 53504\n"
    "s_waitcnt vmcnt(12)\n"
    "s_mov_b64 exec, 1\n"
    "ds_add_u32 v75, v78 offset:80\n"
    "s_mov_b64 exec, -1\n"
    "s_add_u32 m0, s23, 3968\n"
    "s_nop 0\n"
    "buffer_load_dwordx4 v240, s[8:11], s20 offen offset:128 lds\n"
    "buffer_load_dwordx4 v241, s[8:11], s20 offen offset:1152 lds\n"
    "buffer_load_dwordx4 v242, s[8:11], s20 offen offset:2176 lds\n"
    "buffer_load_dwordx4 v243, s[8:11], s20 offen offset:3200 lds\n"
    "s_mov_b32 s48, 0x3f803f80\n"
    "s_mov_b32 s49, 0x3f803f80\n"
    "v_mov_b64 v[200:201], s[48:49]\n"
    "v_mov_b64 v[202:203], s[48:49]\n"
    "v_mov_b64 v[204:205], s[48:49]\n"
    "v_mov_b64 v[206:207], s[48:49]\n"
    "v_mov_b64 v[216:217], s[48:49]\n"
    "v_mov_b64 v[218:219], s[48:49]\n"
    "v_mov_b64 v[220:221], s[48:49]\n"
    "v_mov_b64 v[222:223], s[48:49]\n"
    "v_mov_b64 v[232:233], s[48:49]\n"
    "v_mov_b64 v[234:235], s[48:49]\n"
    "v_mov_b64 v[236:237], s[48:49]\n"
    "v_mov_b64 v[238:239], s[48:49]\n"
    "v_mov_b64 v[248:249], s[48:49]\n"
    "v_mov_b64 v[250:251], s[48:49]\n"
    "v_mov_b64 v[252:253], s[48:49]\n"
    "v_mov_b64 v[254:255], s[48:49]\n"
    "s_waitcnt vmcnt(12)\n"
    "s_mov_b64 exec, 1\n"
    "ds_add_u32 v75, v78 offset:84\n"
    "s_mov_b64 exec, -1\n"
    "s_add_u32 m0, s23, 7936\n"
    "s_nop 0\n"
    "buffer_load_dwordx4 v240, s[8:11], s20 offen offset:256 lds\n"
    "buffer_load_dwordx4 v241, s[8:11], s20 offen offset:1280 lds\n"
    "buffer_load_dwordx4 v242, s[8:11], s20 offen offset:2304 lds\n"
    "buffer_load_dwordx4 v243, s[8:11], s20 offen offset:3328 lds\n"
    "s_waitcnt vmcnt(12)\n"
    "s_mov_b64 exec, 1\n"
    "ds_add_u32 v75, v78 offset:88\n"
    "s_mov_b64 exec, -1\n"
    ".Lb_le3_%=:\n"
    "s_cmp_lg_u32 s25, 0\n"
    "s_cbranch_scc1 .Lb_v4_%=\n"
    "s_waitcnt vmcnt(8)\n"
    "ds_read_b32 v76, v75 offset:64\n"
    "ds_read_b128 v[80:83], v64 offset:0\n"
    "ds_read_b128 v[84:87], v65 offset:0\n"
    "ds_read_b128 v[88:91], v66 offset:0\n"
    "ds_read_b128 v[92:95], v67 offset:0\n"
    "s_waitcnt lgkmcnt(4)\n"
    "v_cmp_gt_u32 vcc, 2, v76\n"
    "s_nop 0\n"
    "s_cbranch_vccnz .Lb_sl7_%=\n"
    ".Lb_ok6_%=:\n"
    "ds_read_b64_tr_b16 v[32:33], v68 offset:0\n"
    "ds_read_b64_tr_b16 v[34:35], v68 offset:512\n"
    "ds_read_b64_tr_b16 v[36:37], v69 offset:0\n"
    "ds_read_b64_tr_b16 v[38:39], v69 offset:512\n"
    "ds_read_b64_tr_b16 v[40:41], v68 offset:2048\n"
    "ds_read_b64_tr_b16 v[42:43], v68 offset:2560\n"
    "ds_read_b64_tr_b16 v[44:45], v69 offset:2048\n"
    "ds_read_b64_tr_b16 v[46:47], v69 offset:2560\n"
    "ds_read_b64_tr_b16 v[48:49], v68 offset:4096\n"
    "ds_read_b64_tr_b16 v[50:51], v68 offset:4608\n"
    "s_waitcnt lgkmcnt(8)\n"
    "v_mfma_f32_32x32x16_bf16 v[0:15], v[32:35], v[80:83], 0\n"
    "ds_read_b64_tr_b16 v[52:53], v69 offset:4096\n"
    "ds_read_b64_tr_b16 v[54:55], v69 offset:4608\n"
    "ds_read_b32 v76, v75 offset:68\n"
    "s_waitcnt lgkmcnt(9)\n"
    "v_mfma_f32_32x32x16_bf16 v[16:31], v[36:39], v[80:83], 0\n"
    "ds_read_b64_tr_b16 v[32:33], v68 offset:6144\n"
    "ds_read_b64_tr_b16 v[34:35], v68 offset:6656\n"
    "s_waitcnt lgkmcnt(9)\n"
    "v_mfma_f32_32x32x16_bf16 v[0:15], v[40:43], v[84:87], v[0:15]\n"
    "s_add_u32 m0, s23, -384\n"
    "s_nop 0\n"
    "buffer_load_dwordx4 v240, s[8:11], s20 offen offset:384 lds\n"
    "ds_read_b64_tr_b16 v[36:37], v69 offset:6144\n"
    "ds_read_b64_tr_b16 v[38:39], v69 offset:6656\n"
    "s_waitcnt lgkmcnt(9)\n"
    "v_mfma_f32_32x32x16_bf16 v[16:31], v[44:47], v[84:87], v[16:31]\n"
    "buffer_load_dwordx4 v241, s[8:11], s20 offen offset:1408 lds\n"
    "s_waitcnt lgkmcnt(4)\n"
    "v_cmp_gt_u32 vcc, 2, v76\n"
    "s_nop 0\n"
    "s_cbranch_vccnz .Lb_sl10_%=\n"
    ".Lb_ok9_%=:\n"
    "ds_read_b64_tr_b16 v[40:41], v68 offset:8192\n"
    "ds_read_b64_tr_b16 v[42:43], v68 offset:8704\n"
    "v_mfma_f32_32x32x16_bf16 v[0:15], v[48:51], v[88:91], v[0:15]\n"
    "buffer_load_dwordx4 v242, s[8:11], s20 offen offset:2432 lds\n"
    "ds_read_b64_tr_b16 v[44:45], v69 offset:8192\n"
    "ds_read_b64_tr_b16 v[46:47], v69 offset:8704\n"
    "v_mfma_f32_32x32x16_bf16 v[16:31], v[52:55], v[88:91], v[16:31]\n"
    "buffer_load_dwordx4 v243, s[8:11], s20 offen offset:3456 lds\n"
    "s_waitcnt vmcnt(8)\n"
    "ds_read_b128 v[96:99], v64 offset:4096\n"
    "ds_read_b128 v[100:103], v65 offset:4096\n"
    "ds_read_b128 v[104:107], v66 offset:4096\n"
    "ds_read_b128 v[108:111], v67 offset:4096\n"
    "ds_read_b64_tr_b16 v[48:49], v68 offset:10240\n"
    "ds_read_b64_tr_b16 v[50:51], v68 offset:10752\n"
    "s_waitcnt lgkmcnt(12)\n"
    "v_mfma_f32_32x32x16_bf16 v[0:15], v[32:35], v[92:95], v[0:15]\n"
    "ds_read_b64_tr_b16 v[52:53], v69 offset:10240\n"
    "ds_read_b64_tr_b16 v[54:55], v69 offset:10752\n"
    "s_waitcnt lgkmcnt(12)\n"
    "v_mfma_f32_32x32x16_bf16 v[16:31], v[36:39], v[92:95], v[16:31]\n"
    "ds_read_b64_tr_b16 v[32:33], v68 offset:12288\n"
    "ds_read_b64_tr_b16 v[34:35], v68 offset:12800\n"
    "s_waitcnt lgkmcnt(9)\n"
    "v_mfma_f32_32x32x16_bf16 v[0:15], v[40:43], v[96:99], v[0:15]\n"
    "ds_read_b64_tr_b16 v[36:37], v69 offset:12288\n"
    "ds_read_b64_tr_b16 v[38:39], v69 offset:12800\n"
    "ds_read_b32 v76, v75 offset:72\n"
    "v_mfma_f32_32x32x16_bf16 v[16:31], v[44:47], v[96:99], v[16:31]\n"
    "s_waitcnt lgkmcnt(9)\n"
    "ds_read_b64_tr_b16 v[40:41], v68 offset:14336\n"
    "ds_read_b64_tr_b16 v[42:43], v68 offset:14848\n"
    "s_waitcnt lgkmcnt(9)\n"
    "v_mfma_f32_32x32x16_bf16 v[0:15], v[48:51], v[100:103], v[0:15]\n"
    "s_add_u32 m0, s23, 3584\n"
    "s_nop 0\n"
    "buffer_load_dwordx4 v240, s[8:11], s20 offen offset:512 lds\n"
    "ds_read_b64_tr_b16 v[44:45], v69 offset:14336\n"
    "ds_read_b64_tr_b16 v[46:47], v69 offset:14848\n"
    "s_waitcnt lgkmcnt(9)\n"
    "v_mfma_f32_32x32x16_bf16 v[16:31], v[52:55], v[100:103], v[16:31]\n"
    "buffer_load_dwordx4 v241, s[8:11], s20 offen offset:1536 lds\n"
    "s_waitcnt lgkmcnt(4)\n"
    "v_cmp_gt_u32 vcc, 2, v76\n"
    "s_nop 0\n"
    "s_cbranch_vccnz .Lb_sl13_%=\n"
    ".Lb_ok12_%=:\n"
    "ds_read_b64_tr_b16 v[48:49], v68 offset:16384\n"
    "ds_read_b64_tr_b16 v[50:51], v68 offset:16896\n"
    "v_mfma_f32_32x32x16_bf16 v[0:15], v[32:35], v[104:107], v[0:15]\n"
    "buffer_load_dwordx4 v242, s[8:11], s20 offen offset:2560 lds\n"
    "ds_read_b64_tr_b16 v[52:53], v69 offset:16384\n"
    "ds_read_b64_tr_b16 v[54:55], v69 offset:16896\n"
    "v_mfma_f32_32x32x16_bf16 v[16:31], v[36:39], v[104:107], v[16:31]\n"
    "buffer_load_dwordx4 v243, s[8:11], s20 offen offset:3584 lds\n"
    "s_waitcnt vmcnt(8)\n"
    "ds_read_b128 v[112:115], v64 offset:8192\n"
    "ds_read_b128 v[116:119], v65 offset:8192\n"
    "ds_read_b128 v[120:123], v66 offset:8192\n"
    "ds_read_b128 v[124:127], v67 offset:8192\n"
    "ds_read_b64_tr_b16 v[32:33], v68 offset:18432\n"
    "ds_read_b64_tr_b16 v[34:35], v68 offset:18944\n"
    "s_waitcnt lgkmcnt(12)\n"
    "v_mfma_f32_32x32x16_bf16 v[0:15], v[40:43], v[108:111], v[0:15]\n"
    "ds_read_b64_tr_b16 v[36:37], v69 offset:18432\n"
    "ds_read_b64_tr_b16 v[38:39], v69 offset:18944\n"
    "s_waitcnt lgkmcnt(12)\n"
    "v_mfma_f32_32x32x16_bf16 v[16:31], v[44:47], v[108:111], v[16:31]\n"
    "ds_read_b64_tr_b16 v[40:41], v68 offset:20480\n"
    "ds_read_b64_tr_b16 v[42:43], v68 offset:20992\n"
    "s_waitcnt lgkmcnt(9)\n"
    "v_mfma_f32_32x32x16_bf16 v[0:15], v[48:51], v[112:115], v[0:15]\n"
    "ds_read_b64_tr_b16 v[44:45], v69 offset:20480\n"
    "ds_read_b64_tr_b16 v[46:47], v69 offset:20992\n"
    "ds_read_b32 v76, v75 offset:76\n"
    "v_mfma_f32_32x32x16_bf16 v[16:31], v[52:55], v[112:115], v[16:31]\n"
    "s_waitcnt lgkmcnt(9)\n"
    "ds_read_b64_tr_b16 v[48:49], v68 offset:22528\n"
    "ds_read_b64_tr_b16 v[50:51], v68 offset:23040\n"
    "s_waitcnt lgkmcnt(9)\n"
    "v_mfma_f32_32x32x16_bf16 v[0:15], v[32:35], v[116:119], v[0:15]\n"
    "s_add_u32 m0, s23, 7552\n"
    "s_nop 0\n"
    "buffer_load_dwordx4 v240, s[8:11], s20 offen offset:640 lds\n"
    "ds_read_b64_tr_b16 v[52:53], v69 offset:22528\n"
    "ds_read_b64_tr_b16 v[54:55], v69 offset:23040\n"
    "s_waitcnt lgkmcnt(9)\n"
    "v_mfma_f32_32x32x16_bf16 v[16:31], v[36:39], v[116:119], v[16:31]\n"
    "buffer_load_dwordx4 v241, s[8:11], s20 offen offset:1664 lds\n"
    "s_waitcnt lgkmcnt(4)\n"
    "v_cmp_gt_u32 vcc, 2, v76\n"
    "s_nop 0\n"
    "s_cbranch_vccnz .Lb_sl16_%=\n"
    ".Lb_ok15_%=:\n"
    "ds_read_b64_tr_b16 v[32:33], v68 offset:24576\n"
    "ds_read_b64_tr_b16 v[34:35], v68 offset:25088\n"
    "v_mfma_f32_32x32x16_bf16 v[0:15], v[40:43], v[120:123], v[0:15]\n"
    "buffer_load_dwordx4 v242, s[8:11], s20 offen offset:2688 lds\n"
    "ds_read_b64_tr_b16 v[36:37], v69 offset:24576\n"
    "ds_read_b64_tr_b16 v[38:39], v69 offset:25088\n"
    "v_mfma_f32_32x32x16_bf16 v[16:31], v[44:47], v[120:123], v[16:31]\n"
    "buffer_load_dwordx4 v243, s[8:11], s20 offen offset:3712 lds\n"
    "s_waitcnt vmcnt(8)\n"
    "ds_read_b128 v[128:131], v64 offset:0\n"
    "ds_read_b128 v[132:135], v65 offset:0\n"
    "ds_read_b128 v[136:139], v66 offset:0\n"
    "ds_read_b128 v[140:143], v67 offset:0\n"
    "ds_read_b64_tr_b16 v[40:41], v68 offset:26624\n"
    "ds_read_b64_tr_b16 v[42:43], v68 offset:27136\n"
    "s_waitcnt lgkmcnt(12)\n"
    "v_mfma_f32_32x32x16_bf16 v[0:15], v[48:51], v[124:127], v[0:15]\n"
    "ds_read_b64_tr_b16 v[44:45], v69 offset:26624\n"
    "ds_read_b64_tr_b16 v[46:47], v69 offset:27136\n"
    "s_waitcnt lgkmcnt(12)\n"
    "v_mfma_f32_32x32x16_bf16 v[16:31], v[52:55], v[124:127], v[16:31]\n"
    "ds_read_b64_tr_b16 v[48:49], v68 offset:28672\n"
    "ds_read_b64_tr_b16 v[50:51], v68 offset:29184\n"
    "s_waitcnt lgkmcnt(9)\n"
    "v_mfma_f32_32x32x16_bf16 v[0:15], v[32:35], v[128:131], v[0:15]\n"
    "ds_read_b64_tr_b16 v[52:53], v69 offset:28672\n"
    "ds_read_b64_tr_b16 v[54:55], v69 offset:29184\n"
    "ds_read_b32 v76, v75 offset:80\n"
    "v_mfma_f32_32x32x16_bf16 v[16:31], v[36:39], v[128:131], v[16:31]\n"
    "s_waitcnt lgkmcnt(9)\n"
    "ds_read_b64_tr_b16 v[32:33], v68 offset:30720\n"
    "ds_read_b64_tr_b16 v[34:35], v68 offset:31232\n"
    "s_waitcnt lgkmcnt(9)\n"
    "v_mfma_f32_32x32x16_bf16 v[0:15], v[40:43], v[132:135], v[0:15]\n"
    "s_add_u32 m0, s23, -768\n"
    "s_nop 0\n"
    "buffer_load_dwordx4 v240, s[8:11], s20 offen offset:768 lds\n"
    "ds_read_b64_tr_b16 v[36:37], v69 offset:30720\n"
    "ds_read_b64_tr_b16 v[38:39], v69 offset:31232\n"
    "s_waitcnt lgkmcnt(9)\n"
    "v_mfma_f32_32x32x16_bf16 v[16:31], v[44:47], v[132:135], v[16:31]\n"
    "buffer_load_dwordx4 v241, s[8:11], s20 offen offset:1792 lds\n"
    "s_waitcnt lgkmcnt(4)\n"
    "v_cmp_gt_u32 vcc, 2, v76\n"
    "s_nop 0\n"
    "s_cbranch_vccnz .Lb_sl19_%=\n"
    ".Lb_ok18_%=:\n"
    "ds_read_b64_tr_b16 v[40:41], v68 offset:32768\n"
    "ds_read_b64_tr_b16 v[42:43], v68 offset:33280\n"
    "v_mfma_f32_32x32x16_bf16 v[0:15], v[48:51], v[136:139], v[0:15]\n"
    "buffer_load_dwordx4 v242, s[8:11], s20 offen offset:2816 lds\n"
    "ds_read_b64_tr_b16 v[44:45], v69 offset:32768\n"
    "ds_read_b64_tr_b16 v[46:47], v69 offset:33280\n"
    "v_mfma_f32_32x32x16_bf16 v[16:31], v[52:55], v[136:139], v[16:31]\n"
    "buffer_load_dwordx4 v243, s[8:11], s20 offen offset:3840 lds\n"
    "s_waitcnt vmcnt(8)\n"
    "ds_read_b128 v[144:147], v64 offset:4096\n"
    "ds_read_b128 v[148:151], v65 offset:4096\n"
    "ds_read_b128 v[152:155], v66 offset:4096\n"
    "ds_read_b128 v[156:159], v67 offset:4096\n"
    "ds_read_b64_tr_b16 v[48:49], v68 offset:34816\n"
    "ds_read_b64_tr_b16 v[50:51], v68 offset:35328\n"
    "s_waitcnt lgkmcnt(12)\n"
    "v_mfma_f32_32x32x16_bf16 v[0:15], v[32:35], v[140:143], v[0:15]\n"
    "ds_read_b64_tr_b16 v[52:53], v69 offset:34816\n"
    "ds_read_b64_tr_b16 v[54:55], v69 offset:35328\n"
    "s_waitcnt lgkmcnt(12)\n"
    "v_mfma_f32_32x32x16_bf16 v[16:31], v[36:39], v[140:143], v[16:31]\n"
    "ds_read_b64_tr_b16 v[32:33], v68 offset:36864\n"
    "ds_read_b64_tr_b16 v[34:35], v68 offset:37376\n"
    "s_waitcnt lgkmcnt(9)\n"
    "v_mfma_f32_32x32x16_bf16 v[0:15], v[40:43], v[144:147], v[0:15]\n"
    "ds_read_b64_tr_b16 v[36:37], v69 offset:36864\n"
    "ds_read_b64_tr_b16 v[38:39], v69 offset:37376\n"
    "ds_read_b32 v76, v75 offset:84\n"
    "v_mfma_f32_32x32x16_bf16 v[16:31], v[44:47], v[144:147], v[16:31]\n"
    "s_waitcnt lgkmcnt(9)\n"
    "ds_read_b64_tr_b16 v[40:41], v68 offset:38912\n"
    "ds_read_b64_tr_b16 v[42:43], v68 offset:39424\n"
    "s_waitcnt lgkmcnt(9)\n"
    "v_mfma_f32_32x32x16_bf16 v[0:15], v[48:51], v[148:151], v[0:15]\n"
    "s_add_u32 m0, s23, 4096\n"
    "s_add_u32 s48, s22, 0\n"
    "buffer_load_dwordx4 v245, s[12:15], s5 offen offset:0 lds\n"
    "ds_read_b64_tr_b16 v[44:45], v69 offset:38912\n"
    "ds_read_b64_tr_b16 v[46:47], v69 offset:39424\n"
    "s_waitcnt lgkmcnt(9)\n"
    "v_mfma_f32_32x32x16_bf16 v[16:31], v[52:55], v[148:151], v[16:31]\n"
    "buffer_load_dwordx4 v246, s[12:15], s5 offen offset:1024 lds\n"
    "s_waitcnt lgkmcnt(4)\n"
    "v_cmp_gt_u32 vcc, 2, v76\n"
    "s_nop 0\n"
    "s_cbranch_vccnz .Lb_sl22_%=\n"
    ".Lb_ok21_%=:\n"
    "ds_read_b64_tr_b16 v[48:49], v68 offset:40960\n"
    "ds_read_b64_tr_b16 v[50:51], v68 offset:41472\n"
    "v_mfma_f32_32x32x16_bf16 v[0:15], v[32:35], v[152:155], v[0:15]\n"
    "s_add_u32 m0, s23, 6144\n"
    "s_add_u32 s48, s22, 172032\n"
    "s_mov_b64 exec, s[6:7]\n"
    "buffer_load_dwordx4 v245, s[12:15], s48 offen offset:0 lds\n"
    "s_mov_b64 exec, -1\n"
    "ds_read_b64_tr_b16 v[52:53], v69 offset:40960\n"
    "ds_read_b64_tr_b16 v[54:55], v69 offset:41472\n"
    "v_mfma_f32_32x32x16_bf16 v[16:31], v[36:39], v[152:155], v[16:31]\n"
    "s_mov_b64 exec, s[6:7]\n"
    "buffer_load_dwordx4 v246, s[12:15], s48 offen offset:1024 lds\n"
    "s_mov_b64 exec, -1\n"
    "s_waitcnt vmcnt(8)\n"
    "ds_read_b128 v[160:163], v64 offset:8192\n"
    "ds_read_b128 v[164:167], v65 offset:8192\n"
    "ds_read_b128 v[168:171], v66 offset:8192\n"
    "ds_read_b128 v[172:175], v67 offset:8192\n"
    "ds_read_b64_tr_b16 v[32:33], v68 offset:43008\n"
    "ds_read_b64_tr_b16 v[34:35], v68 offset:43520\n"
    "s_waitcnt lgkmcnt(12)\n"
    "v_mfma_f32_32x32x16_bf16 v[0:15], v[40:43], v[156:159], v[0:15]\n"
    "ds_read_b64_tr_b16 v[36:37], v69 offset:43008\n"
    "ds_read_b64_tr_b16 v[38:39], v69 offset:43520\n"
    "s_waitcnt lgkmcnt(12)\n"
    "v_mfma_f32_32x32x16_bf16 v[16:31], v[44:47], v[156:159], v[16:31]\n"
    "ds_read_b64_tr_b16 v[40:41], v68 offset:45056\n"
    "ds_read_b64_tr_b16 v[42:43], v68 offset:45568\n"
    "s_waitcnt lgkmcnt(9)\n"
    "v_mfma_f32_32x32x16_bf16 v[0:15], v[48:51], v[160:163], v[0:15]\n"
    "ds_read_b64_tr_b16 v[44:45], v69 offset:45056\n"
    "ds_read_b64_tr_b16 v[46:47], v69 offset:45568\n"
    "ds_read_b32 v76, v75 offset:88\n"
    "v_mfma_f32_32x32x16_bf16 v[16:31], v[52:55], v[160:163], v[16:31]\n"
    "s_waitcnt lgkmcnt(9)\n"
    "ds_read_b64_tr_b16 v[48:49], v68 offset:47104\n"
    "ds_read_b64_tr_b16 v[50:51], v68 offset:47616\n"
    "s_waitcnt lgkmcnt(9)\n"
    "v_mfma_f32_32x32x16_bf16 v[0:15], v[32:35], v[164:167], v[0:15]\n"
    "s_add_u32 m0, s23, 8192\n"
    "s_add_u32 s48, s22, 344064\n"
    "s_mov_b64 exec, s[6:7]\n"
    "buffer_load_dwordx4 v245, s[12:15], s48 offen offset:0 lds\n"
    "s_mov_b64 exec, -1\n"
    "ds_read_b64_tr_b16 v[52:53], v69 offset:47104\n"
    "ds_read_b64_tr_b16 v[54:55], v69 offset:47616\n"
    "s_waitcnt lgkmcnt(9)\n"
    "v_mfma_f32_32x32x16_bf16 v[16:31], v[36:39], v[164:167], v[16:31]\n"
    "s_mov_b64 exec, s[6:7]\n"
    "buffer_load_dwordx4 v246, s[12:15], s48 offen offset:1024 lds\n"
    "s_mov_b64 exec, -1\n"
    "s_waitcnt lgkmcnt(4)\n"
    "v_cmp_gt_u32 vcc, 2, v76\n"
    "s_nop 0\n"
    "s_cbranch_vccnz .Lb_sl25_%=\n"
    ".Lb_ok24_%=:\n"
    "ds_read_b64_tr_b16 v[32:33], v68 offset:49152\n"
    "ds_read_b64_tr_b16 v[34:35], v68 offset:49664\n"
    "v_mfma_f32_32x32x16_bf16 v[0:15], v[40:43], v[168:171], v[0:15]\n"
    "ds_read_b64_tr_b16 v[36:37], v69 offset:49152\n"
    "ds_read_b64_tr_b16 v[38:39], v69 offset:49664\n"
    "v_mfma_f32_32x32x16_bf16 v[16:31], v[44:47], v[168:171], v[16:31]\n"
    "s_waitcnt vmcnt(6)\n"
    "ds_read_b128 v[176:179], v64 offset:0\n"
    "ds_read_b128 v[180:183], v65 offset:0\n"
    "ds_read_b128 v[184:187], v66 offset:0\n"
    "ds_read_b128 v[188:191], v67 offset:0\n"
    "ds_read_b64_tr_b16 v[40:41], v68 offset:51200\n"
    "ds_read_b64_tr_b16 v[42:43], v68 offset:51712\n"
    "s_waitcnt lgkmcnt(12)\n"
    "v_mfma_f32_32x32x16_bf16 v[0:15], v[48:51], v[172:175], v[0:15]\n"
    "ds_read_b64_tr_b16 v[44:45], v69 offset:51200\n"
    "ds_read_b64_tr_b16 v[46:47], v69 offset:51712\n"
    "s_waitcnt lgkmcnt(12)\n"
    "v_mfma_f32_32x32x16_bf16 v[16:31], v[52:55], v[172:175], v[16:31]\n"
    "ds_read_b64_tr_b16 v[48:49], v68 offset:53248\n"
    "ds_read_b64_tr_b16 v[50:51], v68 offset:53760\n"
    "s_waitcnt lgkmcnt(9)\n"
    "v_mfma_f32_32x32x16_bf16 v[0:15], v[32:35], v[176:179], v[0:15]\n"
    "ds_read_b64_tr_b16 v[52:53], v69 offset:53248\n"
    "ds_read_b64_tr_b16 v[54:55], v69 offset:53760\n"
    "v_mfma_f32_32x32x16_bf16 v[16:31], v[36:39], v[176:179], v[16:31]\n"
    "s_waitcnt lgkmcnt(8)\n"
    "ds_read_b64_tr_b16 v[32:33], v68 offset:55296\n"
    "ds_read_b64_tr_b16 v[34:35], v68 offset:55808\n"
    "s_waitcnt lgkmcnt(8)\n"
    "v_mfma_f32_32x32x16_bf16 v[0:15], v[40:43], v[180:183], v[0:15]\n"
    "ds_read_b64_tr_b16 v[36:37], v69 offset:55296\n"
    "ds_read_b64_tr_b16 v[38:39], v69 offset:55808\n"
    "s_waitcnt lgkmcnt(8)\n"
    "v_mfma_f32_32x32x16_bf16 v[16:31], v[44:47], v[180:183], v[16:31]\n"
    "s_waitcnt lgkmcnt(6)\n"
    "v_mfma_f32_32x32x16_bf16 v[0:15], v[48:51], v[184:187], v[0:15]\n"
    "s_waitcnt lgkmcnt(4)\n"
    "v_mfma_f32_32x32x16_bf16 v[16:31], v[52:55], v[184:187], v[16:31]\n"
    "s_waitcnt lgkmcnt(2)\n"
    "v_mfma_f32_32x32x16_bf16 v[0:15], v[32:35], v[188:191], v[0:15]\n"
    "s_waitcnt lgkmcnt(0)\n"
    "v_mfma_f32_32x32x16_bf16 v[16:31], v[36:39], v[188:191], v[16:31]\n"
    "s_branch .Lb_j5_%=\n"
    ".Lb_sl7_%=:\n"
    "s_mov_b32 s4, 4194304\n"
    ".Lb_sp8_%=:\n"
    "ds_read_b32 v76, v75 offset:64\n"
    "s_waitcnt lgkmcnt(0)\n"
    "v_cmp_gt_u32 vcc, 2, v76\n"
    "s_sub_u32 s4, s4, 1\n"
    "s_cbranch_scc1 .Lb_ok6_%=\n"
    "s_cbranch_vccnz .Lb_sp8_%=\n"
    "s_branch .Lb_ok6_%=\n"
    ".Lb_sl10_%=:\n"
    "s_mov_b32 s4, 4194304\n"
    ".Lb_sp11_%=:\n"
    "ds_read_b32 v76, v75 offset:68\n"
    "s_waitcnt lgkmcnt(0)\n"
    "v_cmp_gt_u32 vcc, 2, v76\n"
    "s_sub_u32 s4, s4, 1\n"
    "s_cbranch_scc1 .Lb_ok9_%=\n"
    "s_cbranch_vccnz .Lb_sp11_%=\n"
    "s_branch .Lb_ok9_%=\n"
    ".Lb_sl13_%=:\n"
    "s_mov_b32 s4, 4194304\n"
    ".Lb_sp14_%=:\n"
    "ds_read_b32 v76, v75 offset:72\n"
    "s_waitcnt lgkmcnt(0)\n"
    "v_cmp_gt_u32 vcc, 2, v76\n"
    "s_sub_u32 s4, s4, 1\n"
    "s_cbranch_scc1 .Lb_ok12_%=\n"
    "s_cbranch_vccnz .Lb_sp14_%=\n"
    "s_branch .Lb_ok12_%=\n"
    ".Lb_sl16_%=:\n"
    "s_mov_b32 s4, 4194304\n"
    ".Lb_sp17_%=:\n"
    "ds_read_b32 v76, v75 offset:76\n"
    "s_waitcnt lgkmcnt(0)\n"
    "v_cmp_gt_u32 vcc, 2, v76\n"
    "s_sub_u32 s4, s4, 1\n"
    "s_cbranch_scc1 .Lb_ok15_%=\n"
    "s_cbranch_vccnz .Lb_sp17_%=\n"
    "s_branch .Lb_ok15_%=\n"
    ".Lb_sl19_%=:\n"
    "s_mov_b32 s4, 4194304\n"
    ".Lb_sp20_%=:\n"
    "ds_read_b32 v76, v75 offset:80\n"
    "s_waitcnt lgkmcnt(0)\n"
    "v_cmp_gt_u32 vcc, 2, v76\n"
    "s_sub_u32 s4, s4, 1\n"
    "s_cbranch_scc1 .Lb_ok18_%=\n"
    "s_cbranch_vccnz .Lb_sp20_%=\n"
    "s_branch .Lb_ok18_%=\n"
    ".Lb_sl22_%=:\n"
    "s_mov_b32 s4, 4194304\n"
    ".Lb_sp23_%=:\n"
    "ds_read_b32 v76, v75 offset:84\n"
    "s_waitcnt lgkmcnt(0)\n"
    "v_cmp_gt_u32 vcc, 2, v76\n"
    "s_sub_u32 s4, s4, 1\n"
    "s_cbranch_scc1 .Lb_ok21_%=\n"
    "s_cbranch_vccnz .Lb_sp23_%=\n"
    "s_branch .Lb_ok21_%=\n"
    ".Lb_sl25_%=:\n"
    "s_mov_b32 s4, 4194304\n"
    ".Lb_sp26_%=:\n"
    "ds_read_b32 v76, v75 offset:88\n"
    "s_waitcnt lgkmcnt(0)\n"
    "v_cmp_gt_u32 vcc, 2, v76\n"
    "s_sub_u32 s4, s4, 1\n"
    "s_cbranch_scc1 .Lb_ok24_%=\n"
    "s_cbranch_vccnz .Lb_sp26_%=\n"
    "s_branch .Lb_ok24_%=\n"
    ".Lb_v4_%=:\n"
    "s_waitcnt vmcnt(8)\n"
    "ds_read_b32 v76, v75 offset:64\n"
    "ds_read_b128 v[80:83], v64 offset:0\n"
    "ds_read_b128 v[84:87], v65 offset:0\n"
    "ds_read_b128 v[88:91], v66 offset:0\n"
    "ds_read_b128 v[92:95], v67 offset:0\n"
    "s_waitcnt lgkmcnt(4)\n"
    "v_cmp_gt_u32 vcc, 2, v76\n"
    "s_nop 0\n"
    "s_cbranch_vccnz .Lb_sl28_%=\n"
    ".Lb_ok27_%=:\n"
    "ds_read_b64_tr_b16 v[32:33], v68 offset:0\n"
    "ds_read_b64_tr_b16 v[34:35], v68 offset:512\n"
    "ds_read_b64_tr_b16 v[36:37], v69 offset:0\n"
    "ds_read_b64_tr_b16 v[38:39], v69 offset:512\n"
    "ds_read_b64_tr_b16 v[40:41], v68 offset:2048\n"
    "ds_read_b64_tr_b16 v[42:43], v68 offset:2560\n"
    "ds_read_b64_tr_b16 v[44:45], v69 offset:2048\n"
    "ds_read_b64_tr_b16 v[46:47], v69 offset:2560\n"
    "ds_read_b64_tr_b16 v[48:49], v68 offset:4096\n"
    "ds_read_b64_tr_b16 v[50:51], v68 offset:4608\n"
    "s_waitcnt lgkmcnt(8)\n"
    "v_mfma_f32_32x32x16_bf16 v[0:15], v[80:83], v[32:35], 0\n"
    "ds_read_b64_tr_b16 v[52:53], v69 offset:4096\n"
    "ds_read_b64_tr_b16 v[54:55], v69 offset:4608\n"
    "ds_read_b32 v76, v75 offset:68\n"
    "s_waitcnt lgkmcnt(9)\n"
    "v_mfma_f32_32x32x16_bf16 v[16:31], v[80:83], v[36:39], 0\n"
    "ds_read_b64_tr_b16 v[32:33], v68 offset:6144\n"
    "ds_read_b64_tr_b16 v[34:35], v68 offset:6656\n"
    "s_waitcnt lgkmcnt(9)\n"
    "v_mfma_f32_32x32x16_bf16 v[0:15], v[84:87], v[40:43], v[0:15]\n"
    "s_add_u32 m0, s23, -384\n"
    "s_nop 0\n"
    "buffer_load_dwordx4 v240, s[8:11], s20 offen offset:384 lds\n"
    "ds_read_b64_tr_b16 v[36:37], v69 offset:6144\n"
    "ds_read_b64_tr_b16 v[38:39], v69 offset:6656\n"
    "s_waitcnt lgkmcnt(9)\n"
    "v_mfma_f32_32x32x16_bf16 v[16:31], v[84:87], v[44:47], v[16:31]\n"
    "buffer_load_dwordx4 v241, s[8:11], s20 offen offset:1408 lds\n"
    "s_waitcnt lgkmcnt(4)\n"
    "v_cmp_gt_u32 vcc, 2, v76\n"
    "s_nop 0\n"
    "s_cbranch_vccnz .Lb_sl31_%=\n"
    ".Lb_ok30_%=:\n"
    "ds_read_b64_tr_b16 v[40:41], v68 offset:8192\n"
    "ds_read_b64_tr_b16 v[42:43], v68 offset:8704\n"
    "v_mfma_f32_32x32x16_bf16 v[0:15], v[88:91], v[48:51], v[0:15]\n"
    "buffer_load_dwordx4 v242, s[8:11], s20 offen offset:2432 lds\n"
    "ds_read_b64_tr_b16 v[44:45], v69 offset:8192\n"
    "ds_read_b64_tr_b16 v[46:47], v69 offset:8704\n"
    "v_mfma_f32_32x32x16_bf16 v[16:31], v[88:91], v[52:55], v[16:31]\n"
    "buffer_load_dwordx4 v243, s[8:11], s20 offen offset:3456 lds\n"
    "s_waitcnt vmcnt(8)\n"
    "ds_read_b128 v[96:99], v64 offset:4096\n"
    "ds_read_b128 v[100:103], v65 offset:4096\n"
    "ds_read_b128 v[104:107], v66 offset:4096\n"
    "ds_read_b128 v[108:111], v67 offset:4096\n"
    "ds_read_b64_tr_b16 v[48:49], v68 offset:10240\n"
    "ds_read_b64_tr_b16 v[50:51], v68 offset:10752\n"
    "s_waitcnt lgkmcnt(12)\n"
    "v_mfma_f32_32x32x16_bf16 v[0:15], v[92:95], v[32:35], v[0:15]\n"
    "ds_read_b64_tr_b16 v[52:53], v69 offset:10240\n"
    "ds_read_b64_tr_b16 v[54:55], v69 offset:10752\n"
    "s_waitcnt lgkmcnt(12)\n"
    "v_mfma_f32_32x32x16_bf16 v[16:31], v[92:95], v[36:39], v[16:31]\n"
    "ds_read_b64_tr_b16 v[32:33], v68 offset:12288\n"
    "ds_read_b64_tr_b16 v[34:35], v68 offset:12800\n"
    "s_waitcnt lgkmcnt(9)\n"
    "v_mfma_f32_32x32x16_bf16 v[0:15], v[96:99], v[40:43], v[0:15]\n"
    "ds_read_b64_tr_b16 v[36:37], v69 offset:12288\n"
    "ds_read_b64_tr_b16 v[38:39], v69 offset:12800\n"
    "ds_read_b32 v76, v75 offset:72\n"
    "v_mfma_f32_32x32x16_bf16 v[16:31], v[96:99], v[44:47], v[16:31]\n"
    "s_waitcnt lgkmcnt(9)\n"
    "ds_read_b64_tr_b16 v[40:41], v68 offset:14336\n"
    "ds_read_b64_tr_b16 v[42:43], v68 offset:14848\n"
    "s_waitcnt lgkmcnt(9)\n"
    "v_mfma_f32_32x32x16_bf16 v[0:15], v[100:103], v[48:51], v[0:15]\n"
    "s_add_u32 m0, s23, 3584\n"
    "s_nop 0\n"
    "buffer_load_dwordx4 v240, s[8:11], s20 offen offset:512 lds\n"
    "ds_read_b64_tr_b16 v[44:45], v69 offset:14336\n"
    "ds_read_b64_tr_b16 v[46:47], v69 offset:14848\n"
    "s_waitcnt lgkmcnt(9)\n"
    "v_mfma_f32_32x32x16_bf16 v[16:31], v[100:103], v[52:55], v[16:31]\n"
    "buffer_load_dwordx4 v241, s[8:11], s20 offen offset:1536 lds\n"
    "s_waitcnt lgkmcnt(4)\n"
    "v_cmp_gt_u32 vcc, 2, v76\n"
    "s_nop 0\n"
    "s_cbranch_vccnz .Lb_sl34_%=\n"
    ".Lb_ok33_%=:\n"
    "ds_read_b64_tr_b16 v[48:49], v68 offset:16384\n"
    "ds_read_b64_tr_b16 v[50:51], v68 offset:16896\n"
    "v_mfma_f32_32x32x16_bf16 v[0:15], v[104:107], v[32:35], v[0:15]\n"
    "buffer_load_dwordx4 v242, s[8:11], s20 offen offset:2560 lds\n"
    "ds_read_b64_tr_b16 v[52:53], v69 offset:16384\n"
    "ds_read_b64_tr_b16 v[54:55], v69 offset:16896\n"
    "v_mfma_f32_32x32x16_bf16 v[16:31], v[104:107], v[36:39], v[16:31]\n"
    "buffer_load_dwordx4 v243, s[8:11], s20 offen offset:3584 lds\n"
    "s_waitcnt vmcnt(8)\n"
    "ds_read_b128 v[112:115], v64 offset:8192\n"
    "ds_read_b128 v[116:119], v65 offset:8192\n"
    "ds_read_b128 v[120:123], v66 offset:8192\n"
    "ds_read_b128 v[124:127], v67 offset:8192\n"
    "ds_read_b64_tr_b16 v[32:33], v68 offset:18432\n"
    "ds_read_b64_tr_b16 v[34:35], v68 offset:18944\n"
    "s_waitcnt lgkmcnt(12)\n"
    "v_mfma_f32_32x32x16_bf16 v[0:15], v[108:111], v[40:43], v[0:15]\n"
    "ds_read_b64_tr_b16 v[36:37], v69 offset:18432\n"
    "ds_read_b64_tr_b16 v[38:39], v69 offset:18944\n"
    "s_waitcnt lgkmcnt(12)\n"
    "v_mfma_f32_32x32x16_bf16 v[16:31], v[108:111], v[44:47], v[16:31]\n"
    "ds_read_b64_tr_b16 v[40:41], v68 offset:20480\n"
    "ds_read_b64_tr_b16 v[42:43], v68 offset:20992\n"
    "s_waitcnt lgkmcnt(9)\n"
    "v_mfma_f32_32x32x16_bf16 v[0:15], v[112:115], v[48:51], v[0:15]\n"
    "ds_read_b64_tr_b16 v[44:45], v69 offset:20480\n"
    "ds_read_b64_tr_b16 v[46:47], v69 offset:20992\n"
    "ds_read_b32 v76, v75 offset:76\n"
    "v_mfma_f32_32x32x16_bf16 v[16:31], v[112:115], v[52:55], v[16:31]\n"
    "s_waitcnt lgkmcnt(9)\n"
    "ds_read_b64_tr_b16 v[48:49], v68 offset:22528\n"
    "ds_read_b64_tr_b16 v[50:51], v68 offset:23040\n"
    "s_waitcnt lgkmcnt(9)\n"
    "v_mfma_f32_32x32x16_bf16 v[0:15], v[116:119], v[32:35], v[0:15]\n"
    "s_add_u32 m0, s23, 7552\n"
    "s_nop 0\n"
    "buffer_load_dwordx4 v240, s[8:11], s20 offen offset:640 lds\n"
    "ds_read_b64_tr_b16 v[52:53], v69 offset:22528\n"
    "ds_read_b64_tr_b16 v[54:55], v69 offset:23040\n"
    "s_waitcnt lgkmcnt(9)\n"
    "v_mfma_f32_32x32x16_bf16 v[16:31], v[116:119], v[36:39], v[16:31]\n"
    "buffer_load_dwordx4 v241, s[8:11], s20 offen offset:1664 lds\n"
    "s_waitcnt lgkmcnt(4)\n"
    "v_cmp_gt_u32 vcc, 2, v76\n"
    "s_nop 0\n"
    "s_cbranch_vccnz .Lb_sl37_%=\n"
    ".Lb_ok36_%=:\n"
    "ds_read_b64_tr_b16 v[32:33], v68 offset:24576\n"
    "ds_read_b64_tr_b16 v[34:35], v68 offset:25088\n"
    "v_mfma_f32_32x32x16_bf16 v[0:15], v[120:123], v[40:43], v[0:15]\n"
    "buffer_load_dwordx4 v242, s[8:11], s20 offen offset:2688 lds\n"
    "ds_read_b64_tr_b16 v[36:37], v69 offset:24576\n"
    "ds_read_b64_tr_b16 v[38:39], v69 offset:25088\n"
    "v_mfma_f32_32x32x16_bf16 v[16:31], v[120:123], v[44:47], v[16:31]\n"
    "buffer_load_dwordx4 v243, s[8:11], s20 offen offset:3712 lds\n"
    "s_waitcnt vmcnt(8)\n"
    "ds_read_b128 v[128:131], v64 offset:0\n"
    "ds_read_b128 v[132:135], v65 offset:0\n"
    "ds_read_b128 v[136:139], v66 offset:0\n"
    "ds_read_b128 v[140:143], v67 offset:0\n"
    "ds_read_b64_tr_b16 v[40:41], v68 offset:26624\n"
    "ds_read_b64_tr_b16 v[42:43], v68 offset:27136\n"
    "s_waitcnt lgkmcnt(12)\n"
    "v_mfma_f32_32x32x16_bf16 v[0:15], v[124:127], v[48:51], v[0:15]\n"
    "ds_read_b64_tr_b16 v[44:45], v69 offset:26624\n"
    "ds_read_b64_tr_b16 v[46:47], v69 offset:27136\n"
    "s_waitcnt lgkmcnt(12)\n"
    "v_mfma_f32_32x32x16_bf16 v[16:31], v[124:127], v[52:55], v[16:31]\n"
    "ds_read_b64_tr_b16 v[48:49], v68 offset:28672\n"
    "ds_read_b64_tr_b16 v[50:51], v68 offset:29184\n"
    "s_waitcnt lgkmcnt(9)\n"
    "v_mfma_f32_32x32x16_bf16 v[0:15], v[128:131], v[32:35], v[0:15]\n"
    "ds_read_b64_tr_b16 v[52:53], v69 offset:28672\n"
    "ds_read_b64_tr_b16 v[54:55], v69 offset:29184\n"
    "ds_read_b32 v76, v75 offset:80\n"
    "v_mfma_f32_32x32x16_bf16 v[16:31], v[128:131], v[36:39], v[16:31]\n"
    "s_waitcnt lgkmcnt(9)\n"
    "ds_read_b64_tr_b16 v[32:33], v68 offset:30720\n"
    "ds_read_b64_tr_b16 v[34:35], v68 offset:31232\n"
    "s_waitcnt lgkmcnt(9)\n"
    "v_mfma_f32_32x32x16_bf16 v[0:15], v[132:135], v[40:43], v[0:15]\n"
    "s_add_u32 m0, s23, -768\n"
    "s_nop 0\n"
    "buffer_load_dwordx4 v240, s[8:11], s20 offen offset:768 lds\n"
    "ds_read_b64_tr_b16 v[36:37], v69 offset:30720\n"
    "ds_read_b64_tr_b16 v[38:39], v69 offset:31232\n"
    "s_waitcnt lgkmcnt(9)\n"
    "v_mfma_f32_32x32x16_bf16 v[16:31], v[132:135], v[44:47], v[16:31]\n"
    "buffer_load_dwordx4 v241, s[8:11], s20 offen offset:1792 lds\n"
    "s_waitcnt lgkmcnt(4)\n"
    "v_cmp_gt_u32 vcc, 2, v76\n"
    "s_nop 0\n"
    "s_cbranch_vccnz .Lb_sl40_%=\n"
    ".Lb_ok39_%=:\n"
    "ds_read_b64_tr_b16 v[40:41], v68 offset:32768\n"
    "ds_read_b64_tr_b16 v[42:43], v68 offset:33280\n"
    "v_mfma_f32_32x32x16_bf16 v[0:15], v[136:139], v[48:51], v[0:15]\n"
    "buffer_load_dwordx4 v242, s[8:11], s20 offen offset:2816 lds\n"
    "ds_read_b64_tr_b16 v[44:45], v69 offset:32768\n"
    "ds_read_b64_tr_b16 v[46:47], v69 offset:33280\n"
    "v_mfma_f32_32x32x16_bf16 v[16:31], v[136:139], v[52:55], v[16:31]\n"
    "buffer_load_dwordx4 v243, s[8:11], s20 offen offset:3840 lds\n"
    "s_waitcnt vmcnt(8)\n"
    "ds_read_b128 v[144:147], v64 offset:4096\n"
    "ds_read_b128 v[148:151], v65 offset:4096\n"
    "ds_read_b128 v[152:155], v66 offset:4096\n"
    "ds_read_b128 v[156:159], v67 offset:4096\n"
    "ds_read_b64_tr_b16 v[48:49], v68 offset:34816\n"
    "ds_read_b64_tr_b16 v[50:51], v68 offset:35328\n"
    "s_waitcnt lgkmcnt(12)\n"
    "v_mfma_f32_32x32x16_bf16 v[0:15], v[140:143], v[32:35], v[0:15]\n"
    "ds_read_b64_tr_b16 v[52:53], v69 offset:34816\n"
    "ds_read_b64_tr_b16 v[54:55], v69 offset:35328\n"
    "s_waitcnt lgkmcnt(12)\n"
    "v_mfma_f32_32x32x16_bf16 v[16:31], v[140:143], v[36:39], v[16:31]\n"
    "ds_read_b64_tr_b16 v[32:33], v68 offset:36864\n"
    "ds_read_b64_tr_b16 v[34:35], v68 offset:37376\n"
    "s_waitcnt lgkmcnt(9)\n"
    "v_mfma_f32_32x32x16_bf16 v[0:15], v[144:147], v[40:43], v[0:15]\n"
    "ds_read_b64_tr_b16 v[36:37], v69 offset:36864\n"
    "ds_read_b64_tr_b16 v[38:39], v69 offset:37376\n"
    "ds_read_b32 v76, v75 offset:84\n"
    "v_mfma_f32_32x32x16_bf16 v[16:31], v[144:147], v[44:47], v[16:31]\n"
    "s_waitcnt lgkmcnt(9)\n"
    "ds_read_b64_tr_b16 v[40:41], v68 offset:38912\n"
    "ds_read_b64_tr_b16 v[42:43], v68 offset:39424\n"
    "s_waitcnt lgkmcnt(9)\n"
    "v_mfma_f32_32x32x16_bf16 v[0:15], v[148:151], v[48:51], v[0:15]\n"
    "s_add_u32 m0, s23, 4096\n"
    "s_add_u32 s48, s22, 0\n"
    "buffer_load_dwordx4 v245, s[12:15], s5 offen offset:0 lds\n"
    "ds_read_b64_tr_b16 v[44:45], v69 offset:38912\n"
    "ds_read_b64_tr_b16 v[46:47], v69 offset:39424\n"
    "s_waitcnt lgkmcnt(9)\n"
    "v_mfma_f32_32x32x16_bf16 v[16:31], v[148:151], v[52:55], v[16:31]\n"
    "buffer_load_dwordx4 v246, s[12:15], s5 offen offset:1024 lds\n"
    "s_waitcnt lgkmcnt(4)\n"
    "v_cmp_gt_u32 vcc, 2, v76\n"
    "s_nop 0\n"
    "s_cbranch_vccnz .Lb_sl43_%=\n"
    ".Lb_ok42_%=:\n"
    "ds_read_b64_tr_b16 v[48:49], v68 offset:40960\n"
    "ds_read_b64_tr_b16 v[50:51], v68 offset:41472\n"
    "v_mfma_f32_32x32x16_bf16 v[0:15], v[152:155], v[32:35], v[0:15]\n"
    "s_add_u32 m0, s23, 6144\n"
    "s_add_u32 s48, s22, 172032\n"
    "s_mov_b64 exec, s[6:7]\n"
    "buffer_load_dwordx4 v245, s[12:15], s48 offen offset:0 lds\n"
    "s_mov_b64 exec, -1\n"
    "ds_read_b64_tr_b16 v[52:53], v69 offset:40960\n"
    "ds_read_b64_tr_b16 v[54:55], v69 offset:41472\n"
    "v_mfma_f32_32x32x16_bf16 v[16:31], v[152:155], v[36:39], v[16:31]\n"
    "s_mov_b64 exec, s[6:7]\n"
    "buffer_load_dwordx4 v246, s[12:15], s48 offen offset:1024 lds\n"
    "s_mov_b64 exec, -1\n"
    "s_waitcnt vmcnt(8)\n"
    "ds_read_b128 v[160:163], v64 offset:8192\n"
    "ds_read_b128 v[164:167], v65 offset:8192\n"
    "ds_read_b128 v[168:171], v66 offset:8192\n"
    "ds_read_b128 v[172:175], v67 offset:8192\n"
    "ds_read_b64_tr_b16 v[32:33], v68 offset:43008\n"
    "ds_read_b64_tr_b16 v[34:35], v68 offset:43520\n"
    "s_waitcnt lgkmcnt(12)\n"
    "v_mfma_f32_32x32x16_bf16 v[0:15], v[156:159], v[40:43], v[0:15]\n"
    "ds_read_b64_tr_b16 v[36:37], v69 offset:43008\n"
    "ds_read_b64_tr_b16 v[38:39], v69 offset:43520\n"
    "s_waitcnt lgkmcnt(12)\n"
    "v_mfma_f32_32x32x16_bf16 v[16:31], v[156:159], v[44:47], v[16:31]\n"
    "ds_read_b64_tr_b16 v[40:41], v68 offset:45056\n"
    "ds_read_b64_tr_b16 v[42:43], v68 offset:45568\n"
    "s_waitcnt lgkmcnt(9)\n"
    "v_mfma_f32_32x32x16_bf16 v[0:15], v[160:163], v[48:51], v[0:15]\n"
    "ds_read_b64_tr_b16 v[44:45], v69 offset:45056\n"
    "ds_read_b64_tr_b16 v[46:47], v69 offset:45568\n"
    "ds_read_b32 v76, v75 offset:88\n"
    "v_mfma_f32_32x32x16_bf16 v[16:31], v[160:163], v[52:55], v[16:31]\n"
    "s_waitcnt lgkmcnt(9)\n"
    "ds_read_b64_tr_b16 v[48:49], v68 offset:47104\n"
    "ds_read_b64_tr_b16 v[50:51], v68 offset:47616\n"
    "s_waitcnt lgkmcnt(9)\n"
    "v_mfma_f32_32x32x16_bf16 v[0:15], v[164:167], v[32:35], v[0:15]\n"
    "s_add_u32 m0, s23, 8192\n"
    "s_add_u32 s48, s22, 344064\n"
    "s_mov_b64 exec, s[6:7]\n"
    "buffer_load_dwordx4 v245, s[12:15], s48 offen offset:0 lds\n"
    "s_mov_b64 exec, -1\n"
    "ds_read_b64_tr_b16 v[52:53], v69 offset:47104\n"
    "ds_read_b64_tr_b16 v[54:55], v69 offset:47616\n"
    "s_waitcnt lgkmcnt(9)\n"
    "v_mfma_f32_32x32x16_bf16 v[16:31], v[164:167], v[36:39], v[16:31]\n"
    "s_mov_b64 exec, s[6:7]\n"
    "buffer_load_dwordx4 v246, s[12:15], s48 offen offset:1024 lds\n"
    "s_mov_b64 exec, -1\n"
    "s_waitcnt lgkmcnt(4)\n"
    "v_cmp_gt_u32 vcc, 2, v76\n"
    "s_nop 0\n"
    "s_cbranch_vccnz .Lb_sl46_%=\n"
    ".Lb_ok45_%=:\n"
    "ds_read_b64_tr_b16 v[32:33], v68 offset:49152\n"
    "ds_read_b64_tr_b16 v[34:35], v68 offset:49664\n"
    "v_mfma_f32_32x32x16_bf16 v[0:15], v[168:171], v[40:43], v[0:15]\n"
    "ds_read_b64_tr_b16 v[36:37], v69 offset:49152\n"
    "ds_read_b64_tr_b16 v[38:39], v69 offset:49664\n"
    "v_mfma_f32_32x32x16_bf16 v[16:31], v[168:171], v[44:47], v[16:31]\n"
    "s_waitcnt vmcnt(6)\n"
    "ds_read_b128 v[176:179], v64 offset:0\n"
    "ds_read_b128 v[180:183], v65 offset:0\n"
    "ds_read_b128 v[184:187], v66 offset:0\n"
    "ds_read_b128 v[188:191], v67 offset:0\n"
    "ds_read_b64_tr_b16 v[40:41], v68 offset:51200\n"
    "ds_read_b64_tr_b16 v[42:43], v68 offset:51712\n"
    "s_waitcnt lgkmcnt(12)\n"
    "v_mfma_f32_32x32x16_bf16 v[0:15], v[172:175], v[48:51], v[0:15]\n"
    "ds_read_b64_tr_b16 v[44:45], v69 offset:51200\n"
    "ds_read_b64_tr_b16 v[46:47], v69 offset:51712\n"
    "s_waitcnt lgkmcnt(12)\n"
    "v_mfma_f32_32x32x16_bf16 v[16:31], v[172:175], v[52:55], v[16:31]\n"
    "ds_read_b64_tr_b16 v[48:49], v68 offset:53248\n"
    "ds_read_b64_tr_b16 v[50:51], v68 offset:53760\n"
    "s_waitcnt lgkmcnt(9)\n"
    "v_mfma_f32_32x32x16_bf16 v[0:15], v[176:179], v[32:35], v[0:15]\n"
    "ds_read_b64_tr_b16 v[52:53], v69 offset:53248\n"
    "ds_read_b64_tr_b16 v[54:55], v69 offset:53760\n"
    "v_mfma_f32_32x32x16_bf16 v[16:31], v[176:179], v[36:39], v[16:31]\n"
    "s_waitcnt lgkmcnt(8)\n"
    "ds_read_b64_tr_b16 v[32:33], v68 offset:55296\n"
    "ds_read_b64_tr_b16 v[34:35], v68 offset:55808\n"
    "s_waitcnt lgkmcnt(8)\n"
    "v_mfma_f32_32x32x16_bf16 v[0:15], v[180:183], v[40:43], v[0:15]\n"
    "ds_read_b64_tr_b16 v[36:37], v69 offset:55296\n"
    "ds_read_b64_tr_b16 v[38:39], v69 offset:55808\n"
    "s_waitcnt lgkmcnt(8)\n"
    "v_mfma_f32_32x32x16_bf16 v[16:31], v[180:183], v[44:47], v[16:31]\n"
    "s_waitcnt lgkmcnt(6)\n"
    "v_mfma_f32_32x32x16_bf16 v[0:15], v[184:187], v[48:51], v[0:15]\n"
    "s_waitcnt lgkmcnt(4)\n"
    "v_mfma_f32_32x32x16_bf16 v[16:31], v[184:187], v[52:55], v[16:31]\n"
    "s_waitcnt lgkmcnt(2)\n"
    "v_mfma_f32_32x32x16_bf16 v[0:15], v[188:191], v[32:35], v[0:15]\n"
    "s_waitcnt lgkmcnt(0)\n"
    "v_mfma_f32_32x32x16_bf16 v[16:31], v[188:191], v[36:39], v[16:31]\n"
    ".Lb_j5_%=:\n"
    "s_mov_b64 exec, 1\n"
    "ds_add_u32 v75, v78 offset:108\n"
    "s_mov_b64 exec, -1\n"
    "ds_read_b32 v77, v75 offset:112\n"
    "s_nop 6\n"
    "v_cvt_pk_bf16_f32 v48, v0, v1\n"
    "v_cvt_pk_bf16_f32 v49, v2, v3\n"
    "v_cvt_pk_bf16_f32 v50, v4, v5\n"
    "v_cvt_pk_bf16_f32 v51, v6, v7\n"
    "v_cvt_pk_bf16_f32 v52, v8, v9\n"
    "v_cvt_pk_bf16_f32 v53, v10, v11\n"
    "v_cvt_pk_bf16_f32 v54, v12, v13\n"
    "v_cvt_pk_bf16_f32 v55, v14, v15\n"
    "v_cvt_pk_bf16_f32 v56, v16, v17\n"
    "v_cvt_pk_bf16_f32 v57, v18, v19\n"
    "v_cvt_pk_bf16_f32 v58, v20, v21\n"
    "v_cvt_pk_bf16_f32 v59, v22, v23\n"
    "v_cvt_pk_bf16_f32 v60, v24, v25\n"
    "v_cvt_pk_bf16_f32 v61, v26, v27\n"
    "v_cvt_pk_bf16_f32 v62, v28, v29\n"
    "v_cvt_pk_bf16_f32 v63, v30, v31\n"
    "s_waitcnt lgkmcnt(0)\n"
    "s_cmp_gt_u32 s3, 1\n"
    "s_cbranch_scc1 .Lb_q48_%=\n"
    "buffer_store_dwordx4 v[48:51], v247, s[16:19], s26 offen sc1\n"
    "buffer_store_dwordx4 v[52:55], v247, s[16:19], s37 offen sc1\n"
    "buffer_store_dwordx4 v[56:59], v247, s[16:19], s47 offen sc1\n"
    "buffer_store_dwordx4 v[60:63], v247, s[16:19], s99 offen sc1\n"
    "s_waitcnt vmcnt(4)\n"
    "s_mov_b64 exec, 1\n"
    "ds_add_u32 v75, v78 offset:92\n"
    "s_mov_b64 exec, -1\n"
    "s_mul_i32 s48, s31, 448\n"
    "s_add_u32 s48, s48, 1835008\n"
    "v_add_u32 v72, s48, v72\n"
    "s_lshl_b32 s48, s36, 9\n"
    "v_add_u32 v71, s48, v74\n"
    "s_lshl_b32 s48, s2, 7\n"
    "s_add_u32 s27, s48, 8\n"
    "s_mul_i32 s48, s3, 114688\n"
    "s_add_u32 s28, s48, 65536\n"
    "s_add_u32 s29, s48, 79872\n"
    "s_bitcmp1_b32 s31, 0\n"
    "s_cselect_b32 s40, -1, 0\n"
    "s_cselect_b32 s43, 0, -1\n"
    "s_mov_b32 s41, 0\n"
    "s_mov_b32 s42, 0\n"
    "s_mov_b32 s44, 0x0fffffff\n"
    "s_mov_b32 s45, 0x0fffffff\n"
    "s_mov_b32 s46, 0x3e8b97f6\n"
    "s_lshr_b32 s52, s36, 3\n"
    "s_add_u32 s48, s36, 3\n"
    "s_lshr_b32 s48, s48, 3\n"
    "s_lshl4_add_u32 s52, s48, s52\n"
    "s_mul_i32 s49, s31, 28\n"
    "s_lshr_b32 s48, s49, 6\n"
    "s_lshl_b32 s48, s48, 8\n"
    "s_add_u32 s52, s52, s48\n"
    "s_add_u32 s48, s49, 27\n"
    "s_lshr_b32 s48, s48, 6\n"
    "s_lshl_b32 s48, s48, 12\n"
    "s_add_u32 s52, s52, s48\n"
    "s_xor_b32 s52, s52, 0x7777\n"
    "v_and_b32 v192, 3, v79\n"
    "v_lshlrev_b32 v192, 2, v192\n"
    "v_bfe_u32 v192, s52, v192, 3\n"
    "v_bfe_u32 v193, v79, 1, 1\n"
    "v_lshl_or_b32 v192, v192, 3, v193\n"
    "v_lshrrev_b32 v193, 4, v79\n"
    "v_lshl_or_b32 v192, v193, 1, v192\n"
    "v_lshlrev_b32 v192, 7, v192\n"
    "s_lshl_b32 s48, s3, 13\n"
    "s_add_u32 s48, s48, 8\n"
    "v_add_u32 v73, s48, v192\n"
    "s_add_u32 s48, s3, 1\n"
    "s_and_b32 s48, s48, 3\n"
    "s_mul_i32 s48, s48, 12288\n"
    "s_add_u32 s94, s48, 53504\n"
    "s_add_u32 s48, s3, 2\n"
    "s_and_b32 s48, s48, 3\n"
    "s_mul_i32 s48, s48, 12288\n"
    "s_add_u32 s95, s48, 53504\n"
    "s_add_u32 s48, s3, 3\n"
    "s_and_b32 s48, s48, 3\n"
    "s_mul_i32 s48, s48, 12288\n"
    "s_add_u32 s96, s48, 53504\n"
    "s_add_u32 s48, s3, 4\n"
    "s_and_b32 s48, s48, 3\n"
    "s_mul_i32 s48, s48, 12288\n"
    "s_add_u32 s97, s48, 53504\n"
    "s_lshl_b32 s48, s33, 15\n"
    "s_lshl_b32 s49, s31, 11\n"
    "s_add_u32 s48, s48, s49\n"
    "s_add_u32 s34, s48, 3735552\n"
    "s_andn2_b32 s48, s33, 3\n"
    "s_lshl1_add_u32 s48, s25, s48\n"
    "s_lshr_b32 s49, s32, 2\n"
    "s_add_u32 s48, s48, s49\n"
    "s_lshl2_add_u32 s48, s48, s3\n"
    "s_lshl_b32 s48, s48, 13\n"
    "s_add_u32 s35, s48, 3735552\n"
    "s_load_dwordx2 s[88:89], s[0:1], 0x0\n"
    "s_load_dwordx2 s[90:91], s[0:1], 0x40\n"
    "s_load_dwordx2 s[92:93], s[0:1], 0x60\n"
    "s_lshl_b32 s48, s3, 13\n"
    "s_add_u32 s98, s48, 53504\n"
    "s_mov_b32 s48, 0x3f803f80\n"
    "s_mov_b32 s49, 0x3f803f80\n"
    "v_mov_b64 v[200:201], s[48:49]\n"
    "v_mov_b64 v[202:203], s[48:49]\n"
    "v_mov_b64 v[204:205], s[48:49]\n"
    "v_mov_b64 v[206:207], s[48:49]\n"
    "v_mov_b64 v[216:217], s[48:49]\n"
    "v_mov_b64 v[218:219], s[48:49]\n"
    "v_mov_b64 v[220:221], s[48:49]\n"
    "v_mov_b64 v[222:223], s[48:49]\n"
    "v_mov_b64 v[232:233], s[48:49]\n"
    "v_mov_b64 v[234:235], s[48:49]\n"
    "v_mov_b64 v[236:237], s[48:49]\n"
    "v_mov_b64 v[238:239], s[48:49]\n"
    "v_mov_b64 v[248:249], s[48:49]\n"
    "v_mov_b64 v[250:251], s[48:49]\n"
    "v_mov_b64 v[252:253], s[48:49]\n"
    "v_mov_b64 v[254:255], s[48:49]\n"
    "v_mov_b32 v247, 2.0\n"
    "v_cmp_gt_u32 vcc, 1, v77\n"
    "s_nop 0\n"
    "s_cbranch_vccnz .Lb_sl52_%=\n"
    ".Lb_ok51_%=:\n"
    "s_waitcnt vmcnt(0)\n"
    "s_mov_b64 exec, 1\n"
    "ds_add_rtn_u32 v76, v75, v78 offset:164\n"
    "s_waitcnt lgkmcnt(0)\n"
    "v_cmp_eq_u32 vcc, 1, v76\n"
    "s_nop 1\n"
    "v_cndmask_b32 v76, v247, v75, vcc\n"
    "buffer_store_dword v78, v76, s[16:19], s27 offen sc1\n"
    "s_mov_b64 exec, -1\n"
    "s_add_u32 m0, s23, 10240\n"
    "s_add_u32 s48, s22, 516096\n"
    "buffer_load_dwordx4 v245, s[12:15], s48 offen offset:0 lds\n"
    "buffer_load_dwordx4 v246, s[12:15], s48 offen offset:1024 lds\n"
    "s_add_u32 m0, s23, 0\n"
    "s_add_u32 s48, s22, 688128\n"
    "buffer_load_dwordx4 v245, s[12:15], s48 offen offset:0 lds\n"
    "buffer_load_dwordx4 v246, s[12:15], s48 offen offset:1024 lds\n"
    "s_add_u32 m0, s23, 2048\n"
    "s_add_u32 s48, s22, 860160\n"
    "buffer_load_dwordx4 v245, s[12:15], s48 offen offset:0 lds\n"
    "buffer_load_dwordx4 v246, s[12:15], s48 offen offset:1024 lds\n"
    "s_branch .Lb_q49_%=\n"
    ".Lb_q48_%=:\n"
    "s_waitcnt vmcnt(0)\n"
    "s_mov_b64 exec, 1\n"
    "ds_add_u32 v75, v78 offset:92\n"
    "s_mov_b64 exec, -1\n"
    "buffer_store_dwordx4 v[48:51], v247, s[16:19], s26 offen sc1\n"
    "buffer_store_dwordx4 v[52:55], v247, s[16:19], s37 offen sc1\n"
    "buffer_store_dwordx4 v[56:59], v247, s[16:19], s47 offen sc1\n"
    "buffer_store_dwordx4 v[60:63], v247, s[16:19], s99 offen sc1\n"
    ".Lb_q49_%=:\n"
    "s_cmp_lt_u32 s94, 78080\n"
    "s_cselect_b32 s49, 30720, 0\n"
    "s_add_u32 s49, s49, s94\n"
    "v_add_u32 v64, s49, v74\n"
    "s_cmp_lt_u32 s95, 78080\n"
    "s_cselect_b32 s49, 30720, 0\n"
    "s_add_u32 s49, s49, s95\n"
    "v_add_u32 v65, s49, v74\n"
    "s_cmp_lt_u32 s96, 78080\n"
    "s_cselect_b32 s49, 30720, 0\n"
    "s_add_u32 s49, s49, s96\n"
    "v_add_u32 v66, s49, v74\n"
    "s_cmp_lt_u32 s97, 78080\n"
    "s_cselect_b32 s49, 30720, 0\n"
    "s_add_u32 s49, s49, s97\n"
    "v_add_u32 v67, s49, v74\n"
    "v_add_u32 v68, s98, v74\n"
    "v_mov_b64 v[16:17], 0\n"
    "v_mov_b64 v[18:19], 0\n"
    "v_mov_b64 v[20:21], 0\n"
    "v_mov_b64 v[22:23], 0\n"
    "v_mov_b64 v[24:25], 0\n"
    "v_mov_b64 v[26:27], 0\n"
    "v_mov_b64 v[28:29], 0\n"
    "v_mov_b64 v[30:31], 0\n"
    "s_mov_b32 s5, s27\n"
    "v_mov_b32 v58, 4\n"
    "v_cmp_gt_u32 vcc, 1, v77\n"
    "s_nop 0\n"
    "s_cbranch_vccnz .Lb_sl55_%=\n"
    ".Lb_ok54_%=:\n"
    "ds_read_b32 v76, v75 offset:92\n"
    "s_waitcnt lgkmcnt(0)\n"
    "v_cmp_gt_u32 vcc, 4, v76\n"
    "s_nop 0\n"
    "s_cbranch_vccnz .Lb_sl58_%=\n"
    ".Lb_ok57_%=:\n"
    "ds_read_b64_tr_b16 v[32:33], v70 offset:28672\n"
    "ds_read_b64_tr_b16 v[34:35], v70 offset:28928\n"
    "ds_read_b64_tr_b16 v[36:37], v70 offset:29696\n"
    "ds_read_b64_tr_b16 v[38:39], v70 offset:29952\n"
    "ds_read_b64_tr_b16 v[40:41], v70 offset:40960\n"
    "ds_read_b64_tr_b16 v[42:43], v70 offset:41216\n"
    "ds_read_b64_tr_b16 v[44:45], v70 offset:41984\n"
    "ds_read_b64_tr_b16 v[46:47], v70 offset:42240\n"
    "s_waitcnt lgkmcnt(6)\n"
    "v_mfma_f32_32x32x16_bf16 v[0:15], v[32:35], v[176:179], 0\n"
    "ds_read_b64_tr_b16 v[32:33], v70 offset:4096\n"
    "ds_read_b64_tr_b16 v[34:35], v70 offset:4352\n"
    "s_waitcnt lgkmcnt(6)\n"
    "v_mfma_f32_32x32x16_bf16 v[0:15], v[36:39], v[180:183], v[0:15]\n"
    "ds_read_b64_tr_b16 v[36:37], v70 offset:5120\n"
    "ds_read_b64_tr_b16 v[38:39], v70 offset:5376\n"
    "s_waitcnt lgkmcnt(6)\n"
    "v_mfma_f32_32x32x16_bf16 v[0:15], v[40:43], v[184:187], v[0:15]\n"
    "ds_read_b64_tr_b16 v[40:41], v70 offset:16384\n"
    "ds_read_b64_tr_b16 v[42:43], v70 offset:16640\n"
    "s_waitcnt lgkmcnt(6)\n"
    "v_mfma_f32_32x32x16_bf16 v[0:15], v[44:47], v[188:191], v[0:15]\n"
    "ds_read_b64_tr_b16 v[44:45], v70 offset:17408\n"
    "ds_read_b64_tr_b16 v[46:47], v70 offset:17664\n"
    "s_waitcnt lgkmcnt(6)\n"
    "v_mfma_f32_32x32x16_bf16 v[0:15], v[32:35], v[80:83], v[0:15]\n"
    "ds_read_b64_tr_b16 v[32:33], v70 offset:6144\n"
    "ds_read_b64_tr_b16 v[34:35], v70 offset:6400\n"
    "s_waitcnt lgkmcnt(6)\n"
    "v_mfma_f32_32x32x16_bf16 v[0:15], v[36:39], v[84:87], v[0:15]\n"
    "ds_read_b64_tr_b16 v[36:37], v70 offset:7168\n"
    "ds_read_b64_tr_b16 v[38:39], v70 offset:7424\n"
    "s_waitcnt lgkmcnt(6)\n"
    "v_mfma_f32_32x32x16_bf16 v[0:15], v[40:43], v[88:91], v[0:15]\n"
    "ds_read_b64_tr_b16 v[40:41], v70 offset:18432\n"
    "ds_read_b64_tr_b16 v[42:43], v70 offset:18688\n"
    "s_waitcnt lgkmcnt(6)\n"
    "v_mfma_f32_32x32x16_bf16 v[0:15], v[44:47], v[92:95], v[0:15]\n"
    "ds_read_b64_tr_b16 v[44:45], v70 offset:19456\n"
    "ds_read_b64_tr_b16 v[46:47], v70 offset:19712\n"
    "s_waitcnt lgkmcnt(6)\n"
    "v_mfma_f32_32x32x16_bf16 v[0:15], v[32:35], v[96:99], v[0:15]\n"
    "s_cmp_gt_u32 s3, 1\n"
    "s_cbranch_scc1 .Lb_sk60_%=\n"
    "s_waitcnt vmcnt(0)\n"
    "s_mov_b64 exec, 1\n"
    "ds_add_u32 v75, v78 offset:96\n"
    "s_mov_b64 exec, -1\n"
    ".Lb_sk60_%=:\n"
    "ds_read_b64_tr_b16 v[32:33], v70 offset:8192\n"
    "ds_read_b64_tr_b16 v[34:35], v70 offset:8448\n"
    "s_waitcnt lgkmcnt(6)\n"
    "v_mfma_f32_32x32x16_bf16 v[0:15], v[36:39], v[100:103], v[0:15]\n"
    "ds_read_b64_tr_b16 v[36:37], v70 offset:9216\n"
    "ds_read_b64_tr_b16 v[38:39], v70 offset:9472\n"
    "s_waitcnt lgkmcnt(6)\n"
    "v_mfma_f32_32x32x16_bf16 v[0:15], v[40:43], v[104:107], v[0:15]\n"
    "ds_read_b64_tr_b16 v[40:41], v70 offset:20480\n"
    "ds_read_b64_tr_b16 v[42:43], v70 offset:20736\n"
    "s_waitcnt lgkmcnt(6)\n"
    "v_mfma_f32_32x32x16_bf16 v[0:15], v[44:47], v[108:111], v[0:15]\n"
    "ds_read_b32 v76, v75 offset:96\n"
    "ds_read_b64_tr_b16 v[44:45], v70 offset:21504\n"
    "ds_read_b64_tr_b16 v[46:47], v70 offset:21760\n"
    "s_waitcnt lgkmcnt(7)\n"
    "v_mfma_f32_32x32x16_bf16 v[0:15], v[32:35], v[112:115], v[0:15]\n"
    "s_waitcnt lgkmcnt(2)\n"
    "v_cmp_gt_u32 vcc, 2, v76\n"
    "s_nop 0\n"
    "s_cbranch_vccnz .Lb_sl62_%=\n"
    ".Lb_ok61_%=:\n"
    "ds_read_b64_tr_b16 v[32:33], v70 offset:10240\n"
    "ds_read_b64_tr_b16 v[34:35], v70 offset:10496\n"
    "v_mfma_f32_32x32x16_bf16 v[0:15], v[36:39], v[116:119], v[0:15]\n"
    "ds_read_b64_tr_b16 v[36:37], v70 offset:11264\n"
    "ds_read_b64_tr_b16 v[38:39], v70 offset:11520\n"
    "v_mfma_f32_32x32x16_bf16 v[0:15], v[40:43], v[120:123], v[0:15]\n"
    "ds_read_b64_tr_b16 v[40:41], v70 offset:22528\n"
    "ds_read_b64_tr_b16 v[42:43], v70 offset:22784\n"
    "s_waitcnt lgkmcnt(6)\n"
    "v_mfma_f32_32x32x16_bf16 v[0:15], v[44:47], v[124:127], v[0:15]\n"
    "ds_read_b64_tr_b16 v[44:45], v70 offset:23552\n"
    "ds_read_b64_tr_b16 v[46:47], v70 offset:23808\n"
    "s_waitcnt lgkmcnt(6)\n"
    "v_mfma_f32_32x32x16_bf16 v[0:15], v[32:35], v[128:131], v[0:15]\n"
    "s_waitcnt vmcnt(0)\n"
    "s_mov_b64 exec, 1\n"
    "ds_add_rtn_u32 v57, v75, v78 offset:168\n"
    "s_mov_b64 exec, -1\n"
    "s_and_b32 s24, s2, 7\n"
    "s_lshr_b32 s49, s2, 3\n"
    "s_min_u32 s54, s24, 6\n"
    "s_lshr_b32 s50, s49, 3\n"
    "s_lshl_b32 s50, s50, 9\n"
    "ds_read_b64_tr_b16 v[32:33], v70 offset:0\n"
    "ds_read_b64_tr_b16 v[34:35], v70 offset:256\n"
    "s_waitcnt lgkmcnt(7)\n"
    "v_mfma_f32_32x32x16_bf16 v[0:15], v[36:39], v[132:135], v[0:15]\n"
    "s_and_b32 s51, s49, 6\n"
    "s_lshl2_add_u32 s50, s51, s50\n"
    "s_and_b32 s51, s49, 1\n"
    "s_add_u32 s50, s50, s51\n"
    "s_lshl_b32 s51, s3, 6\n"
    "ds_read_b64_tr_b16 v[36:37], v70 offset:1024\n"
    "ds_read_b64_tr_b16 v[38:39], v70 offset:1280\n"
    "s_waitcnt lgkmcnt(7)\n"
    "v_mfma_f32_32x32x16_bf16 v[0:15], v[40:43], v[136:139], v[0:15]\n"
    "s_mov_b32 exec_hi, 0\n"
    "buffer_load_dword v77, v73, s[16:19], 0 offen offset:0 sc1\n"
    "s_mov_b32 exec_hi, -1\n"
    "s_mov_b64 exec, 1\n"
    "s_waitcnt lgkmcnt(4)\n"
    "v_cmp_eq_u32 vcc, 3, v57\n"
    "s_nop 1\n"
    "v_cndmask_b32 v57, 2.0, v58, vcc\n"
    "buffer_store_dword v78, v57, s[16:19], s5 offen sc1\n"
    "s_mov_b64 exec, -1\n"
    "s_add_u32 s55, s50, s51\n"
    "s_lshl_b32 s54, s54, 7\n"
    "s_lshl2_add_u32 s50, s24, s3\n"
    "s_mul_i32 s50, s50, 67584\n"
    "s_add_u32 s50, s50, 7725056\n"
    "ds_read_b64_tr_b16 v[40:41], v70 offset:12288\n"
    "ds_read_b64_tr_b16 v[42:43], v70 offset:12544\n"
    "v_mfma_f32_32x32x16_bf16 v[0:15], v[44:47], v[140:143], v[0:15]\n"
    "s_lshl_b32 s51, s49, 7\n"
    "s_add_u32 s30, s50, s51\n"
    "s_add_u32 s20, s30, 33792\n"
    "s_or_b32 s51, s51, 0x380\n"
    "s_add_u32 s21, s50, s51\n"
    "ds_read_b64_tr_b16 v[44:45], v70 offset:13312\n"
    "ds_read_b64_tr_b16 v[46:47], v70 offset:13568\n"
    "s_waitcnt lgkmcnt(6)\n"
    "v_mfma_f32_32x32x16_bf16 v[0:15], v[32:35], v[144:147], v[0:15]\n"
    "s_add_u32 s22, s21, 33792\n"
    "s_lshl_b32 s51, s3, 1\n"
    "s_lshl_b32 s50, s3, 11\n"
    "s_add_u32 s50, s50, 18688\n"
    "s_add_u32 s27, s35, 4096\n"
    "ds_read_b64_tr_b16 v[32:33], v70 offset:2048\n"
    "ds_read_b64_tr_b16 v[34:35], v70 offset:2304\n"
    "s_waitcnt lgkmcnt(6)\n"
    "v_mfma_f32_32x32x16_bf16 v[0:15], v[36:39], v[148:151], v[0:15]\n"
    "v_and_b32 v96, 32, v79\n"
    "v_bfe_u32 v97, v79, 3, 2\n"
    "v_lshlrev_b32 v96, 3, v96\n"
    "v_lshl_add_u32 v96, v97, 1, v96\n"
    "v_add_u32 v96, s55, v96\n"
    "ds_read_b64_tr_b16 v[36:37], v70 offset:3072\n"
    "ds_read_b64_tr_b16 v[38:39], v70 offset:3328\n"
    "s_waitcnt lgkmcnt(6)\n"
    "v_mfma_f32_32x32x16_bf16 v[0:15], v[40:43], v[152:155], v[0:15]\n"
    "v_and_b32 v98, 7, v79\n"
    "v_lshl_add_u32 v119, v98, 4, s54\n"
    "v_mul_u32_u24 v100, 896, v96\n"
    "v_add_u32 v120, v100, v119\n"
    "v_add_u32 v121, 28672, v120\n"
    "ds_read_b64_tr_b16 v[40:41], v70 offset:14336\n"
    "ds_read_b64_tr_b16 v[42:43], v70 offset:14592\n"
    "s_waitcnt lgkmcnt(6)\n"
    "v_mfma_f32_32x32x16_bf16 v[0:15], v[44:47], v[156:159], v[0:15]\n"
    "v_add_u32 v112, v100, v120\n"
    "v_add_u32 v113, 57344, v112\n"
    "v_lshrrev_b32 v100, 3, v79\n"
    "v_mul_u32_u24 v102, 4224, v100\n"
    "v_lshl_or_b32 v114, v98, 4, v102\n"
    "ds_read_b64_tr_b16 v[44:45], v70 offset:15360\n"
    "ds_read_b64_tr_b16 v[46:47], v70 offset:15616\n"
    "s_waitcnt lgkmcnt(6)\n"
    "v_mfma_f32_32x32x16_bf16 v[0:15], v[32:35], v[160:163], v[0:15]\n"
    "v_xor_b32 v101, s51, v98\n"
    "v_lshlrev_b32 v101, 4, v101\n"
    "v_lshl_or_b32 v102, v100, 7, v101\n"
    "v_add_u32 v115, s50, v102\n"
    "v_xor_b32 v116, 16, v115\n"
    "s_waitcnt lgkmcnt(4)\n"
    "v_mfma_f32_32x32x16_bf16 v[0:15], v[36:39], v[164:167], v[0:15]\n"
    "v_lshl_or_b32 v101, v100, 1, v101\n"
    "v_lshl_or_b32 v101, v98, 10, v101\n"
    "v_add_u32 v117, 18688, v101\n"
    "v_xor_b32 v118, 16, v117\n"
    "s_waitcnt lgkmcnt(2)\n"
    "v_mfma_f32_32x32x16_bf16 v[0:15], v[40:43], v[168:171], v[0:15]\n"
    "s_mov_b32 exec_hi, 0\n"
    "s_waitcnt vmcnt(1)\n"
    "v_cmp_ne_u32 vcc, 1, v77\n"
    "s_nop 0\n"
    "s_cbranch_vccnz .Lb_sl65_%=\n"
    ".Lb_ok64_%=:\n"
    "s_mov_b32 exec_hi, -1\n"
    "buffer_load_dwordx4 v[192:195], v71, s[16:19], s28 offen offset:0 sc1\n"
    "buffer_load_dwordx4 v[196:199], v71, s[16:19], s28 offen offset:1024 sc1\n"
    "s_mov_b64 exec, s[44:45]\n"
    "buffer_load_dwordx4 v[200:203], v72, s[16:19], s28 offen sc1\n"
    "buffer_load_dwordx4 v[204:207], v72, s[16:19], s29 offen sc1\n"
    "s_mov_b64 exec, -1\n"
    "s_add_u32 s28, s28, 28672\n"
    "s_add_u32 s29, s29, 28672\n"
    "s_waitcnt lgkmcnt(0)\n"
    "v_mfma_f32_32x32x16_bf16 v[0:15], v[44:47], v[172:175], v[0:15]\n"
    "buffer_load_dwordx4 v[208:211], v71, s[16:19], s28 offen offset:0 sc1\n"
    "buffer_load_dwordx4 v[212:215], v71, s[16:19], s28 offen offset:1024 sc1\n"
    "s_mov_b64 exec, s[44:45]\n"
    "buffer_load_dwordx4 v[216:219], v72, s[16:19], s28 offen sc1\n"
    "buffer_load_dwordx4 v[220:223], v72, s[16:19], s29 offen sc1\n"
    "s_mov_b64 exec, -1\n"
    "s_add_u32 s28, s28, 430080\n"
    "s_add_u32 s29, s29, 430080\n"
    "v_mov_b64 v[32:33], 0\n"
    "v_mov_b64 v[34:35], 0\n"
    "v_mov_b64 v[36:37], 0\n"
    "v_mov_b64 v[38:39], 0\n"
    "v_mov_b64 v[40:41], 0\n"
    "v_mov_b64 v[42:43], 0\n"
    "v_mov_b64 v[44:45], 0\n"
    "v_mov_b64 v[46:47], 0\n"
    "v_mul_f32 v0, s46, v0\n"
    "v_mul_f32 v1, s46, v1\n"
    "v_mul_f32 v2, s46, v2\n"
    "v_mul_f32 v3, s46, v3\n"
    "v_mul_f32 v4, s46, v4\n"
    "v_mul_f32 v5, s46, v5\n"
    "v_mul_f32 v6, s46, v6\n"
    "v_mul_f32 v7, s46, v7\n"
    "v_mul_f32 v8, s46, v8\n"
    "v_mul_f32 v9, s46, v9\n"
    "v_mul_f32 v10, s46, v10\n"
    "v_mul_f32 v11, s46, v11\n"
    "v_mul_f32 v12, s46, v12\n"
    "v_mul_f32 v13, s46, v13\n"
    "v_mul_f32 v14, s46, v14\n"
    "v_mul_f32 v15, s46, v15\n"
    "v_cvt_pk_bf16_f32 v128, v0, v1\n"
    "v_cvt_pk_bf16_f32 v129, v2, v3\n"
    "v_cvt_pk_bf16_f32 v130, v4, v5\n"
    "v_cvt_pk_bf16_f32 v131, v6, v7\n"
    "v_cvt_pk_bf16_f32 v132, v8, v9\n"
    "v_cvt_pk_bf16_f32 v133, v10, v11\n"
    "v_cvt_pk_bf16_f32 v134, v12, v13\n"
    "v_cvt_pk_bf16_f32 v135, v14, v15\n"
    "s_mov_b64 exec, s[40:41]\n"
    "v_mov_b64 v[128:129], 0\n"
    "s_mov_b64 exec, s[42:43]\n"
    "v_mov_b64 v[134:135], 0\n"
    "s_mov_b64 exec, -1\n"
    "ds_write_b128 v67, v[128:131]\n"
    "ds_write_b128 v67, v[132:135] offset:1024\n"
    "s_mov_b64 exec, 1\n"
    "ds_add_u32 v75, v78 offset:104\n"
    "s_mov_b64 exec, -1\n"
    "v_mov_b64 v[0:1], 0\n"
    "v_mov_b64 v[2:3], 0\n"
    "v_mov_b64 v[4:5], 0\n"
    "v_mov_b64 v[6:7], 0\n"
    "v_mov_b64 v[8:9], 0\n"
    "v_mov_b64 v[10:11], 0\n"
    "v_mov_b64 v[12:13], 0\n"
    "v_mov_b64 v[14:15], 0\n"
    "ds_read_b32 v76, v75 offset:104\n"
    "s_waitcnt lgkmcnt(0)\n"
    "v_cmp_gt_u32 vcc, 4, v76\n"
    "s_nop 0\n"
    "s_cbranch_vccnz .Lb_sl68_%=\n"
    ".Lb_ok67_%=:\n"
    "ds_read_b128 v[80:83], v64 offset:0\n"
    "ds_read_b128 v[84:87], v64 offset:1024\n"
    "ds_read_b128 v[88:91], v65 offset:0\n"
    "ds_read_b128 v[92:95], v65 offset:1024\n"
    "ds_read_b128 v[96:99], v66 offset:0\n"
    "ds_read_b128 v[100:103], v66 offset:1024\n"
    "ds_read_b128 v[104:107], v67 offset:0\n"
    "ds_read_b128 v[108:111], v67 offset:1024\n"
    "s_mov_b64 exec, 1\n"
    "ds_add_u32 v75, v78 offset:104\n"
    "s_mov_b64 exec, -1\n"
    "buffer_load_dword v77, v73, s[16:19], 0 offen offset:0 sc1\n"
    "buffer_load_dwordx4 v[224:227], v71, s[16:19], s28 offen offset:0 sc1\n"
    "buffer_load_dwordx4 v[228:231], v71, s[16:19], s28 offen offset:1024 sc1\n"
    "s_mov_b64 exec, s[44:45]\n"
    "buffer_load_dwordx4 v[232:235], v72, s[16:19], s28 offen sc1\n"
    "buffer_load_dwordx4 v[236:239], v72, s[16:19], s29 offen sc1\n"
    "s_mov_b64 exec, -1\n"
    "s_add_u32 s28, s28, 28672\n"
    "s_add_u32 s29, s29, 28672\n"
    "v_mov_b64 v[48:49], 0\n"
    "v_mov_b64 v[50:51], 0\n"
    "v_mov_b64 v[52:53], 0\n"
    "v_mov_b64 v[54:55], 0\n"
    "v_mov_b64 v[56:57], 0\n"
    "v_mov_b64 v[58:59], 0\n"
    "v_mov_b64 v[60:61], 0\n"
    "v_mov_b64 v[62:63], 0\n"
    "v_mov_b32 v78, 0xdddddddd\n"
    "v_lshl_add_u32 v70, v79, 2, s97\n"
    "s_setprio 0\n"
    "s_waitcnt vmcnt(12)\n"
    "s_waitcnt lgkmcnt(8)\n"
    "v_mfma_f32_32x32x16_bf16 v[128:143], v[192:195], v[80:83], 0\n"
    "s_waitcnt vmcnt(11)\n"
    "s_waitcnt lgkmcnt(7)\n"
    "v_mfma_f32_32x32x16_bf16 v[128:143], v[196:199], v[84:87], v[128:143]\n"
    "s_waitcnt lgkmcnt(6)\n"
    "v_mfma_f32_32x32x16_bf16 v[144:159], v[192:195], v[88:91], 0\n"
    "buffer_load_dwordx4 v[240:243], v71, s[16:19], s28 offen offset:0 sc1\n"
    "s_waitcnt lgkmcnt(5)\n"
    "v_mfma_f32_32x32x16_bf16 v[144:159], v[196:199], v[92:95], v[144:159]\n"
    "buffer_load_dwordx4 v[244:247], v71, s[16:19], s28 offen offset:1024 sc1\n"
    "s_waitcnt lgkmcnt(0)\n"
    "s_nop 4\n"
    "v_exp_f32 v128, v128\n"
    "v_exp_f32 v129, v129\n"
    "v_exp_f32 v130, v130\n"
    "v_exp_f32 v131, v131\n"
    "v_exp_f32 v132, v132\n"
    "v_exp_f32 v133, v133\n"
    "v_exp_f32 v134, v134\n"
    "v_exp_f32 v135, v135\n"
    "v_exp_f32 v136, v136\n"
    "v_exp_f32 v137, v137\n"
    "v_exp_f32 v138, v138\n"
    "v_exp_f32 v139, v139\n"
    "s_waitcnt vmcnt(11)\n"
    "v_exp_f32 v140, v140\n"
    "v_exp_f32 v141, v141\n"
    "v_exp_f32 v142, v142\n"
    "v_exp_f32 v143, v143\n"
    "v_mfma_f32_32x32x16_bf16 v[160:175], v[192:195], v[96:99], 0\n"
    "v_exp_f32 v144, v144\n"
    "v_exp_f32 v145, v145\n"
    "v_exp_f32 v146, v146\n"
    "v_exp_f32 v147, v147\n"
    "v_mfma_f32_32x32x16_bf16 v[160:175], v[196:199], v[100:103], v[160:175]\n"
    "v_exp_f32 v148, v148\n"
    "v_exp_f32 v149, v149\n"
    "v_exp_f32 v150, v150\n"
    "v_exp_f32 v151, v151\n"
    "v_smfmac_f32_32x32x32_bf16 v[0:15], v[200:203], v[128:135], v78\n"
    "s_mov_b64 exec, s[44:45]\n"
    "buffer_load_dwordx4 v[248:251], v72, s[16:19], s28 offen sc1\n"
    "buffer_load_dwordx4 v[252:255], v72, s[16:19], s29 offen sc1\n"
    "s_mov_b64 exec, -1\n"
    "v_exp_f32 v152, v152\n"
    "v_exp_f32 v153, v153\n"
    "v_exp_f32 v154, v154\n"
    "v_exp_f32 v155, v155\n"
    "v_smfmac_f32_32x32x32_bf16 v[0:15], v[204:207], v[136:143], v78\n"
    "v_exp_f32 v156, v156\n"
    "v_exp_f32 v157, v157\n"
    "v_exp_f32 v158, v158\n"
    "v_exp_f32 v159, v159\n"
    "v_mfma_f32_32x32x16_bf16 v[176:191], v[192:195], v[104:107], 0\n"
    "s_add_u32 s28, s28, 430080\n"
    "v_exp_f32 v160, v160\n"
    "v_exp_f32 v161, v161\n"
    "v_exp_f32 v162, v162\n"
    "v_exp_f32 v163, v163\n"
    "v_mfma_f32_32x32x16_bf16 v[176:191], v[196:199], v[108:111], v[176:191]\n"
    "s_add_u32 s29, s29, 430080\n"
    "v_exp_f32 v164, v164\n"
    "v_exp_f32 v165, v165\n"
    "v_exp_f32 v166, v166\n"
    "v_exp_f32 v167, v167\n"
    "v_smfmac_f32_32x32x32_bf16 v[16:31], v[200:203], v[144:151], v78\n"
    "v_exp_f32 v168, v168\n"
    "v_exp_f32 v169, v169\n"
    "v_exp_f32 v170, v170\n"
    "v_exp_f32 v171, v171\n"
    "v_smfmac_f32_32x32x32_bf16 v[16:31], v[204:207], v[152:159], v78\n"
    "s_waitcnt vmcnt(9)\n"
    "v_exp_f32 v172, v172\n"
    "v_exp_f32 v173, v173\n"
    "v_exp_f32 v174, v174\n"
    "v_exp_f32 v175, v175\n"
    "v_mfma_f32_32x32x16_bf16 v[128:143], v[208:211], v[80:83], 0\n"
    "v_exp_f32 v176, v176\n"
    "v_exp_f32 v177, v177\n"
    "v_exp_f32 v178, v178\n"
    "v_exp_f32 v179, v179\n"
    "v_mfma_f32_32x32x16_bf16 v[128:143], v[212:215], v[84:87], v[128:143]\n"
    "v_exp_f32 v180, v180\n"
    "v_exp_f32 v181, v181\n"
    "v_exp_f32 v182, v182\n"
    "v_exp_f32 v183, v183\n"
    "v_smfmac_f32_32x32x32_bf16 v[32:47], v[200:203], v[160:167], v78\n"
    "v_exp_f32 v184, v184\n"
    "v_exp_f32 v185, v185\n"
    "v_exp_f32 v186, v186\n"
    "v_exp_f32 v187, v187\n"
    "v_smfmac_f32_32x32x32_bf16 v[32:47], v[204:207], v[168:175], v78\n"
    "v_exp_f32 v188, v188\n"
    "v_exp_f32 v189, v189\n"
    "v_exp_f32 v190, v190\n"
    "v_exp_f32 v191, v191\n"
    "v_mfma_f32_32x32x16_bf16 v[144:159], v[208:211], v[88:91], 0\n"
    "v_exp_f32 v128, v128\n"
    "v_exp_f32 v129, v129\n"
    "v_exp_f32 v130, v130\n"
    "v_exp_f32 v131, v131\n"
    "v_mfma_f32_32x32x16_bf16 v[144:159], v[212:215], v[92:95], v[144:159]\n"
    "v_exp_f32 v132, v132\n"
    "v_exp_f32 v133, v133\n"
    "v_exp_f32 v134, v134\n"
    "v_exp_f32 v135, v135\n"
    "v_smfmac_f32_32x32x32_bf16 v[48:63], v[200:203], v[176:183], v78\n"
    "s_waitcnt vmcnt(8)\n"
    "v_cmp_ne_u32 vcc, 1, v77\n"
    "s_nop 0\n"
    "s_cbranch_vccnz .Lb_sl71_%=\n"
    ".Lb_ok70_%=:\n"
    "buffer_load_dwordx4 v[192:195], v71, s[16:19], s28 offen offset:0 sc1\n"
    "v_exp_f32 v136, v136\n"
    "v_exp_f32 v137, v137\n"
    "v_exp_f32 v138, v138\n"
    "v_exp_f32 v139, v139\n"
    "v_smfmac_f32_32x32x32_bf16 v[48:63], v[204:207], v[184:191], v78\n"
    "buffer_load_dwordx4 v[196:199], v71, s[16:19], s28 offen offset:1024 sc1\n"
    "v_exp_f32 v140, v140\n"
    "v_exp_f32 v141, v141\n"
    "v_exp_f32 v142, v142\n"
    "v_exp_f32 v143, v143\n"
    "v_mfma_f32_32x32x16_bf16 v[160:175], v[208:211], v[96:99], 0\n"
    "v_exp_f32 v144, v144\n"
    "v_exp_f32 v145, v145\n"
    "v_exp_f32 v146, v146\n"
    "v_exp_f32 v147, v147\n"
    "v_mfma_f32_32x32x16_bf16 v[160:175], v[212:215], v[100:103], v[160:175]\n"
    "v_exp_f32 v148, v148\n"
    "v_exp_f32 v149, v149\n"
    "v_exp_f32 v150, v150\n"
    "v_exp_f32 v151, v151\n"
    "v_smfmac_f32_32x32x32_bf16 v[0:15], v[216:219], v[128:135], v78\n"
    "s_mov_b64 exec, s[44:45]\n"
    "buffer_load_dwordx4 v[200:203], v72, s[16:19], s28 offen sc1\n"
    "buffer_load_dwordx4 v[204:207], v72, s[16:19], s29 offen sc1\n"
    "s_mov_b64 exec, -1\n"
    "v_exp_f32 v152, v152\n"
    "v_exp_f32 v153, v153\n"
    "v_exp_f32 v154, v154\n"
    "v_exp_f32 v155, v155\n"
    "v_smfmac_f32_32x32x32_bf16 v[0:15], v[220:223], v[136:143], v78\n"
    "v_exp_f32 v156, v156\n"
    "v_exp_f32 v157, v157\n"
    "v_exp_f32 v158, v158\n"
    "v_exp_f32 v159, v159\n"
    "v_mfma_f32_32x32x16_bf16 v[176:191], v[208:211], v[104:107], 0\n"
    "s_add_u32 s28, s28, 28672\n"
    "v_exp_f32 v160, v160\n"
    "v_exp_f32 v161, v161\n"
    "v_exp_f32 v162, v162\n"
    "v_exp_f32 v163, v163\n"
    "v_mfma_f32_32x32x16_bf16 v[176:191], v[212:215], v[108:111], v[176:191]\n"
    "s_add_u32 s29, s29, 28672\n"
    "v_exp_f32 v164, v164\n"
    "v_exp_f32 v165, v165\n"
    "v_exp_f32 v166, v166\n"
    "v_exp_f32 v167, v167\n"
    "v_smfmac_f32_32x32x32_bf16 v[16:31], v[216:219], v[144:151], v78\n"
    "v_exp_f32 v168, v168\n"
    "v_exp_f32 v169, v169\n"
    "v_exp_f32 v170, v170\n"
    "v_exp_f32 v171, v171\n"
    "v_smfmac_f32_32x32x32_bf16 v[16:31], v[220:223], v[152:159], v78\n"
    "s_waitcnt vmcnt(8)\n"
    "v_exp_f32 v172, v172\n"
    "v_exp_f32 v173, v173\n"
    "v_exp_f32 v174, v174\n"
    "v_exp_f32 v175, v175\n"
    "v_mfma_f32_32x32x16_bf16 v[128:143], v[224:227], v[80:83], 0\n"
    "v_exp_f32 v176, v176\n"
    "v_exp_f32 v177, v177\n"
    "v_exp_f32 v178, v178\n"
    "v_exp_f32 v179, v179\n"
    "v_mfma_f32_32x32x16_bf16 v[128:143], v[228:231], v[84:87], v[128:143]\n"
    "v_exp_f32 v180, v180\n"
    "v_exp_f32 v181, v181\n"
    "v_exp_f32 v182, v182\n"
    "v_exp_f32 v183, v183\n"
    "v_smfmac_f32_32x32x32_bf16 v[32:47], v[216:219], v[160:167], v78\n"
    "v_exp_f32 v184, v184\n"
    "v_exp_f32 v185, v185\n"
    "v_exp_f32 v186, v186\n"
    "v_exp_f32 v187, v187\n"
    "v_smfmac_f32_32x32x32_bf16 v[32:47], v[220:223], v[168:175], v78\n"
    "v_exp_f32 v188, v188\n"
    "v_exp_f32 v189, v189\n"
    "v_exp_f32 v190, v190\n"
    "v_exp_f32 v191, v191\n"
    "v_mfma_f32_32x32x16_bf16 v[144:159], v[224:227], v[88:91], 0\n"
    "v_exp_f32 v128, v128\n"
    "v_exp_f32 v129, v129\n"
    "v_exp_f32 v130, v130\n"
    "v_exp_f32 v131, v131\n"
    "v_mfma_f32_32x32x16_bf16 v[144:159], v[228:231], v[92:95], v[144:159]\n"
    "v_exp_f32 v132, v132\n"
    "v_exp_f32 v133, v133\n"
    "v_exp_f32 v134, v134\n"
    "v_exp_f32 v135, v135\n"
    "v_smfmac_f32_32x32x32_bf16 v[48:63], v[216:219], v[176:183], v78\n"
    "buffer_load_dwordx4 v[208:211], v71, s[16:19], s28 offen offset:0 sc1\n"
    "v_exp_f32 v136, v136\n"
    "v_exp_f32 v137, v137\n"
    "v_exp_f32 v138, v138\n"
    "v_exp_f32 v139, v139\n"
    "v_smfmac_f32_32x32x32_bf16 v[48:63], v[220:223], v[184:191], v78\n"
    "buffer_load_dwordx4 v[212:215], v71, s[16:19], s28 offen offset:1024 sc1\n"
    "v_exp_f32 v140, v140\n"
    "v_exp_f32 v141, v141\n"
    "v_exp_f32 v142, v142\n"
    "v_exp_f32 v143, v143\n"
    "v_mfma_f32_32x32x16_bf16 v[160:175], v[224:227], v[96:99], 0\n"
    "v_exp_f32 v144, v144\n"
    "v_exp_f32 v145, v145\n"
    "v_exp_f32 v146, v146\n"
    "v_exp_f32 v147, v147\n"
    "v_mfma_f32_32x32x16_bf16 v[160:175], v[228:231], v[100:103], v[160:175]\n"
    "v_exp_f32 v148, v148\n"
    "v_exp_f32 v149, v149\n"
    "v_exp_f32 v150, v150\n"
    "v_exp_f32 v151, v151\n"
    "v_smfmac_f32_32x32x32_bf16 v[0:15], v[232:235], v[128:135], v78\n"
    "s_mov_b64 exec, s[44:45]\n"
    "buffer_load_dwordx4 v[216:219], v72, s[16:19], s28 offen sc1\n"
    "buffer_load_dwordx4 v[220:223], v72, s[16:19], s29 offen sc1\n"
    "s_mov_b64 exec, -1\n"
    "v_exp_f32 v152, v152\n"
    "v_exp_f32 v153, v153\n"
    "v_exp_f32 v154, v154\n"
    "v_exp_f32 v155, v155\n"
    "v_smfmac_f32_32x32x32_bf16 v[0:15], v[236:239], v[136:143], v78\n"
    "v_exp_f32 v156, v156\n"
    "v_exp_f32 v157, v157\n"
    "v_exp_f32 v158, v158\n"
    "v_exp_f32 v159, v159\n"
    "v_mfma_f32_32x32x16_bf16 v[176:191], v[224:227], v[104:107], 0\n"
    "s_add_u32 s28, s28, 430080\n"
    "v_exp_f32 v160, v160\n"
    "v_exp_f32 v161, v161\n"
    "v_exp_f32 v162, v162\n"
    "v_exp_f32 v163, v163\n"
    "v_mfma_f32_32x32x16_bf16 v[176:191], v[228:231], v[108:111], v[176:191]\n"
    "s_add_u32 s29, s29, 430080\n"
    "v_exp_f32 v164, v164\n"
    "v_exp_f32 v165, v165\n"
    "v_exp_f32 v166, v166\n"
    "v_exp_f32 v167, v167\n"
    "v_smfmac_f32_32x32x32_bf16 v[16:31], v[232:235], v[144:151], v78\n"
    "v_exp_f32 v168, v168\n"
    "v_exp_f32 v169, v169\n"
    "v_exp_f32 v170, v170\n"
    "v_exp_f32 v171, v171\n"
    "v_smfmac_f32_32x32x32_bf16 v[16:31], v[236:239], v[152:159], v78\n"
    "s_waitcnt vmcnt(8)\n"
    "v_exp_f32 v172, v172\n"
    "v_exp_f32 v173, v173\n"
    "v_exp_f32 v174, v174\n"
    "v_exp_f32 v175, v175\n"
    "v_mfma_f32_32x32x16_bf16 v[128:143], v[240:243], v[80:83], 0\n"
    "v_exp_f32 v176, v176\n"
    "v_exp_f32 v177, v177\n"
    "v_exp_f32 v178, v178\n"
    "v_exp_f32 v179, v179\n"
    "v_mfma_f32_32x32x16_bf16 v[128:143], v[244:247], v[84:87], v[128:143]\n"
    "v_exp_f32 v180, v180\n"
    "v_exp_f32 v181, v181\n"
    "v_exp_f32 v182, v182\n"
    "v_exp_f32 v183, v183\n"
    "v_smfmac_f32_32x32x32_bf16 v[32:47], v[232:235], v[160:167], v78\n"
    "buffer_load_dword v77, v73, s[16:19], 0 offen offset:4 sc1\n"
    "v_exp_f32 v184, v184\n"
    "v_exp_f32 v185, v185\n"
    "v_exp_f32 v186, v186\n"
    "v_exp_f32 v187, v187\n"
    "v_smfmac_f32_32x32x32_bf16 v[32:47], v[236:239], v[168:175], v78\n"
    "v_exp_f32 v188, v188\n"
    "v_exp_f32 v189, v189\n"
    "v_exp_f32 v190, v190\n"
    "v_exp_f32 v191, v191\n"
    "v_mfma_f32_32x32x16_bf16 v[144:159], v[240:243], v[88:91], 0\n"
    "v_exp_f32 v128, v128\n"
    "v_exp_f32 v129, v129\n"
    "v_exp_f32 v130, v130\n"
    "v_exp_f32 v131, v131\n"
    "v_mfma_f32_32x32x16_bf16 v[144:159], v[244:247], v[92:95], v[144:159]\n"
    "v_exp_f32 v132, v132\n"
    "v_exp_f32 v133, v133\n"
    "v_exp_f32 v134, v134\n"
    "v_exp_f32 v135, v135\n"
    "v_smfmac_f32_32x32x32_bf16 v[48:63], v[232:235], v[176:183], v78\n"
    "buffer_load_dwordx4 v[224:227], v71, s[16:19], s28 offen offset:0 sc1\n"
    "v_exp_f32 v136, v136\n"
    "v_exp_f32 v137, v137\n"
    "v_exp_f32 v138, v138\n"
    "v_exp_f32 v139, v139\n"
    "v_smfmac_f32_32x32x32_bf16 v[48:63], v[236:239], v[184:191], v78\n"
    "buffer_load_dwordx4 v[228:231], v71, s[16:19], s28 offen offset:1024 sc1\n"
    "v_exp_f32 v140, v140\n"
    "v_exp_f32 v141, v141\n"
    "v_exp_f32 v142, v142\n"
    "v_exp_f32 v143, v143\n"
    "v_mfma_f32_32x32x16_bf16 v[160:175], v[240:243], v[96:99], 0\n"
    "v_exp_f32 v144, v144\n"
    "v_exp_f32 v145, v145\n"
    "v_exp_f32 v146, v146\n"
    "v_exp_f32 v147, v147\n"
    "v_mfma_f32_32x32x16_bf16 v[160:175], v[244:247], v[100:103], v[160:175]\n"
    "v_exp_f32 v148, v148\n"
    "v_exp_f32 v149, v149\n"
    "v_exp_f32 v150, v150\n"
    "v_exp_f32 v151, v151\n"
    "v_smfmac_f32_32x32x32_bf16 v[0:15], v[248:251], v[128:135], v78\n"
    "s_mov_b64 exec, s[44:45]\n"
    "buffer_load_dwordx4 v[232:235], v72, s[16:19], s28 offen sc1\n"
    "buffer_load_dwordx4 v[236:239], v72, s[16:19], s29 offen sc1\n"
    "s_mov_b64 exec, -1\n"
    "v_exp_f32 v152, v152\n"
    "v_exp_f32 v153, v153\n"
    "v_exp_f32 v154, v154\n"
    "v_exp_f32 v155, v155\n"
    "v_smfmac_f32_32x32x32_bf16 v[0:15], v[252:255], v[136:143], v78\n"
    "v_exp_f32 v156, v156\n"
    "v_exp_f32 v157, v157\n"
    "v_exp_f32 v158, v158\n"
    "v_exp_f32 v159, v159\n"
    "v_mfma_f32_32x32x16_bf16 v[176:191], v[240:243], v[104:107], 0\n"
    "s_add_u32 s28, s28, 28672\n"
    "v_exp_f32 v160, v160\n"
    "v_exp_f32 v161, v161\n"
    "v_exp_f32 v162, v162\n"
    "v_exp_f32 v163, v163\n"
    "v_mfma_f32_32x32x16_bf16 v[176:191], v[244:247], v[108:111], v[176:191]\n"
    "s_add_u32 s29, s29, 28672\n"
    "v_exp_f32 v164, v164\n"
    "v_exp_f32 v165, v165\n"
    "v_exp_f32 v166, v166\n"
    "v_exp_f32 v167, v167\n"
    "v_smfmac_f32_32x32x32_bf16 v[16:31], v[248:251], v[144:151], v78\n"
    "v_exp_f32 v168, v168\n"
    "v_exp_f32 v169, v169\n"
    "v_exp_f32 v170, v170\n"
    "v_exp_f32 v171, v171\n"
    "v_smfmac_f32_32x32x32_bf16 v[16:31], v[252:255], v[152:159], v78\n"
    "s_waitcnt vmcnt(9)\n"
    "v_exp_f32 v172, v172\n"
    "v_exp_f32 v173, v173\n"
    "v_exp_f32 v174, v174\n"
    "v_exp_f32 v175, v175\n"
    "v_mfma_f32_32x32x16_bf16 v[128:143], v[192:195], v[80:83], 0\n"
    "v_exp_f32 v176, v176\n"
    "v_exp_f32 v177, v177\n"
    "v_exp_f32 v178, v178\n"
    "v_exp_f32 v179, v179\n"
    "v_mfma_f32_32x32x16_bf16 v[128:143], v[196:199], v[84:87], v[128:143]\n"
    "v_exp_f32 v180, v180\n"
    "v_exp_f32 v181, v181\n"
    "v_exp_f32 v182, v182\n"
    "v_exp_f32 v183, v183\n"
    "v_smfmac_f32_32x32x32_bf16 v[32:47], v[248:251], v[160:167], v78\n"
    "v_exp_f32 v184, v184\n"
    "v_exp_f32 v185, v185\n"
    "v_exp_f32 v186, v186\n"
    "v_exp_f32 v187, v187\n"
    "v_smfmac_f32_32x32x32_bf16 v[32:47], v[252:255], v[168:175], v78\n"
    "v_exp_f32 v188, v188\n"
    "v_exp_f32 v189, v189\n"
    "v_exp_f32 v190, v190\n"
    "v_exp_f32 v191, v191\n"
    "v_mfma_f32_32x32x16_bf16 v[144:159], v[192:195], v[88:91], 0\n"
    "v_exp_f32 v128, v128\n"
    "v_exp_f32 v129, v129\n"
    "v_exp_f32 v130, v130\n"
    "v_exp_f32 v131, v131\n"
    "v_mfma_f32_32x32x16_bf16 v[144:159], v[196:199], v[92:95], v[144:159]\n"
    "v_exp_f32 v132, v132\n"
    "v_exp_f32 v133, v133\n"
    "v_exp_f32 v134, v134\n"
    "v_exp_f32 v135, v135\n"
    "v_smfmac_f32_32x32x32_bf16 v[48:63], v[248:251], v[176:183], v78\n"
    "buffer_load_dwordx4 v[240:243], v71, s[16:19], s28 offen offset:0 sc1\n"
    "v_exp_f32 v136, v136\n"
    "v_exp_f32 v137, v137\n"
    "v_exp_f32 v138, v138\n"
    "v_exp_f32 v139, v139\n"
    "v_smfmac_f32_32x32x32_bf16 v[48:63], v[252:255], v[184:191], v78\n"
    "buffer_load_dwordx4 v[244:247], v71, s[16:19], s28 offen offset:1024 sc1\n"
    "v_exp_f32 v140, v140\n"
    "v_exp_f32 v141, v141\n"
    "v_exp_f32 v142, v142\n"
    "v_exp_f32 v143, v143\n"
    "v_mfma_f32_32x32x16_bf16 v[160:175], v[192:195], v[96:99], 0\n"
    "v_exp_f32 v144, v144\n"
    "v_exp_f32 v145, v145\n"
    "v_exp_f32 v146, v146\n"
    "v_exp_f32 v147, v147\n"
    "v_mfma_f32_32x32x16_bf16 v[160:175], v[196:199], v[100:103], v[160:175]\n"
    "v_exp_f32 v148, v148\n"
    "v_exp_f32 v149, v149\n"
    "v_exp_f32 v150, v150\n"
    "v_exp_f32 v151, v151\n"
    "v_smfmac_f32_32x32x32_bf16 v[0:15], v[200:203], v[128:135], v78\n"
    "s_mov_b64 exec, s[44:45]\n"
    "buffer_load_dwordx4 v[248:251], v72, s[16:19], s28 offen sc1\n"
    "buffer_load_dwordx4 v[252:255], v72, s[16:19], s29 offen sc1\n"
    "s_mov_b64 exec, -1\n"
    "v_exp_f32 v152, v152\n"
    "v_exp_f32 v153, v153\n"
    "v_exp_f32 v154, v154\n"
    "v_exp_f32 v155, v155\n"
    "v_smfmac_f32_32x32x32_bf16 v[0:15], v[204:207], v[136:143], v78\n"
    "v_exp_f32 v156, v156\n"
    "v_exp_f32 v157, v157\n"
    "v_exp_f32 v158, v158\n"
    "v_exp_f32 v159, v159\n"
    "v_mfma_f32_32x32x16_bf16 v[176:191], v[192:195], v[104:107], 0\n"
    "s_add_u32 s28, s28, 4293619712\n"
    "v_exp_f32 v160, v160\n"
    "v_exp_f32 v161, v161\n"
    "v_exp_f32 v162, v162\n"
    "v_exp_f32 v163, v163\n"
    "v_mfma_f32_32x32x16_bf16 v[176:191], v[196:199], v[108:111], v[176:191]\n"
    "s_add_u32 s29, s29, 4293619712\n"
    "v_exp_f32 v164, v164\n"
    "v_exp_f32 v165, v165\n"
    "v_exp_f32 v166, v166\n"
    "v_exp_f32 v167, v167\n"
    "v_smfmac_f32_32x32x32_bf16 v[16:31], v[200:203], v[144:151], v78\n"
    "v_exp_f32 v168, v168\n"
    "v_exp_f32 v169, v169\n"
    "v_exp_f32 v170, v170\n"
    "v_exp_f32 v171, v171\n"
    "v_smfmac_f32_32x32x32_bf16 v[16:31], v[204:207], v[152:159], v78\n"
    "s_waitcnt vmcnt(9)\n"
    "v_exp_f32 v172, v172\n"
    "v_exp_f32 v173, v173\n"
    "v_exp_f32 v174, v174\n"
    "v_exp_f32 v175, v175\n"
    "v_mfma_f32_32x32x16_bf16 v[128:143], v[208:211], v[80:83], 0\n"
    "v_exp_f32 v176, v176\n"
    "v_exp_f32 v177, v177\n"
    "v_exp_f32 v178, v178\n"
    "v_exp_f32 v179, v179\n"
    "v_mfma_f32_32x32x16_bf16 v[128:143], v[212:215], v[84:87], v[128:143]\n"
    "v_exp_f32 v180, v180\n"
    "v_exp_f32 v181, v181\n"
    "v_exp_f32 v182, v182\n"
    "v_exp_f32 v183, v183\n"
    "v_smfmac_f32_32x32x32_bf16 v[32:47], v[200:203], v[160:167], v78\n"
    "s_waitcnt vmcnt(8)\n"
    "v_cmp_ne_u32 vcc, 1, v77\n"
    "s_nop 0\n"
    "s_cbranch_vccnz .Lb_sl74_%=\n"
    ".Lb_ok73_%=:\n"
    "v_exp_f32 v184, v184\n"
    "v_exp_f32 v185, v185\n"
    "v_exp_f32 v186, v186\n"
    "v_exp_f32 v187, v187\n"
    "v_smfmac_f32_32x32x32_bf16 v[32:47], v[204:207], v[168:175], v78\n"
    "v_exp_f32 v188, v188\n"
    "v_exp_f32 v189, v189\n"
    "v_exp_f32 v190, v190\n"
    "v_exp_f32 v191, v191\n"
    "v_mfma_f32_32x32x16_bf16 v[144:159], v[208:211], v[88:91], 0\n"
    "v_exp_f32 v128, v128\n"
    "v_exp_f32 v129, v129\n"
    "v_exp_f32 v130, v130\n"
    "v_exp_f32 v131, v131\n"
    "v_mfma_f32_32x32x16_bf16 v[144:159], v[212:215], v[92:95], v[144:159]\n"
    "v_exp_f32 v132, v132\n"
    "v_exp_f32 v133, v133\n"
    "v_exp_f32 v134, v134\n"
    "v_exp_f32 v135, v135\n"
    "v_smfmac_f32_32x32x32_bf16 v[48:63], v[200:203], v[176:183], v78\n"
    "buffer_load_dwordx4 v[192:195], v71, s[16:19], s28 offen offset:0 sc1\n"
    "v_exp_f32 v136, v136\n"
    "v_exp_f32 v137, v137\n"
    "v_exp_f32 v138, v138\n"
    "v_exp_f32 v139, v139\n"
    "v_smfmac_f32_32x32x32_bf16 v[48:63], v[204:207], v[184:191], v78\n"
    "buffer_load_dwordx4 v[196:199], v71, s[16:19], s28 offen offset:1024 sc1\n"
    "v_exp_f32 v140, v140\n"
    "v_exp_f32 v141, v141\n"
    "v_exp_f32 v142, v142\n"
    "v_exp_f32 v143, v143\n"
    "v_mfma_f32_32x32x16_bf16 v[160:175], v[208:211], v[96:99], 0\n"
    "v_exp_f32 v144, v144\n"
    "v_exp_f32 v145, v145\n"
    "v_exp_f32 v146, v146\n"
    "v_exp_f32 v147, v147\n"
    "v_mfma_f32_32x32x16_bf16 v[160:175], v[212:215], v[100:103], v[160:175]\n"
    "v_exp_f32 v148, v148\n"
    "v_exp_f32 v149, v149\n"
    "v_exp_f32 v150, v150\n"
    "v_exp_f32 v151, v151\n"
    "v_smfmac_f32_32x32x32_bf16 v[0:15], v[216:219], v[128:135], v78\n"
    "s_mov_b64 exec, s[44:45]\n"
    "buffer_load_dwordx4 v[200:203], v72, s[16:19], s28 offen sc1\n"
    "buffer_load_dwordx4 v[204:207], v72, s[16:19], s29 offen sc1\n"
    "s_mov_b64 exec, -1\n"
    "v_exp_f32 v152, v152\n"
    "v_exp_f32 v153, v153\n"
    "v_exp_f32 v154, v154\n"
    "v_exp_f32 v155, v155\n"
    "v_smfmac_f32_32x32x32_bf16 v[0:15], v[220:223], v[136:143], v78\n"
    "v_exp_f32 v156, v156\n"
    "v_exp_f32 v157, v157\n"
    "v_exp_f32 v158, v158\n"
    "v_exp_f32 v159, v159\n"
    "v_mfma_f32_32x32x16_bf16 v[176:191], v[208:211], v[104:107], 0\n"
    "s_add_u32 s28, s28, 28672\n"
    "v_exp_f32 v160, v160\n"
    "v_exp_f32 v161, v161\n"
    "v_exp_f32 v162, v162\n"
    "v_exp_f32 v163, v163\n"
    "v_mfma_f32_32x32x16_bf16 v[176:191], v[212:215], v[108:111], v[176:191]\n"
    "s_add_u32 s29, s29, 28672\n"
    "v_exp_f32 v164, v164\n"
    "v_exp_f32 v165, v165\n"
    "v_exp_f32 v166, v166\n"
    "v_exp_f32 v167, v167\n"
    "v_smfmac_f32_32x32x32_bf16 v[16:31], v[216:219], v[144:151], v78\n"
    "v_exp_f32 v168, v168\n"
    "v_exp_f32 v169, v169\n"
    "v_exp_f32 v170, v170\n"
    "v_exp_f32 v171, v171\n"
    "v_smfmac_f32_32x32x32_bf16 v[16:31], v[220:223], v[152:159], v78\n"
    "s_waitcnt vmcnt(8)\n"
    "v_exp_f32 v172, v172\n"
    "v_exp_f32 v173, v173\n"
    "v_exp_f32 v174, v174\n"
    "v_exp_f32 v175, v175\n"
    "v_mfma_f32_32x32x16_bf16 v[128:143], v[224:227], v[80:83], 0\n"
    "v_exp_f32 v176, v176\n"
    "v_exp_f32 v177, v177\n"
    "v_exp_f32 v178, v178\n"
    "v_exp_f32 v179, v179\n"
    "v_mfma_f32_32x32x16_bf16 v[128:143], v[228:231], v[84:87], v[128:143]\n"
    "v_exp_f32 v180, v180\n"
    "v_exp_f32 v181, v181\n"
    "v_exp_f32 v182, v182\n"
    "v_exp_f32 v183, v183\n"
    "v_smfmac_f32_32x32x32_bf16 v[32:47], v[216:219], v[160:167], v78\n"
    "v_exp_f32 v184, v184\n"
    "v_exp_f32 v185, v185\n"
    "v_exp_f32 v186, v186\n"
    "v_exp_f32 v187, v187\n"
    "v_smfmac_f32_32x32x32_bf16 v[32:47], v[220:223], v[168:175], v78\n"
    "v_exp_f32 v188, v188\n"
    "v_exp_f32 v189, v189\n"
    "v_exp_f32 v190, v190\n"
    "v_exp_f32 v191, v191\n"
    "v_mfma_f32_32x32x16_bf16 v[144:159], v[224:227], v[88:91], 0\n"
    "v_exp_f32 v128, v128\n"
    "v_exp_f32 v129, v129\n"
    "v_exp_f32 v130, v130\n"
    "v_exp_f32 v131, v131\n"
    "v_mfma_f32_32x32x16_bf16 v[144:159], v[228:231], v[92:95], v[144:159]\n"
    "v_exp_f32 v132, v132\n"
    "v_exp_f32 v133, v133\n"
    "v_exp_f32 v134, v134\n"
    "v_exp_f32 v135, v135\n"
    "v_smfmac_f32_32x32x32_bf16 v[48:63], v[216:219], v[176:183], v78\n"
    "buffer_load_dwordx4 v[208:211], v71, s[16:19], s28 offen offset:0 sc1\n"
    "v_exp_f32 v136, v136\n"
    "v_exp_f32 v137, v137\n"
    "v_exp_f32 v138, v138\n"
    "v_exp_f32 v139, v139\n"
    "v_smfmac_f32_32x32x32_bf16 v[48:63], v[220:223], v[184:191], v78\n"
    "buffer_load_dwordx4 v[212:215], v71, s[16:19], s28 offen offset:1024 sc1\n"
    "v_exp_f32 v140, v140\n"
    "v_exp_f32 v141, v141\n"
    "v_exp_f32 v142, v142\n"
    "v_exp_f32 v143, v143\n"
    "v_mfma_f32_32x32x16_bf16 v[160:175], v[224:227], v[96:99], 0\n"
    "v_exp_f32 v144, v144\n"
    "v_exp_f32 v145, v145\n"
    "v_exp_f32 v146, v146\n"
    "v_exp_f32 v147, v147\n"
    "v_mfma_f32_32x32x16_bf16 v[160:175], v[228:231], v[100:103], v[160:175]\n"
    "v_exp_f32 v148, v148\n"
    "v_exp_f32 v149, v149\n"
    "v_exp_f32 v150, v150\n"
    "v_exp_f32 v151, v151\n"
    "v_smfmac_f32_32x32x32_bf16 v[0:15], v[232:235], v[128:135], v78\n"
    "s_mov_b64 exec, s[44:45]\n"
    "buffer_load_dwordx4 v[216:219], v72, s[16:19], s28 offen sc1\n"
    "buffer_load_dwordx4 v[220:223], v72, s[16:19], s29 offen sc1\n"
    "s_mov_b64 exec, -1\n"
    "v_exp_f32 v152, v152\n"
    "v_exp_f32 v153, v153\n"
    "v_exp_f32 v154, v154\n"
    "v_exp_f32 v155, v155\n"
    "v_smfmac_f32_32x32x32_bf16 v[0:15], v[236:239], v[136:143], v78\n"
    "v_exp_f32 v156, v156\n"
    "v_exp_f32 v157, v157\n"
    "v_exp_f32 v158, v158\n"
    "v_exp_f32 v159, v159\n"
    "v_mfma_f32_32x32x16_bf16 v[176:191], v[224:227], v[104:107], 0\n"
    "s_add_u32 s28, s28, 430080\n"
    "v_exp_f32 v160, v160\n"
    "v_exp_f32 v161, v161\n"
    "v_exp_f32 v162, v162\n"
    "v_exp_f32 v163, v163\n"
    "v_mfma_f32_32x32x16_bf16 v[176:191], v[228:231], v[108:111], v[176:191]\n"
    "s_add_u32 s29, s29, 430080\n"
    "v_exp_f32 v164, v164\n"
    "v_exp_f32 v165, v165\n"
    "v_exp_f32 v166, v166\n"
    "v_exp_f32 v167, v167\n"
    "v_smfmac_f32_32x32x32_bf16 v[16:31], v[232:235], v[144:151], v78\n"
    "v_exp_f32 v168, v168\n"
    "v_exp_f32 v169, v169\n"
    "v_exp_f32 v170, v170\n"
    "v_exp_f32 v171, v171\n"
    "v_smfmac_f32_32x32x32_bf16 v[16:31], v[236:239], v[152:159], v78\n"
    "s_waitcnt vmcnt(8)\n"
    "v_exp_f32 v172, v172\n"
    "v_exp_f32 v173, v173\n"
    "v_exp_f32 v174, v174\n"
    "v_exp_f32 v175, v175\n"
    "v_mfma_f32_32x32x16_bf16 v[128:143], v[240:243], v[80:83], 0\n"
    "v_exp_f32 v176, v176\n"
    "v_exp_f32 v177, v177\n"
    "v_exp_f32 v178, v178\n"
    "v_exp_f32 v179, v179\n"
    "v_mfma_f32_32x32x16_bf16 v[128:143], v[244:247], v[84:87], v[128:143]\n"
    "v_exp_f32 v180, v180\n"
    "v_exp_f32 v181, v181\n"
    "v_exp_f32 v182, v182\n"
    "v_exp_f32 v183, v183\n"
    "v_smfmac_f32_32x32x32_bf16 v[32:47], v[232:235], v[160:167], v78\n"
    "v_exp_f32 v184, v184\n"
    "v_exp_f32 v185, v185\n"
    "v_exp_f32 v186, v186\n"
    "v_exp_f32 v187, v187\n"
    "v_smfmac_f32_32x32x32_bf16 v[32:47], v[236:239], v[168:175], v78\n"
    "v_exp_f32 v188, v188\n"
    "v_exp_f32 v189, v189\n"
    "v_exp_f32 v190, v190\n"
    "v_exp_f32 v191, v191\n"
    "v_mfma_f32_32x32x16_bf16 v[144:159], v[240:243], v[88:91], 0\n"
    "v_exp_f32 v128, v128\n"
    "v_exp_f32 v129, v129\n"
    "v_exp_f32 v130, v130\n"
    "v_exp_f32 v131, v131\n"
    "v_mfma_f32_32x32x16_bf16 v[144:159], v[244:247], v[92:95], v[144:159]\n"
    "v_exp_f32 v132, v132\n"
    "v_exp_f32 v133, v133\n"
    "v_exp_f32 v134, v134\n"
    "v_exp_f32 v135, v135\n"
    "v_smfmac_f32_32x32x32_bf16 v[48:63], v[232:235], v[176:183], v78\n"
    "buffer_load_dwordx4 v[224:227], v71, s[16:19], s28 offen offset:0 sc1\n"
    "v_exp_f32 v136, v136\n"
    "v_exp_f32 v137, v137\n"
    "v_exp_f32 v138, v138\n"
    "v_exp_f32 v139, v139\n"
    "v_smfmac_f32_32x32x32_bf16 v[48:63], v[236:239], v[184:191], v78\n"
    "buffer_load_dwordx4 v[228:231], v71, s[16:19], s28 offen offset:1024 sc1\n"
    "v_exp_f32 v140, v140\n"
    "v_exp_f32 v141, v141\n"
    "v_exp_f32 v142, v142\n"
    "v_exp_f32 v143, v143\n"
    "v_mfma_f32_32x32x16_bf16 v[160:175], v[240:243], v[96:99], 0\n"
    "v_exp_f32 v144, v144\n"
    "v_exp_f32 v145, v145\n"
    "v_exp_f32 v146, v146\n"
    "v_exp_f32 v147, v147\n"
    "v_mfma_f32_32x32x16_bf16 v[160:175], v[244:247], v[100:103], v[160:175]\n"
    "v_exp_f32 v148, v148\n"
    "v_exp_f32 v149, v149\n"
    "v_exp_f32 v150, v150\n"
    "v_exp_f32 v151, v151\n"
    "v_smfmac_f32_32x32x32_bf16 v[0:15], v[248:251], v[128:135], v78\n"
    "s_mov_b64 exec, s[44:45]\n"
    "buffer_load_dwordx4 v[232:235], v72, s[16:19], s28 offen sc1\n"
    "buffer_load_dwordx4 v[236:239], v72, s[16:19], s29 offen sc1\n"
    "s_mov_b64 exec, -1\n"
    "v_exp_f32 v152, v152\n"
    "v_exp_f32 v153, v153\n"
    "v_exp_f32 v154, v154\n"
    "v_exp_f32 v155, v155\n"
    "v_smfmac_f32_32x32x32_bf16 v[0:15], v[252:255], v[136:143], v78\n"
    "v_exp_f32 v156, v156\n"
    "v_exp_f32 v157, v157\n"
    "v_exp_f32 v158, v158\n"
    "v_exp_f32 v159, v159\n"
    "v_mfma_f32_32x32x16_bf16 v[176:191], v[240:243], v[104:107], 0\n"
    "s_add_u32 s28, s28, 28672\n"
    "v_exp_f32 v160, v160\n"
    "v_exp_f32 v161, v161\n"
    "v_exp_f32 v162, v162\n"
    "v_exp_f32 v163, v163\n"
    "v_mfma_f32_32x32x16_bf16 v[176:191], v[244:247], v[108:111], v[176:191]\n"
    "s_add_u32 s29, s29, 28672\n"
    "v_exp_f32 v164, v164\n"
    "v_exp_f32 v165, v165\n"
    "v_exp_f32 v166, v166\n"
    "v_exp_f32 v167, v167\n"
    "v_smfmac_f32_32x32x32_bf16 v[16:31], v[248:251], v[144:151], v78\n"
    "v_exp_f32 v168, v168\n"
    "v_exp_f32 v169, v169\n"
    "v_exp_f32 v170, v170\n"
    "v_exp_f32 v171, v171\n"
    "v_smfmac_f32_32x32x32_bf16 v[16:31], v[252:255], v[152:159], v78\n"
    "s_waitcnt vmcnt(8)\n"
    "v_exp_f32 v172, v172\n"
    "v_exp_f32 v173, v173\n"
    "v_exp_f32 v174, v174\n"
    "v_exp_f32 v175, v175\n"
    "v_mfma_f32_32x32x16_bf16 v[128:143], v[192:195], v[80:83], 0\n"
    "v_exp_f32 v176, v176\n"
    "v_exp_f32 v177, v177\n"
    "v_exp_f32 v178, v178\n"
    "v_exp_f32 v179, v179\n"
    "v_mfma_f32_32x32x16_bf16 v[128:143], v[196:199], v[84:87], v[128:143]\n"
    "v_exp_f32 v180, v180\n"
    "v_exp_f32 v181, v181\n"
    "v_exp_f32 v182, v182\n"
    "v_exp_f32 v183, v183\n"
    "v_smfmac_f32_32x32x32_bf16 v[32:47], v[248:251], v[160:167], v78\n"
    "v_exp_f32 v184, v184\n"
    "v_exp_f32 v185, v185\n"
    "v_exp_f32 v186, v186\n"
    "v_exp_f32 v187, v187\n"
    "v_smfmac_f32_32x32x32_bf16 v[32:47], v[252:255], v[168:175], v78\n"
    "v_exp_f32 v188, v188\n"
    "v_exp_f32 v189, v189\n"
    "v_exp_f32 v190, v190\n"
    "v_exp_f32 v191, v191\n"
    "v_mfma_f32_32x32x16_bf16 v[144:159], v[192:195], v[88:91], 0\n"
    "v_exp_f32 v128, v128\n"
    "v_exp_f32 v129, v129\n"
    "v_exp_f32 v130, v130\n"
    "v_exp_f32 v131, v131\n"
    "v_mfma_f32_32x32x16_bf16 v[144:159], v[196:199], v[92:95], v[144:159]\n"
    "v_exp_f32 v132, v132\n"
    "v_exp_f32 v133, v133\n"
    "v_exp_f32 v134, v134\n"
    "v_exp_f32 v135, v135\n"
    "v_smfmac_f32_32x32x32_bf16 v[48:63], v[248:251], v[176:183], v78\n"
    "buffer_load_dwordx4 v[240:243], v71, s[16:19], s28 offen offset:0 sc1\n"
    "v_exp_f32 v136, v136\n"
    "v_exp_f32 v137, v137\n"
    "v_exp_f32 v138, v138\n"
    "v_exp_f32 v139, v139\n"
    "v_smfmac_f32_32x32x32_bf16 v[48:63], v[252:255], v[184:191], v78\n"
    "buffer_load_dwordx4 v[244:247], v71, s[16:19], s28 offen offset:1024 sc1\n"
    "v_exp_f32 v140, v140\n"
    "v_exp_f32 v141, v141\n"
    "v_exp_f32 v142, v142\n"
    "v_exp_f32 v143, v143\n"
    "v_mfma_f32_32x32x16_bf16 v[160:175], v[192:195], v[96:99], 0\n"
    "v_exp_f32 v144, v144\n"
    "v_exp_f32 v145, v145\n"
    "v_exp_f32 v146, v146\n"
    "v_exp_f32 v147, v147\n"
    "v_mfma_f32_32x32x16_bf16 v[160:175], v[196:199], v[100:103], v[160:175]\n"
    "v_exp_f32 v148, v148\n"
    "v_exp_f32 v149, v149\n"
    "v_exp_f32 v150, v150\n"
    "v_exp_f32 v151, v151\n"
    "v_smfmac_f32_32x32x32_bf16 v[0:15], v[200:203], v[128:135], v78\n"
    "s_mov_b64 exec, s[44:45]\n"
    "buffer_load_dwordx4 v[248:251], v72, s[16:19], s28 offen sc1\n"
    "buffer_load_dwordx4 v[252:255], v72, s[16:19], s29 offen sc1\n"
    "s_mov_b64 exec, -1\n"
    "v_exp_f32 v152, v152\n"
    "v_exp_f32 v153, v153\n"
    "v_exp_f32 v154, v154\n"
    "v_exp_f32 v155, v155\n"
    "v_smfmac_f32_32x32x32_bf16 v[0:15], v[204:207], v[136:143], v78\n"
    "v_exp_f32 v156, v156\n"
    "v_exp_f32 v157, v157\n"
    "v_exp_f32 v158, v158\n"
    "v_exp_f32 v159, v159\n"
    "v_mfma_f32_32x32x16_bf16 v[176:191], v[192:195], v[104:107], 0\n"
    "s_add_u32 s28, s28, 430080\n"
    "v_exp_f32 v160, v160\n"
    "v_exp_f32 v161, v161\n"
    "v_exp_f32 v162, v162\n"
    "v_exp_f32 v163, v163\n"
    "v_mfma_f32_32x32x16_bf16 v[176:191], v[196:199], v[108:111], v[176:191]\n"
    "s_add_u32 s29, s29, 430080\n"
    "v_exp_f32 v164, v164\n"
    "v_exp_f32 v165, v165\n"
    "v_exp_f32 v166, v166\n"
    "v_exp_f32 v167, v167\n"
    "v_smfmac_f32_32x32x32_bf16 v[16:31], v[200:203], v[144:151], v78\n"
    "v_exp_f32 v168, v168\n"
    "v_exp_f32 v169, v169\n"
    "v_exp_f32 v170, v170\n"
    "v_exp_f32 v171, v171\n"
    "v_smfmac_f32_32x32x32_bf16 v[16:31], v[204:207], v[152:159], v78\n"
    "s_waitcnt vmcnt(8)\n"
    "v_exp_f32 v172, v172\n"
    "v_exp_f32 v173, v173\n"
    "v_exp_f32 v174, v174\n"
    "v_exp_f32 v175, v175\n"
    "v_mfma_f32_32x32x16_bf16 v[128:143], v[208:211], v[80:83], 0\n"
    "v_exp_f32 v176, v176\n"
    "v_exp_f32 v177, v177\n"
    "v_exp_f32 v178, v178\n"
    "v_exp_f32 v179, v179\n"
    "v_mfma_f32_32x32x16_bf16 v[128:143], v[212:215], v[84:87], v[128:143]\n"
    "v_exp_f32 v180, v180\n"
    "v_exp_f32 v181, v181\n"
    "v_exp_f32 v182, v182\n"
    "v_exp_f32 v183, v183\n"
    "v_smfmac_f32_32x32x32_bf16 v[32:47], v[200:203], v[160:167], v78\n"
    "v_exp_f32 v184, v184\n"
    "v_exp_f32 v185, v185\n"
    "v_exp_f32 v186, v186\n"
    "v_exp_f32 v187, v187\n"
    "v_smfmac_f32_32x32x32_bf16 v[32:47], v[204:207], v[168:175], v78\n"
    "v_exp_f32 v188, v188\n"
    "v_exp_f32 v189, v189\n"
    "v_exp_f32 v190, v190\n"
    "v_exp_f32 v191, v191\n"
    "v_mfma_f32_32x32x16_bf16 v[144:159], v[208:211], v[88:91], 0\n"
    "v_exp_f32 v128, v128\n"
    "v_exp_f32 v129, v129\n"
    "v_exp_f32 v130, v130\n"
    "v_exp_f32 v131, v131\n"
    "v_mfma_f32_32x32x16_bf16 v[144:159], v[212:215], v[92:95], v[144:159]\n"
    "v_exp_f32 v132, v132\n"
    "v_exp_f32 v133, v133\n"
    "v_exp_f32 v134, v134\n"
    "v_exp_f32 v135, v135\n"
    "v_smfmac_f32_32x32x32_bf16 v[48:63], v[200:203], v[176:183], v78\n"
    "buffer_load_dwordx4 v[192:195], v71, s[16:19], s28 offen offset:0 sc1\n"
    "v_exp_f32 v136, v136\n"
    "v_exp_f32 v137, v137\n"
    "v_exp_f32 v138, v138\n"
    "v_exp_f32 v139, v139\n"
    "v_smfmac_f32_32x32x32_bf16 v[48:63], v[204:207], v[184:191], v78\n"
    "buffer_load_dwordx4 v[196:199], v71, s[16:19], s28 offen offset:1024 sc1\n"
    "v_exp_f32 v140, v140\n"
    "v_exp_f32 v141, v141\n"
    "v_exp_f32 v142, v142\n"
    "v_exp_f32 v143, v143\n"
    "v_mfma_f32_32x32x16_bf16 v[160:175], v[208:211], v[96:99], 0\n"
    "v_exp_f32 v144, v144\n"
    "v_exp_f32 v145, v145\n"
    "v_exp_f32 v146, v146\n"
    "v_exp_f32 v147, v147\n"
    "v_mfma_f32_32x32x16_bf16 v[160:175], v[212:215], v[100:103], v[160:175]\n"
    "v_exp_f32 v148, v148\n"
    "v_exp_f32 v149, v149\n"
    "v_exp_f32 v150, v150\n"
    "v_exp_f32 v151, v151\n"
    "v_smfmac_f32_32x32x32_bf16 v[0:15], v[216:219], v[128:135], v78\n"
    "s_mov_b64 exec, s[44:45]\n"
    "buffer_load_dwordx4 v[200:203], v72, s[16:19], s28 offen sc1\n"
    "buffer_load_dwordx4 v[204:207], v72, s[16:19], s29 offen sc1\n"
    "s_mov_b64 exec, -1\n"
    "v_exp_f32 v152, v152\n"
    "v_exp_f32 v153, v153\n"
    "v_exp_f32 v154, v154\n"
    "v_exp_f32 v155, v155\n"
    "v_smfmac_f32_32x32x32_bf16 v[0:15], v[220:223], v[136:143], v78\n"
    "v_exp_f32 v156, v156\n"
    "v_exp_f32 v157, v157\n"
    "v_exp_f32 v158, v158\n"
    "v_exp_f32 v159, v159\n"
    "v_mfma_f32_32x32x16_bf16 v[176:191], v[208:211], v[104:107], 0\n"
    "s_add_u32 s28, s28, 28672\n"
    "v_exp_f32 v160, v160\n"
    "v_exp_f32 v161, v161\n"
    "v_exp_f32 v162, v162\n"
    "v_exp_f32 v163, v163\n"
    "v_mfma_f32_32x32x16_bf16 v[176:191], v[212:215], v[108:111], v[176:191]\n"
    "s_add_u32 s29, s29, 28672\n"
    "v_exp_f32 v164, v164\n"
    "v_exp_f32 v165, v165\n"
    "v_exp_f32 v166, v166\n"
    "v_exp_f32 v167, v167\n"
    "v_smfmac_f32_32x32x32_bf16 v[16:31], v[216:219], v[144:151], v78\n"
    "v_exp_f32 v168, v168\n"
    "v_exp_f32 v169, v169\n"
    "v_exp_f32 v170, v170\n"
    "v_exp_f32 v171, v171\n"
    "v_smfmac_f32_32x32x32_bf16 v[16:31], v[220:223], v[152:159], v78\n"
    "s_waitcnt vmcnt(8)\n"
    "v_exp_f32 v172, v172\n"
    "v_exp_f32 v173, v173\n"
    "v_exp_f32 v174, v174\n"
    "v_exp_f32 v175, v175\n"
    "v_mfma_f32_32x32x16_bf16 v[128:143], v[224:227], v[80:83], 0\n"
    "v_exp_f32 v176, v176\n"
    "v_exp_f32 v177, v177\n"
    "v_exp_f32 v178, v178\n"
    "v_exp_f32 v179, v179\n"
    "v_mfma_f32_32x32x16_bf16 v[128:143], v[228:231], v[84:87], v[128:143]\n"
    "v_exp_f32 v180, v180\n"
    "v_exp_f32 v181, v181\n"
    "v_exp_f32 v182, v182\n"
    "v_exp_f32 v183, v183\n"
    "v_smfmac_f32_32x32x32_bf16 v[32:47], v[216:219], v[160:167], v78\n"
    "v_exp_f32 v184, v184\n"
    "v_exp_f32 v185, v185\n"
    "v_exp_f32 v186, v186\n"
    "v_exp_f32 v187, v187\n"
    "v_smfmac_f32_32x32x32_bf16 v[32:47], v[220:223], v[168:175], v78\n"
    "v_exp_f32 v188, v188\n"
    "v_exp_f32 v189, v189\n"
    "v_exp_f32 v190, v190\n"
    "v_exp_f32 v191, v191\n"
    "v_mfma_f32_32x32x16_bf16 v[144:159], v[224:227], v[88:91], 0\n"
    "v_exp_f32 v128, v128\n"
    "v_exp_f32 v129, v129\n"
    "v_exp_f32 v130, v130\n"
    "v_exp_f32 v131, v131\n"
    "v_mfma_f32_32x32x16_bf16 v[144:159], v[228:231], v[92:95], v[144:159]\n"
    "v_exp_f32 v132, v132\n"
    "v_exp_f32 v133, v133\n"
    "v_exp_f32 v134, v134\n"
    "v_exp_f32 v135, v135\n"
    "v_smfmac_f32_32x32x32_bf16 v[48:63], v[216:219], v[176:183], v78\n"
    "buffer_load_dwordx4 v[208:211], v71, s[16:19], s28 offen offset:0 sc1\n"
    "v_exp_f32 v136, v136\n"
    "v_exp_f32 v137, v137\n"
    "v_exp_f32 v138, v138\n"
    "v_exp_f32 v139, v139\n"
    "v_smfmac_f32_32x32x32_bf16 v[48:63], v[220:223], v[184:191], v78\n"
    "buffer_load_dwordx4 v[212:215], v71, s[16:19], s28 offen offset:1024 sc1\n"
    "v_exp_f32 v140, v140\n"
    "v_exp_f32 v141, v141\n"
    "v_exp_f32 v142, v142\n"
    "v_exp_f32 v143, v143\n"
    "v_mfma_f32_32x32x16_bf16 v[160:175], v[224:227], v[96:99], 0\n"
    "v_exp_f32 v144, v144\n"
    "v_exp_f32 v145, v145\n"
    "v_exp_f32 v146, v146\n"
    "v_exp_f32 v147, v147\n"
    "v_mfma_f32_32x32x16_bf16 v[160:175], v[228:231], v[100:103], v[160:175]\n"
    "v_exp_f32 v148, v148\n"
    "v_exp_f32 v149, v149\n"
    "v_exp_f32 v150, v150\n"
    "v_exp_f32 v151, v151\n"
    "v_smfmac_f32_32x32x32_bf16 v[0:15], v[232:235], v[128:135], v78\n"
    "s_mov_b64 exec, s[44:45]\n"
    "buffer_load_dwordx4 v[216:219], v72, s[16:19], s28 offen sc1\n"
    "buffer_load_dwordx4 v[220:223], v72, s[16:19], s29 offen sc1\n"
    "s_mov_b64 exec, -1\n"
    "v_exp_f32 v152, v152\n"
    "v_exp_f32 v153, v153\n"
    "v_exp_f32 v154, v154\n"
    "v_exp_f32 v155, v155\n"
    "v_smfmac_f32_32x32x32_bf16 v[0:15], v[236:239], v[136:143], v78\n"
    "v_exp_f32 v156, v156\n"
    "v_exp_f32 v157, v157\n"
    "v_exp_f32 v158, v158\n"
    "v_exp_f32 v159, v159\n"
    "v_mfma_f32_32x32x16_bf16 v[176:191], v[224:227], v[104:107], 0\n"
    "s_add_u32 s28, s28, 430080\n"
    "v_exp_f32 v160, v160\n"
    "v_exp_f32 v161, v161\n"
    "v_exp_f32 v162, v162\n"
    "v_exp_f32 v163, v163\n"
    "v_mfma_f32_32x32x16_bf16 v[176:191], v[228:231], v[108:111], v[176:191]\n"
    "s_add_u32 s29, s29, 430080\n"
    "v_exp_f32 v164, v164\n"
    "v_exp_f32 v165, v165\n"
    "v_exp_f32 v166, v166\n"
    "v_exp_f32 v167, v167\n"
    "v_smfmac_f32_32x32x32_bf16 v[16:31], v[232:235], v[144:151], v78\n"
    "v_exp_f32 v168, v168\n"
    "v_exp_f32 v169, v169\n"
    "v_exp_f32 v170, v170\n"
    "v_exp_f32 v171, v171\n"
    "v_smfmac_f32_32x32x32_bf16 v[16:31], v[236:239], v[152:159], v78\n"
    "s_waitcnt vmcnt(8)\n"
    "v_exp_f32 v172, v172\n"
    "v_exp_f32 v173, v173\n"
    "v_exp_f32 v174, v174\n"
    "v_exp_f32 v175, v175\n"
    "v_mfma_f32_32x32x16_bf16 v[128:143], v[240:243], v[80:83], 0\n"
    "v_exp_f32 v176, v176\n"
    "v_exp_f32 v177, v177\n"
    "v_exp_f32 v178, v178\n"
    "v_exp_f32 v179, v179\n"
    "v_mfma_f32_32x32x16_bf16 v[128:143], v[244:247], v[84:87], v[128:143]\n"
    "v_exp_f32 v180, v180\n"
    "v_exp_f32 v181, v181\n"
    "v_exp_f32 v182, v182\n"
    "v_exp_f32 v183, v183\n"
    "v_smfmac_f32_32x32x32_bf16 v[32:47], v[232:235], v[160:167], v78\n"
    "v_exp_f32 v184, v184\n"
    "v_exp_f32 v185, v185\n"
    "v_exp_f32 v186, v186\n"
    "v_exp_f32 v187, v187\n"
    "v_smfmac_f32_32x32x32_bf16 v[32:47], v[236:239], v[168:175], v78\n"
    "v_exp_f32 v188, v188\n"
    "v_exp_f32 v189, v189\n"
    "v_exp_f32 v190, v190\n"
    "v_exp_f32 v191, v191\n"
    "v_mfma_f32_32x32x16_bf16 v[144:159], v[240:243], v[88:91], 0\n"
    "v_exp_f32 v128, v128\n"
    "v_exp_f32 v129, v129\n"
    "v_exp_f32 v130, v130\n"
    "v_exp_f32 v131, v131\n"
    "v_mfma_f32_32x32x16_bf16 v[144:159], v[244:247], v[92:95], v[144:159]\n"
    "v_exp_f32 v132, v132\n"
    "v_exp_f32 v133, v133\n"
    "v_exp_f32 v134, v134\n"
    "v_exp_f32 v135, v135\n"
    "v_smfmac_f32_32x32x32_bf16 v[48:63], v[232:235], v[176:183], v78\n"
    "buffer_load_dwordx4 v[224:227], v71, s[16:19], s28 offen offset:0 sc1\n"
    "v_exp_f32 v136, v136\n"
    "v_exp_f32 v137, v137\n"
    "v_exp_f32 v138, v138\n"
    "v_exp_f32 v139, v139\n"
    "v_smfmac_f32_32x32x32_bf16 v[48:63], v[236:239], v[184:191], v78\n"
    "buffer_load_dwordx4 v[228:231], v71, s[16:19], s28 offen offset:1024 sc1\n"
    "v_exp_f32 v140, v140\n"
    "v_exp_f32 v141, v141\n"
    "v_exp_f32 v142, v142\n"
    "v_exp_f32 v143, v143\n"
    "v_mfma_f32_32x32x16_bf16 v[160:175], v[240:243], v[96:99], 0\n"
    "v_exp_f32 v144, v144\n"
    "v_exp_f32 v145, v145\n"
    "v_exp_f32 v146, v146\n"
    "v_exp_f32 v147, v147\n"
    "v_mfma_f32_32x32x16_bf16 v[160:175], v[244:247], v[100:103], v[160:175]\n"
    "v_exp_f32 v148, v148\n"
    "v_exp_f32 v149, v149\n"
    "v_exp_f32 v150, v150\n"
    "v_exp_f32 v151, v151\n"
    "v_smfmac_f32_32x32x32_bf16 v[0:15], v[248:251], v[128:135], v78\n"
    "s_mov_b64 exec, s[44:45]\n"
    "buffer_load_dwordx4 v[232:235], v72, s[16:19], s28 offen sc1\n"
    "buffer_load_dwordx4 v[236:239], v72, s[16:19], s29 offen sc1\n"
    "s_mov_b64 exec, -1\n"
    "v_exp_f32 v152, v152\n"
    "v_exp_f32 v153, v153\n"
    "v_exp_f32 v154, v154\n"
    "v_exp_f32 v155, v155\n"
    "v_smfmac_f32_32x32x32_bf16 v[0:15], v[252:255], v[136:143], v78\n"
    "v_exp_f32 v156, v156\n"
    "v_exp_f32 v157, v157\n"
    "v_exp_f32 v158, v158\n"
    "v_exp_f32 v159, v159\n"
    "v_mfma_f32_32x32x16_bf16 v[176:191], v[240:243], v[104:107], 0\n"
    "s_add_u32 s28, s28, 28672\n"
    "v_exp_f32 v160, v160\n"
    "v_exp_f32 v161, v161\n"
    "v_exp_f32 v162, v162\n"
    "v_exp_f32 v163, v163\n"
    "v_mfma_f32_32x32x16_bf16 v[176:191], v[244:247], v[108:111], v[176:191]\n"
    "s_add_u32 s29, s29, 28672\n"
    "v_exp_f32 v164, v164\n"
    "v_exp_f32 v165, v165\n"
    "v_exp_f32 v166, v166\n"
    "v_exp_f32 v167, v167\n"
    "v_smfmac_f32_32x32x32_bf16 v[16:31], v[248:251], v[144:151], v78\n"
    "v_exp_f32 v168, v168\n"
    "v_exp_f32 v169, v169\n"
    "v_exp_f32 v170, v170\n"
    "v_exp_f32 v171, v171\n"
    "v_smfmac_f32_32x32x32_bf16 v[16:31], v[252:255], v[152:159], v78\n"
    "s_waitcnt vmcnt(8)\n"
    "v_exp_f32 v172, v172\n"
    "v_exp_f32 v173, v173\n"
    "v_exp_f32 v174, v174\n"
    "v_exp_f32 v175, v175\n"
    "v_mfma_f32_32x32x16_bf16 v[128:143], v[192:195], v[80:83], 0\n"
    "v_exp_f32 v176, v176\n"
    "v_exp_f32 v177, v177\n"
    "v_exp_f32 v178, v178\n"
    "v_exp_f32 v179, v179\n"
    "v_mfma_f32_32x32x16_bf16 v[128:143], v[196:199], v[84:87], v[128:143]\n"
    "v_exp_f32 v180, v180\n"
    "v_exp_f32 v181, v181\n"
    "v_exp_f32 v182, v182\n"
    "v_exp_f32 v183, v183\n"
    "v_smfmac_f32_32x32x32_bf16 v[32:47], v[248:251], v[160:167], v78\n"
    "v_exp_f32 v184, v184\n"
    "v_exp_f32 v185, v185\n"
    "v_exp_f32 v186, v186\n"
    "v_exp_f32 v187, v187\n"
    "v_smfmac_f32_32x32x32_bf16 v[32:47], v[252:255], v[168:175], v78\n"
    "v_exp_f32 v188, v188\n"
    "v_exp_f32 v189, v189\n"
    "v_exp_f32 v190, v190\n"
    "v_exp_f32 v191, v191\n"
    "v_mfma_f32_32x32x16_bf16 v[144:159], v[192:195], v[88:91], 0\n"
    "v_exp_f32 v128, v128\n"
    "v_exp_f32 v129, v129\n"
    "v_exp_f32 v130, v130\n"
    "v_exp_f32 v131, v131\n"
    "v_mfma_f32_32x32x16_bf16 v[144:159], v[196:199], v[92:95], v[144:159]\n"
    "v_exp_f32 v132, v132\n"
    "v_exp_f32 v133, v133\n"
    "v_exp_f32 v134, v134\n"
    "v_exp_f32 v135, v135\n"
    "v_smfmac_f32_32x32x32_bf16 v[48:63], v[248:251], v[176:183], v78\n"
    "buffer_load_dwordx4 v[240:243], v71, s[16:19], s28 offen offset:0 sc1\n"
    "v_exp_f32 v136, v136\n"
    "v_exp_f32 v137, v137\n"
    "v_exp_f32 v138, v138\n"
    "v_exp_f32 v139, v139\n"
    "v_smfmac_f32_32x32x32_bf16 v[48:63], v[252:255], v[184:191], v78\n"
    "buffer_load_dwordx4 v[244:247], v71, s[16:19], s28 offen offset:1024 sc1\n"
    "v_exp_f32 v140, v140\n"
    "v_exp_f32 v141, v141\n"
    "v_exp_f32 v142, v142\n"
    "v_exp_f32 v143, v143\n"
    "v_mfma_f32_32x32x16_bf16 v[160:175], v[192:195], v[96:99], 0\n"
    "v_exp_f32 v144, v144\n"
    "v_exp_f32 v145, v145\n"
    "v_exp_f32 v146, v146\n"
    "v_exp_f32 v147, v147\n"
    "v_mfma_f32_32x32x16_bf16 v[160:175], v[196:199], v[100:103], v[160:175]\n"
    "v_exp_f32 v148, v148\n"
    "v_exp_f32 v149, v149\n"
    "v_exp_f32 v150, v150\n"
    "v_exp_f32 v151, v151\n"
    "v_smfmac_f32_32x32x32_bf16 v[0:15], v[200:203], v[128:135], v78\n"
    "s_mov_b64 exec, s[44:45]\n"
    "buffer_load_dwordx4 v[248:251], v72, s[16:19], s28 offen sc1\n"
    "buffer_load_dwordx4 v[252:255], v72, s[16:19], s29 offen sc1\n"
    "s_mov_b64 exec, -1\n"
    "v_exp_f32 v152, v152\n"
    "v_exp_f32 v153, v153\n"
    "v_exp_f32 v154, v154\n"
    "v_exp_f32 v155, v155\n"
    "v_smfmac_f32_32x32x32_bf16 v[0:15], v[204:207], v[136:143], v78\n"
    "v_exp_f32 v156, v156\n"
    "v_exp_f32 v157, v157\n"
    "v_exp_f32 v158, v158\n"
    "v_exp_f32 v159, v159\n"
    "v_mfma_f32_32x32x16_bf16 v[176:191], v[192:195], v[104:107], 0\n"
    "v_exp_f32 v160, v160\n"
    "v_exp_f32 v161, v161\n"
    "v_exp_f32 v162, v162\n"
    "v_exp_f32 v163, v163\n"
    "v_mfma_f32_32x32x16_bf16 v[176:191], v[196:199], v[108:111], v[176:191]\n"
    "v_exp_f32 v164, v164\n"
    "v_exp_f32 v165, v165\n"
    "v_exp_f32 v166, v166\n"
    "v_exp_f32 v167, v167\n"
    "v_smfmac_f32_32x32x32_bf16 v[16:31], v[200:203], v[144:151], v78\n"
    "v_exp_f32 v168, v168\n"
    "v_exp_f32 v169, v169\n"
    "v_exp_f32 v170, v170\n"
    "v_exp_f32 v171, v171\n"
    "v_smfmac_f32_32x32x32_bf16 v[16:31], v[204:207], v[152:159], v78\n"
    "s_waitcnt vmcnt(8)\n"
    "v_exp_f32 v172, v172\n"
    "v_exp_f32 v173, v173\n"
    "v_exp_f32 v174, v174\n"
    "v_exp_f32 v175, v175\n"
    "v_mfma_f32_32x32x16_bf16 v[128:143], v[208:211], v[80:83], 0\n"
    "v_exp_f32 v176, v176\n"
    "v_exp_f32 v177, v177\n"
    "v_exp_f32 v178, v178\n"
    "v_exp_f32 v179, v179\n"
    "v_mfma_f32_32x32x16_bf16 v[128:143], v[212:215], v[84:87], v[128:143]\n"
    "v_exp_f32 v180, v180\n"
    "v_exp_f32 v181, v181\n"
    "v_exp_f32 v182, v182\n"
    "v_exp_f32 v183, v183\n"
    "v_smfmac_f32_32x32x32_bf16 v[32:47], v[200:203], v[160:167], v78\n"
    "v_exp_f32 v184, v184\n"
    "v_exp_f32 v185, v185\n"
    "v_exp_f32 v186, v186\n"
    "v_exp_f32 v187, v187\n"
    "v_smfmac_f32_32x32x32_bf16 v[32:47], v[204:207], v[168:175], v78\n"
    "s_waitcnt vmcnt(4)\n"
    "v_exp_f32 v188, v188\n"
    "v_exp_f32 v189, v189\n"
    "v_exp_f32 v190, v190\n"
    "v_exp_f32 v191, v191\n"
    "v_mfma_f32_32x32x16_bf16 v[144:159], v[224:227], v[80:83], 0\n"
    "v_exp_f32 v128, v128\n"
    "v_exp_f32 v129, v129\n"
    "v_exp_f32 v130, v130\n"
    "v_exp_f32 v131, v131\n"
    "v_mfma_f32_32x32x16_bf16 v[144:159], v[228:231], v[84:87], v[144:159]\n"
    "v_exp_f32 v132, v132\n"
    "v_exp_f32 v133, v133\n"
    "v_exp_f32 v134, v134\n"
    "v_exp_f32 v135, v135\n"
    "v_smfmac_f32_32x32x32_bf16 v[48:63], v[200:203], v[176:183], v78\n"
    "v_exp_f32 v136, v136\n"
    "v_exp_f32 v137, v137\n"
    "v_exp_f32 v138, v138\n"
    "v_exp_f32 v139, v139\n"
    "v_smfmac_f32_32x32x32_bf16 v[48:63], v[204:207], v[184:191], v78\n"
    "s_waitcnt vmcnt(0)\n"
    "v_exp_f32 v140, v140\n"
    "v_exp_f32 v141, v141\n"
    "v_exp_f32 v142, v142\n"
    "v_exp_f32 v143, v143\n"
    "v_mfma_f32_32x32x16_bf16 v[160:175], v[240:243], v[80:83], 0\n"
    "v_exp_f32 v144, v144\n"
    "v_exp_f32 v145, v145\n"
    "v_exp_f32 v146, v146\n"
    "v_exp_f32 v147, v147\n"
    "v_mfma_f32_32x32x16_bf16 v[160:175], v[244:247], v[84:87], v[160:175]\n"
    "v_exp_f32 v148, v148\n"
    "v_exp_f32 v149, v149\n"
    "v_exp_f32 v150, v150\n"
    "v_exp_f32 v151, v151\n"
    "v_smfmac_f32_32x32x32_bf16 v[0:15], v[216:219], v[128:135], v78\n"
    "ds_read_b32 v76, v75 offset:104\n"
    "v_exp_f32 v152, v152\n"
    "v_exp_f32 v153, v153\n"
    "v_exp_f32 v154, v154\n"
    "v_exp_f32 v155, v155\n"
    "v_smfmac_f32_32x32x32_bf16 v[0:15], v[220:223], v[136:143], v78\n"
    "v_exp_f32 v156, v156\n"
    "v_exp_f32 v157, v157\n"
    "v_exp_f32 v158, v158\n"
    "v_exp_f32 v159, v159\n"
    "v_mfma_f32_32x32x16_bf16 v[176:191], v[208:211], v[88:91], 0\n"
    "v_exp_f32 v160, v160\n"
    "v_exp_f32 v161, v161\n"
    "v_exp_f32 v162, v162\n"
    "v_exp_f32 v163, v163\n"
    "v_mfma_f32_32x32x16_bf16 v[176:191], v[212:215], v[92:95], v[176:191]\n"
    "v_exp_f32 v164, v164\n"
    "v_exp_f32 v165, v165\n"
    "v_exp_f32 v166, v166\n"
    "v_exp_f32 v167, v167\n"
    "v_smfmac_f32_32x32x32_bf16 v[0:15], v[232:235], v[144:151], v78\n"
    "s_waitcnt lgkmcnt(0)\n"
    "v_cmp_gt_u32 vcc, 8, v76\n"
    "s_nop 0\n"
    "s_cbranch_vccnz .Lb_sl77_%=\n"
    ".Lb_ok76_%=:\n"
    "v_exp_f32 v168, v168\n"
    "v_exp_f32 v169, v169\n"
    "v_exp_f32 v170, v170\n"
    "v_exp_f32 v171, v171\n"
    "v_smfmac_f32_32x32x32_bf16 v[0:15], v[236:239], v[152:159], v78\n"
    "v_exp_f32 v172, v172\n"
    "v_exp_f32 v173, v173\n"
    "v_exp_f32 v174, v174\n"
    "v_exp_f32 v175, v175\n"
    "v_mfma_f32_32x32x16_bf16 v[128:143], v[224:227], v[88:91], 0\n"
    "v_exp_f32 v176, v176\n"
    "v_exp_f32 v177, v177\n"
    "v_exp_f32 v178, v178\n"
    "v_exp_f32 v179, v179\n"
    "v_mfma_f32_32x32x16_bf16 v[128:143], v[228:231], v[92:95], v[128:143]\n"
    "v_exp_f32 v180, v180\n"
    "v_exp_f32 v181, v181\n"
    "v_exp_f32 v182, v182\n"
    "v_exp_f32 v183, v183\n"
    "v_smfmac_f32_32x32x32_bf16 v[0:15], v[248:251], v[160:167], v78\n"
    "global_load_dwordx4 v[192:195], v119, s[90:91]\n"
    "v_exp_f32 v184, v184\n"
    "v_exp_f32 v185, v185\n"
    "v_exp_f32 v186, v186\n"
    "v_exp_f32 v187, v187\n"
    "v_smfmac_f32_32x32x32_bf16 v[0:15], v[252:255], v[168:175], v78\n"
    "global_load_dwordx4 v[196:199], v120, s[88:89]\n"
    "v_exp_f32 v188, v188\n"
    "v_exp_f32 v189, v189\n"
    "v_exp_f32 v190, v190\n"
    "v_exp_f32 v191, v191\n"
    "v_mfma_f32_32x32x16_bf16 v[144:159], v[240:243], v[88:91], 0\n"
    "s_sub_u32 m0, s94, 53248\n"
    "v_exp_f32 v128, v128\n"
    "v_exp_f32 v129, v129\n"
    "v_exp_f32 v130, v130\n"
    "v_exp_f32 v131, v131\n"
    "v_mfma_f32_32x32x16_bf16 v[144:159], v[244:247], v[92:95], v[144:159]\n"
    "global_load_dwordx4 v[200:203], v121, s[88:89]\n"
    "v_exp_f32 v132, v132\n"
    "v_exp_f32 v133, v133\n"
    "v_exp_f32 v134, v134\n"
    "v_exp_f32 v135, v135\n"
    "v_smfmac_f32_32x32x32_bf16 v[16:31], v[216:219], v[176:183], v78\n"
    "ds_write_addtid_b32 v0 offset:53248\n"
    "ds_write_addtid_b32 v1 offset:53504\n"
    "v_exp_f32 v136, v136\n"
    "v_exp_f32 v137, v137\n"
    "v_exp_f32 v138, v138\n"
    "v_exp_f32 v139, v139\n"
    "v_smfmac_f32_32x32x32_bf16 v[16:31], v[220:223], v[184:191], v78\n"
    "ds_write_addtid_b32 v2 offset:53760\n"
    "ds_write_addtid_b32 v3 offset:54016\n"
    "v_exp_f32 v140, v140\n"
    "v_exp_f32 v141, v141\n"
    "v_exp_f32 v142, v142\n"
    "v_exp_f32 v143, v143\n"
    "v_mfma_f32_32x32x16_bf16 v[160:175], v[208:211], v[96:99], 0\n"
    "ds_write_addtid_b32 v4 offset:54272\n"
    "v_exp_f32 v144, v144\n"
    "v_exp_f32 v145, v145\n"
    "v_exp_f32 v146, v146\n"
    "v_exp_f32 v147, v147\n"
    "v_mfma_f32_32x32x16_bf16 v[160:175], v[212:215], v[100:103], v[160:175]\n"
    "ds_write_addtid_b32 v5 offset:54528\n"
    "v_exp_f32 v148, v148\n"
    "v_exp_f32 v149, v149\n"
    "v_exp_f32 v150, v150\n"
    "v_exp_f32 v151, v151\n"
    "v_smfmac_f32_32x32x32_bf16 v[16:31], v[232:235], v[128:135], v78\n"
    "ds_write_addtid_b32 v6 offset:54784\n"
    "ds_write_addtid_b32 v7 offset:55040\n"
    "v_exp_f32 v152, v152\n"
    "v_exp_f32 v153, v153\n"
    "v_exp_f32 v154, v154\n"
    "v_exp_f32 v155, v155\n"
    "v_smfmac_f32_32x32x32_bf16 v[16:31], v[236:239], v[136:143], v78\n"
    "ds_write_addtid_b32 v8 offset:55296\n"
    "ds_write_addtid_b32 v9 offset:55552\n"
    "v_exp_f32 v156, v156\n"
    "v_exp_f32 v157, v157\n"
    "v_exp_f32 v158, v158\n"
    "v_exp_f32 v159, v159\n"
    "v_mfma_f32_32x32x16_bf16 v[176:191], v[224:227], v[96:99], 0\n"
    "ds_write_addtid_b32 v10 offset:55808\n"
    "v_exp_f32 v160, v160\n"
    "v_exp_f32 v161, v161\n"
    "v_exp_f32 v162, v162\n"
    "v_exp_f32 v163, v163\n"
    "v_mfma_f32_32x32x16_bf16 v[176:191], v[228:231], v[100:103], v[176:191]\n"
    "ds_write_addtid_b32 v11 offset:56064\n"
    "v_exp_f32 v164, v164\n"
    "v_exp_f32 v165, v165\n"
    "v_exp_f32 v166, v166\n"
    "v_exp_f32 v167, v167\n"
    "v_smfmac_f32_32x32x32_bf16 v[16:31], v[248:251], v[144:151], v78\n"
    "ds_write_addtid_b32 v12 offset:56320\n"
    "ds_write_addtid_b32 v13 offset:56576\n"
    "v_exp_f32 v168, v168\n"
    "v_exp_f32 v169, v169\n"
    "v_exp_f32 v170, v170\n"
    "v_exp_f32 v171, v171\n"
    "v_smfmac_f32_32x32x32_bf16 v[16:31], v[252:255], v[152:159], v78\n"
    "ds_write_addtid_b32 v14 offset:56832\n"
    "ds_write_addtid_b32 v15 offset:57088\n"
    "v_exp_f32 v172, v172\n"
    "v_exp_f32 v173, v173\n"
    "v_exp_f32 v174, v174\n"
    "v_exp_f32 v175, v175\n"
    "v_mfma_f32_32x32x16_bf16 v[128:143], v[240:243], v[96:99], 0\n"
    "s_sub_u32 m0, s95, 53248\n"
    "v_exp_f32 v176, v176\n"
    "v_exp_f32 v177, v177\n"
    "v_exp_f32 v178, v178\n"
    "v_exp_f32 v179, v179\n"
    "v_mfma_f32_32x32x16_bf16 v[128:143], v[244:247], v[100:103], v[128:143]\n"
    "v_exp_f32 v180, v180\n"
    "v_exp_f32 v181, v181\n"
    "v_exp_f32 v182, v182\n"
    "v_exp_f32 v183, v183\n"
    "v_smfmac_f32_32x32x32_bf16 v[32:47], v[216:219], v[160:167], v78\n"
    "ds_write_addtid_b32 v16 offset:57344\n"
    "ds_write_addtid_b32 v17 offset:57600\n"
    "v_exp_f32 v184, v184\n"
    "v_exp_f32 v185, v185\n"
    "v_exp_f32 v186, v186\n"
    "v_exp_f32 v187, v187\n"
    "v_smfmac_f32_32x32x32_bf16 v[32:47], v[220:223], v[168:175], v78\n"
    "ds_write_addtid_b32 v18 offset:57856\n"
    "ds_write_addtid_b32 v19 offset:58112\n"
    "v_exp_f32 v188, v188\n"
    "v_exp_f32 v189, v189\n"
    "v_exp_f32 v190, v190\n"
    "v_exp_f32 v191, v191\n"
    "v_mfma_f32_32x32x16_bf16 v[144:159], v[208:211], v[104:107], 0\n"
    "ds_write_addtid_b32 v20 offset:58368\n"
    "v_exp_f32 v128, v128\n"
    "v_exp_f32 v129, v129\n"
    "v_exp_f32 v130, v130\n"
    "v_exp_f32 v131, v131\n"
    "v_mfma_f32_32x32x16_bf16 v[144:159], v[212:215], v[108:111], v[144:159]\n"
    "ds_write_addtid_b32 v21 offset:58624\n"
    "v_exp_f32 v132, v132\n"
    "v_exp_f32 v133, v133\n"
    "v_exp_f32 v134, v134\n"
    "v_exp_f32 v135, v135\n"
    "v_smfmac_f32_32x32x32_bf16 v[32:47], v[232:235], v[176:183], v78\n"
    "ds_write_addtid_b32 v22 offset:58880\n"
    "ds_write_addtid_b32 v23 offset:59136\n"
    "v_exp_f32 v136, v136\n"
    "v_exp_f32 v137, v137\n"
    "v_exp_f32 v138, v138\n"
    "v_exp_f32 v139, v139\n"
    "v_smfmac_f32_32x32x32_bf16 v[32:47], v[236:239], v[184:191], v78\n"
    "ds_write_addtid_b32 v24 offset:59392\n"
    "ds_write_addtid_b32 v25 offset:59648\n"
    "v_exp_f32 v140, v140\n"
    "v_exp_f32 v141, v141\n"
    "v_exp_f32 v142, v142\n"
    "v_exp_f32 v143, v143\n"
    "v_mfma_f32_32x32x16_bf16 v[160:175], v[224:227], v[104:107], 0\n"
    "ds_write_addtid_b32 v26 offset:59904\n"
    "v_exp_f32 v144, v144\n"
    "v_exp_f32 v145, v145\n"
    "v_exp_f32 v146, v146\n"
    "v_exp_f32 v147, v147\n"
    "v_mfma_f32_32x32x16_bf16 v[160:175], v[228:231], v[108:111], v[160:175]\n"
    "ds_write_addtid_b32 v27 offset:60160\n"
    "v_exp_f32 v148, v148\n"
    "v_exp_f32 v149, v149\n"
    "v_exp_f32 v150, v150\n"
    "v_exp_f32 v151, v151\n"
    "v_smfmac_f32_32x32x32_bf16 v[32:47], v[248:251], v[128:135], v78\n"
    "ds_write_addtid_b32 v28 offset:60416\n"
    "ds_write_addtid_b32 v29 offset:60672\n"
    "v_exp_f32 v152, v152\n"
    "v_exp_f32 v153, v153\n"
    "v_exp_f32 v154, v154\n"
    "v_exp_f32 v155, v155\n"
    "v_smfmac_f32_32x32x32_bf16 v[32:47], v[252:255], v[136:143], v78\n"
    "ds_write_addtid_b32 v30 offset:60928\n"
    "ds_write_addtid_b32 v31 offset:61184\n"
    "v_exp_f32 v156, v156\n"
    "v_exp_f32 v157, v157\n"
    "v_exp_f32 v158, v158\n"
    "v_exp_f32 v159, v159\n"
    "v_mfma_f32_32x32x16_bf16 v[176:191], v[240:243], v[104:107], 0\n"
    "s_sub_u32 m0, s96, 53248\n"
    "v_exp_f32 v160, v160\n"
    "v_exp_f32 v161, v161\n"
    "v_exp_f32 v162, v162\n"
    "v_exp_f32 v163, v163\n"
    "v_mfma_f32_32x32x16_bf16 v[176:191], v[244:247], v[108:111], v[176:191]\n"
    "v_exp_f32 v164, v164\n"
    "v_exp_f32 v165, v165\n"
    "v_exp_f32 v166, v166\n"
    "v_exp_f32 v167, v167\n"
    "v_smfmac_f32_32x32x32_bf16 v[48:63], v[216:219], v[144:151], v78\n"
    "ds_write_addtid_b32 v32 offset:61440\n"
    "ds_write_addtid_b32 v33 offset:61696\n"
    "v_exp_f32 v168, v168\n"
    "v_exp_f32 v169, v169\n"
    "v_exp_f32 v170, v170\n"
    "v_exp_f32 v171, v171\n"
    "v_smfmac_f32_32x32x32_bf16 v[48:63], v[220:223], v[152:159], v78\n"
    "ds_write_addtid_b32 v34 offset:61952\n"
    "ds_write_addtid_b32 v35 offset:62208\n"
    "v_exp_f32 v172, v172\n"
    "v_exp_f32 v173, v173\n"
    "v_exp_f32 v174, v174\n"
    "v_exp_f32 v175, v175\n"
    "v_exp_f32 v176, v176\n"
    "v_exp_f32 v177, v177\n"
    "v_smfmac_f32_32x32x32_bf16 v[48:63], v[232:235], v[160:167], v78\n"
    "ds_write_addtid_b32 v36 offset:62464\n"
    "ds_write_addtid_b32 v37 offset:62720\n"
    "v_exp_f32 v178, v178\n"
    "v_exp_f32 v179, v179\n"
    "v_exp_f32 v180, v180\n"
    "v_exp_f32 v181, v181\n"
    "v_smfmac_f32_32x32x32_bf16 v[48:63], v[236:239], v[168:175], v78\n"
    "ds_write_addtid_b32 v38 offset:62976\n"
    "ds_write_addtid_b32 v39 offset:63232\n"
    "v_exp_f32 v182, v182\n"
    "v_exp_f32 v183, v183\n"
    "v_exp_f32 v184, v184\n"
    "v_exp_f32 v185, v185\n"
    "v_exp_f32 v186, v186\n"
    "v_smfmac_f32_32x32x32_bf16 v[48:63], v[248:251], v[176:183], v78\n"
    "ds_write_addtid_b32 v40 offset:63488\n"
    "ds_write_addtid_b32 v41 offset:63744\n"
    "v_exp_f32 v187, v187\n"
    "v_exp_f32 v188, v188\n"
    "v_exp_f32 v189, v189\n"
    "v_exp_f32 v190, v190\n"
    "v_exp_f32 v191, v191\n"
    "s_setprio 2\n"
    "ds_write_addtid_b32 v42 offset:64000\n"
    "v_smfmac_f32_32x32x32_bf16 v[48:63], v[252:255], v[184:191], v78\n"
    "ds_write_addtid_b32 v43 offset:64256\n"
    "ds_write_addtid_b32 v44 offset:64512\n"
    "ds_write_addtid_b32 v45 offset:64768\n"
    "ds_write_addtid_b32 v46 offset:65024\n"
    "ds_write_addtid_b32 v47 offset:65280\n"
    "s_waitcnt lgkmcnt(0)\n"
    "s_barrier\n"
    "buffer_load_dword v124, off, s[16:19], s35 offset:2044 sc1\n"
    "buffer_load_dword v125, off, s[16:19], s35 offset:3836 sc1\n"
    "buffer_load_dword v126, off, s[16:19], s27 offset:1532 sc1\n"
    "buffer_load_dword v127, off, s[16:19], s27 offset:3324 sc1\n"
    "ds_read2st64_b32 v[140:141], v70 offset0:12 offset1:13\n"
    "ds_read2st64_b32 v[156:157], v70 offset0:28 offset1:29\n"
    "ds_read2st64_b32 v[172:173], v70 offset0:44 offset1:45\n"
    "ds_read2st64_b32 v[142:143], v70 offset0:14 offset1:15\n"
    "ds_read2st64_b32 v[158:159], v70 offset0:30 offset1:31\n"
    "ds_read2st64_b32 v[174:175], v70 offset0:46 offset1:47\n"
    "ds_read2st64_b32 v[128:129], v70 offset0:0 offset1:1\n"
    "ds_read2st64_b32 v[144:145], v70 offset0:16 offset1:17\n"
    "ds_read2st64_b32 v[160:161], v70 offset0:32 offset1:33\n"
    "ds_read2st64_b32 v[130:131], v70 offset0:2 offset1:3\n"
    "ds_read2st64_b32 v[146:147], v70 offset0:18 offset1:19\n"
    "ds_read2st64_b32 v[162:163], v70 offset0:34 offset1:35\n"
    "ds_read2st64_b32 v[132:133], v70 offset0:4 offset1:5\n"
    "ds_read2st64_b32 v[148:149], v70 offset0:20 offset1:21\n"
    "ds_read2st64_b32 v[164:165], v70 offset0:36 offset1:37\n"
    "ds_read2st64_b32 v[134:135], v70 offset0:6 offset1:7\n"
    "ds_read2st64_b32 v[150:151], v70 offset0:22 offset1:23\n"
    "ds_read2st64_b32 v[166:167], v70 offset0:38 offset1:39\n"
    "ds_read2st64_b32 v[136:137], v70 offset0:8 offset1:9\n"
    "ds_read2st64_b32 v[152:153], v70 offset0:24 offset1:25\n"
    "ds_read2st64_b32 v[168:169], v70 offset0:40 offset1:41\n"
    "ds_read2st64_b32 v[138:139], v70 offset0:10 offset1:11\n"
    "ds_read2st64_b32 v[154:155], v70 offset0:26 offset1:27\n"
    "ds_read2st64_b32 v[170:171], v70 offset0:42 offset1:43\n"
    "v_mov_b32 v77, 1\n"
    "s_mov_b64 exec, 1\n"
    "ds_add_u32 v75, v77 offset:116\n"
    "s_mov_b64 exec, -1\n"
    "s_waitcnt lgkmcnt(14)\n"
    "v_add_f32 v60, v60, v140\n"
    "v_add_f32 v61, v61, v141\n"
    "v_add_f32 v62, v62, v142\n"
    "v_add_f32 v63, v63, v143\n"
    "v_add_f32 v60, v60, v156\n"
    "v_add_f32 v61, v61, v157\n"
    "v_add_f32 v62, v62, v158\n"
    "v_add_f32 v63, v63, v159\n"
    "v_add_f32 v60, v60, v172\n"
    "v_add_f32 v61, v61, v173\n"
    "v_add_f32 v62, v62, v174\n"
    "v_add_f32 v63, v63, v175\n"
    "v_mov_b32 v0, v60\n"
    "v_mov_b32 v1, v60\n"
    "v_add_f32 v48, v48, v128\n"
    "v_add_f32 v49, v49, v129\n"
    "v_add_f32 v50, v50, v130\n"
    "v_add_f32 v51, v51, v131\n"
    "v_add_f32 v48, v48, v144\n"
    "v_add_f32 v49, v49, v145\n"
    "v_add_f32 v50, v50, v146\n"
    "v_add_f32 v51, v51, v147\n"
    "v_add_f32 v48, v48, v160\n"
    "v_add_f32 v49, v49, v161\n"
    "s_waitcnt lgkmcnt(13)\n"
    "v_add_f32 v50, v50, v162\n"
    "v_add_f32 v51, v51, v163\n"
    "v_permlane32_swap_b32 v0, v1\n"
    "s_waitcnt lgkmcnt(12)\n"
    "v_add_f32 v52, v52, v132\n"
    "v_add_f32 v53, v53, v133\n"
    "s_waitcnt lgkmcnt(9)\n"
    "v_add_f32 v54, v54, v134\n"
    "v_add_f32 v55, v55, v135\n"
    "v_add_f32 v52, v52, v148\n"
    "v_add_f32 v53, v53, v149\n"
    "s_waitcnt lgkmcnt(8)\n"
    "v_add_f32 v54, v54, v150\n"
    "v_add_f32 v55, v55, v151\n"
    "v_add_f32 v52, v52, v164\n"
    "v_add_f32 v53, v53, v165\n"
    "s_waitcnt lgkmcnt(7)\n"
    "v_add_f32 v54, v54, v166\n"
    "v_add_f32 v55, v55, v167\n"
    "v_rcp_f32 v0, v1\n"
    "v_cmp_nle_f32 vcc, 0x3a800000, v1\n"
    "s_mov_b32 s48, 0x71800000\n"
    "s_waitcnt lgkmcnt(6)\n"
    "v_add_f32 v56, v56, v136\n"
    "v_add_f32 v57, v57, v137\n"
    "s_waitcnt lgkmcnt(3)\n"
    "v_add_f32 v58, v58, v138\n"
    "v_add_f32 v59, v59, v139\n"
    "v_add_f32 v56, v56, v152\n"
    "v_add_f32 v57, v57, v153\n"
    "s_waitcnt lgkmcnt(2)\n"
    "v_add_f32 v58, v58, v154\n"
    "v_add_f32 v59, v59, v155\n"
    "v_add_f32 v56, v56, v168\n"
    "v_add_f32 v57, v57, v169\n"
    "s_waitcnt lgkmcnt(1)\n"
    "v_add_f32 v58, v58, v170\n"
    "v_add_f32 v59, v59, v171\n"
    "v_maximum3_f32 v1, v1, |v60|, |v61|\n"
    "v_maximum3_f32 v1, v1, |v62|, |v63|\n"
    "v_maximum3_f32 v1, v1, |v48|, |v49|\n"
    "v_maximum3_f32 v1, v1, |v50|, |v51|\n"
    "v_maximum3_f32 v1, v1, |v52|, |v53|\n"
    "v_maximum3_f32 v1, v1, |v54|, |v55|\n"
    "v_maximum3_f32 v1, v1, |v56|, |v57|\n"
    "v_maximum3_f32 v1, v1, |v58|, |v59|\n"
    "v_cmp_nge_f32 s[50:51], s48, v1\n"
    "s_nop 0\n"
    "s_or_b64 vcc, vcc, s[50:51]\n"
    "s_cbranch_scc1 .Lb_fb79_%=\n"
    "v_mul_f32 v48, v0, v48\n"
    "v_mul_f32 v49, v0, v49\n"
    "v_mul_f32 v50, v0, v50\n"
    "v_mul_f32 v51, v0, v51\n"
    "v_mul_f32 v52, v0, v52\n"
    "v_mul_f32 v53, v0, v53\n"
    "v_mul_f32 v54, v0, v54\n"
    "v_mul_f32 v55, v0, v55\n"
    "v_mul_f32 v56, v0, v56\n"
    "v_mul_f32 v57, v0, v57\n"
    "v_mul_f32 v58, v0, v58\n"
    "v_mul_f32 v59, v0, v59\n"
    "v_mul_f32 v60, v0, v60\n"
    "v_mul_f32 v61, v0, v61\n"
    "v_mul_f32 v62, v0, v62\n"
    "v_mul_f32 v63, v0, v63\n"
    "v_cvt_pk_bf16_f32 v8, v48, v49\n"
    "v_cvt_pk_bf16_f32 v9, v50, v51\n"
    "v_cvt_pk_bf16_f32 v10, v52, v53\n"
    "v_cvt_pk_bf16_f32 v11, v54, v55\n"
    "buffer_store_dwordx4 v[8:11], v74, s[16:19], s34 offen sc1\n"
    "v_cvt_pk_bf16_f32 v12, v56, v57\n"
    "v_cvt_pk_bf16_f32 v13, v58, v59\n"
    "v_cvt_pk_bf16_f32 v14, v60, v61\n"
    "v_cvt_pk_bf16_f32 v15, v62, v63\n"
    ".Lb_bk80_%=:\n"
    "ds_read_b32 v76, v75 offset:116\n"
    "s_mov_b32 exec_lo, 0\n"
    "v_mov_b64 v[14:15], 0\n"
    "s_mov_b32 exec_lo, -1\n"
    "buffer_store_dwordx4 v[12:15], v74, s[16:19], s34 offen offset:1024 sc1\n"
    "s_waitcnt lgkmcnt(0)\n"
    "v_cmp_gt_u32 vcc, 4, v76\n"
    "s_nop 0\n"
    "s_cbranch_vccnz .Lb_sl83_%=\n"
    ".Lb_ok82_%=:\n"
    "s_waitcnt vmcnt(5)\n"
    "v_cmp_eq_u32 s[38:39], -1, v124\n"
    "s_waitcnt vmcnt(4)\n"
    "v_cmp_eq_u32 s[40:41], -1, v125\n"
    "s_waitcnt vmcnt(3)\n"
    "v_cmp_eq_u32 s[42:43], -1, v126\n"
    "s_waitcnt vmcnt(2)\n"
    "v_cmp_eq_u32 s[44:45], -1, v127\n"
    "s_mov_b32 s4, 39\n"
    "s_not_b64 exec, s[38:39]\n"
    "s_cbranch_execz .Lb_sk91_%=\n"
    "s_add_u32 m0, s98, 0\n"
    "s_nop 0\n"
    "buffer_load_dwordx4 v74, s[16:19], s35 offen offset:0 sc1 lds\n"
    "buffer_load_dwordx4 v74, s[16:19], s35 offen offset:1024 sc1 lds\n"
    ".Lb_sk91_%=:\n"
    "s_not_b64 exec, s[40:41]\n"
    "s_cbranch_execz .Lb_sk92_%=\n"
    "s_add_u32 m0, s98, 0\n"
    "s_nop 0\n"
    "buffer_load_dwordx4 v74, s[16:19], s35 offen offset:2048 sc1 lds\n"
    "buffer_load_dwordx4 v74, s[16:19], s35 offen offset:3072 sc1 lds\n"
    ".Lb_sk92_%=:\n"
    "s_not_b64 exec, s[42:43]\n"
    "s_cbranch_execz .Lb_sk93_%=\n"
    "s_add_u32 m0, s98, 4096\n"
    "s_nop 0\n"
    "buffer_load_dwordx4 v74, s[16:19], s27 offen offset:0 sc1 lds\n"
    "buffer_load_dwordx4 v74, s[16:19], s27 offen offset:1024 sc1 lds\n"
    ".Lb_sk93_%=:\n"
    "s_not_b64 exec, s[44:45]\n"
    "s_cbranch_execz .Lb_sk94_%=\n"
    "s_add_u32 m0, s98, 4096\n"
    "s_nop 0\n"
    "buffer_load_dwordx4 v74, s[16:19], s27 offen offset:2048 sc1 lds\n"
    "buffer_load_dwordx4 v74, s[16:19], s27 offen offset:3072 sc1 lds\n"
    ".Lb_sk94_%=:\n"
    "s_mov_b64 exec, -1\n"
    "s_or_b64 vcc, s[38:39], s[40:41]\n"
    "s_or_b64 vcc, vcc, s[42:43]\n"
    "s_or_b64 vcc, vcc, s[44:45]\n"
    "s_cbranch_scc1 .Lb_pl88_%=\n"
    ".Lb_pd89_%=:\n"
    "s_mov_b32 s4, 0x40000\n"
    "s_waitcnt vmcnt(0)\n"
    ".Lb_so85_%=:\n"
    "ds_read_b128 v[16:19], v68 offset:0\n"
    "ds_read_b128 v[20:23], v68 offset:1024\n"
    "ds_read_b128 v[24:27], v68 offset:2048\n"
    "ds_read_b128 v[28:31], v68 offset:3072\n"
    "ds_read_b128 v[32:35], v68 offset:4096\n"
    "ds_read_b128 v[36:39], v68 offset:5120\n"
    "ds_read_b128 v[40:43], v68 offset:6144\n"
    "ds_read_b128 v[44:47], v68 offset:7168\n"
    "s_waitcnt lgkmcnt(7)\n"
    "v_max3_u32 v4, v16, v17, v18\n"
    "s_waitcnt lgkmcnt(6)\n"
    "v_max3_u32 v4, v4, v19, v20\n"
    "v_max3_u32 v4, v4, v21, v22\n"
    "v_max_u32 v4, v4, v23\n"
    "v_cmp_eq_u32 s[38:39], -1, v4\n"
    "s_waitcnt lgkmcnt(5)\n"
    "v_max3_u32 v5, v24, v25, v26\n"
    "s_waitcnt lgkmcnt(4)\n"
    "v_max3_u32 v5, v5, v27, v28\n"
    "v_max3_u32 v5, v5, v29, v30\n"
    "v_max_u32 v5, v5, v31\n"
    "v_cmp_eq_u32 s[40:41], -1, v5\n"
    "s_waitcnt lgkmcnt(3)\n"
    "v_max3_u32 v6, v32, v33, v34\n"
    "s_waitcnt lgkmcnt(2)\n"
    "v_max3_u32 v6, v6, v35, v36\n"
    "v_max3_u32 v6, v6, v37, v38\n"
    "v_max_u32 v6, v6, v39\n"
    "v_cmp_eq_u32 s[42:43], -1, v6\n"
    "s_waitcnt lgkmcnt(1)\n"
    "v_max3_u32 v7, v40, v41, v42\n"
    "s_waitcnt lgkmcnt(0)\n"
    "v_max3_u32 v7, v7, v43, v44\n"
    "v_max3_u32 v7, v7, v45, v46\n"
    "v_max_u32 v7, v7, v47\n"
    "v_cmp_eq_u32 s[44:45], -1, v7\n"
    "s_or_b64 vcc, s[38:39], s[40:41]\n"
    "s_or_b64 vcc, vcc, s[42:43]\n"
    "s_or_b64 vcc, vcc, s[44:45]\n"
    "s_cbranch_scc1 .Lb_sl87_%=\n"
    ".Lb_ok86_%=:\n"
    "s_barrier\n"
    "s_cmp_eq_u32 s24, 7\n"
    "s_cbranch_scc1 .Lb_en81_%=\n"
    "s_mov_b32 s4, 0x40000\n"
    "buffer_load_dwordx4 v[48:51], v114, s[16:19], s30 offen sc1\n"
    "buffer_load_dwordx4 v[52:55], v114, s[16:19], s20 offen sc1\n"
    "v_mov_b64 v[144:145], 0\n"
    "v_mov_b64 v[146:147], 0\n"
    "v_mov_b64 v[148:149], 0\n"
    "v_mov_b64 v[150:151], 0\n"
    "v_mov_b64 v[152:153], 0\n"
    "v_mov_b64 v[154:155], 0\n"
    "v_mov_b64 v[156:157], 0\n"
    "v_mov_b64 v[158:159], 0\n"
    "v_lshlrev_b32 v160, 16, v192\n"
    "v_and_b32 v161, 0xffff0000, v192\n"
    "v_lshlrev_b32 v162, 16, v193\n"
    "v_and_b32 v163, 0xffff0000, v193\n"
    "v_lshlrev_b32 v164, 16, v194\n"
    "v_and_b32 v165, 0xffff0000, v194\n"
    "v_lshlrev_b32 v166, 16, v195\n"
    "v_and_b32 v167, 0xffff0000, v195\n"
    "v_lshlrev_b32 v0, 16, v196\n"
    "v_and_b32 v1, 0xffff0000, v196\n"
    "v_mul_f32 v0, v0, v160\n"
    "v_mul_f32 v1, v1, v161\n"
    "v_cvt_pk_bf16_f32 v0, v0, v1\n"
    "v_lshlrev_b32 v128, 16, v0\n"
    "v_and_b32 v129, 0xffff0000, v0\n"
    "v_lshlrev_b32 v0, 16, v197\n"
    "v_and_b32 v1, 0xffff0000, v197\n"
    "v_mul_f32 v0, v0, v162\n"
    "v_mul_f32 v1, v1, v163\n"
    "v_cvt_pk_bf16_f32 v0, v0, v1\n"
    "v_lshlrev_b32 v130, 16, v0\n"
    "v_and_b32 v131, 0xffff0000, v0\n"
    "v_lshlrev_b32 v0, 16, v198\n"
    "v_and_b32 v1, 0xffff0000, v198\n"
    "v_mul_f32 v0, v0, v164\n"
    "v_mul_f32 v1, v1, v165\n"
    "v_cvt_pk_bf16_f32 v0, v0, v1\n"
    "v_lshlrev_b32 v132, 16, v0\n"
    "v_and_b32 v133, 0xffff0000, v0\n"
    "v_lshlrev_b32 v0, 16, v199\n"
    "v_and_b32 v1, 0xffff0000, v199\n"
    "v_mul_f32 v0, v0, v166\n"
    "v_mul_f32 v1, v1, v167\n"
    "v_cvt_pk_bf16_f32 v0, v0, v1\n"
    "v_lshlrev_b32 v134, 16, v0\n"
    "v_and_b32 v135, 0xffff0000, v0\n"
    "v_lshlrev_b32 v0, 16, v200\n"
    "v_and_b32 v1, 0xffff0000, v200\n"
    "v_mul_f32 v0, v0, v160\n"
    "v_mul_f32 v1, v1, v161\n"
    "v_cvt_pk_bf16_f32 v0, v0, v1\n"
    "v_lshlrev_b32 v136, 16, v0\n"
    "v_and_b32 v137, 0xffff0000, v0\n"
    "v_lshlrev_b32 v0, 16, v201\n"
    "v_and_b32 v1, 0xffff0000, v201\n"
    "v_mul_f32 v0, v0, v162\n"
    "v_mul_f32 v1, v1, v163\n"
    "v_cvt_pk_bf16_f32 v0, v0, v1\n"
    "v_lshlrev_b32 v138, 16, v0\n"
    "v_and_b32 v139, 0xffff0000, v0\n"
    "v_lshlrev_b32 v0, 16, v202\n"
    "v_and_b32 v1, 0xffff0000, v202\n"
    "v_mul_f32 v0, v0, v164\n"
    "v_mul_f32 v1, v1, v165\n"
    "v_cvt_pk_bf16_f32 v0, v0, v1\n"
    "v_lshlrev_b32 v140, 16, v0\n"
    "v_and_b32 v141, 0xffff0000, v0\n"
    "v_lshlrev_b32 v0, 16, v203\n"
    "v_and_b32 v1, 0xffff0000, v203\n"
    "v_mul_f32 v0, v0, v166\n"
    "v_mul_f32 v1, v1, v167\n"
    "v_cvt_pk_bf16_f32 v0, v0, v1\n"
    "v_lshlrev_b32 v142, 16, v0\n"
    "v_and_b32 v143, 0xffff0000, v0\n"
    ".Lb_so107_%=:\n"
    "s_waitcnt vmcnt(1)\n"
    "v_max3_u32 v4, v48, v49, v50\n"
    "v_max_u32 v4, v4, v51\n"
    "v_cmp_eq_u32 s[38:39], -1, v4\n"
    "s_waitcnt vmcnt(0)\n"
    "v_max3_u32 v5, v52, v53, v54\n"
    "v_max_u32 v5, v5, v55\n"
    "v_cmp_eq_u32 s[40:41], -1, v5\n"
    "s_nop 0\n"
    "s_or_b64 vcc, s[38:39], s[40:41]\n"
    "s_cbranch_scc1 .Lb_sl109_%=\n"
    ".Lb_ok108_%=:\n"
    "ds_write_b128 v115, v[48:51]\n"
    "ds_write_b128 v116, v[52:55] offset:1024\n"
    "s_mov_b64 exec, 1\n"
    "ds_add_u32 v75, v77 offset:120\n"
    "s_mov_b64 exec, -1\n"
    "ds_read_b32 v76, v75 offset:120\n"
    "s_waitcnt lgkmcnt(0)\n"
    "v_cmp_gt_u32 vcc, 4, v76\n"
    "s_nop 0\n"
    "s_cbranch_vccnz .Lb_sl113_%=\n"
    ".Lb_ok112_%=:\n"
    "ds_read_u16_d16_hi v144, v117 offset:0\n"
    "ds_read_u16_d16_hi v145, v117 offset:128\n"
    "ds_read_u16_d16_hi v146, v117 offset:256\n"
    "ds_read_u16_d16_hi v147, v117 offset:384\n"
    "ds_read_u16_d16_hi v148, v117 offset:512\n"
    "ds_read_u16_d16_hi v149, v117 offset:640\n"
    "ds_read_u16_d16_hi v150, v117 offset:768\n"
    "ds_read_u16_d16_hi v151, v117 offset:896\n"
    "ds_read_u16_d16_hi v152, v118 offset:0\n"
    "ds_read_u16_d16_hi v153, v118 offset:128\n"
    "ds_read_u16_d16_hi v154, v118 offset:256\n"
    "ds_read_u16_d16_hi v155, v118 offset:384\n"
    "ds_read_u16_d16_hi v156, v118 offset:512\n"
    "ds_read_u16_d16_hi v157, v118 offset:640\n"
    "ds_read_u16_d16_hi v158, v118 offset:768\n"
    "ds_read_u16_d16_hi v159, v118 offset:896\n"
    "s_waitcnt lgkmcnt(14)\n"
    "v_add_f32 v32, v144, v128\n"
    "v_add_f32 v33, v145, v129\n"
    "s_waitcnt lgkmcnt(13)\n"
    "v_add_f32 v34, v146, v130\n"
    "s_waitcnt lgkmcnt(12)\n"
    "v_add_f32 v35, v147, v131\n"
    "s_waitcnt lgkmcnt(11)\n"
    "v_add_f32 v36, v148, v132\n"
    "s_waitcnt lgkmcnt(10)\n"
    "v_add_f32 v37, v149, v133\n"
    "s_waitcnt lgkmcnt(9)\n"
    "v_add_f32 v38, v150, v134\n"
    "s_waitcnt lgkmcnt(8)\n"
    "v_add_f32 v39, v151, v135\n"
    "v_cvt_pk_bf16_f32 v24, v32, v33\n"
    "v_cvt_pk_bf16_f32 v25, v34, v35\n"
    "v_cvt_pk_bf16_f32 v26, v36, v37\n"
    "v_cvt_pk_bf16_f32 v27, v38, v39\n"
    "global_store_dwordx4 v112, v[24:27], s[92:93] sc1\n"
    "s_waitcnt lgkmcnt(7)\n"
    "v_add_f32 v40, v152, v136\n"
    "s_waitcnt lgkmcnt(6)\n"
    "v_add_f32 v41, v153, v137\n"
    "s_waitcnt lgkmcnt(5)\n"
    "v_add_f32 v42, v154, v138\n"
    "s_waitcnt lgkmcnt(4)\n"
    "v_add_f32 v43, v155, v139\n"
    "s_waitcnt lgkmcnt(3)\n"
    "v_add_f32 v44, v156, v140\n"
    "s_waitcnt lgkmcnt(2)\n"
    "v_add_f32 v45, v157, v141\n"
    "s_waitcnt lgkmcnt(1)\n"
    "v_add_f32 v46, v158, v142\n"
    "s_waitcnt lgkmcnt(0)\n"
    "v_add_f32 v47, v159, v143\n"
    "v_cvt_pk_bf16_f32 v28, v40, v41\n"
    "v_cvt_pk_bf16_f32 v29, v42, v43\n"
    "v_cvt_pk_bf16_f32 v30, v44, v45\n"
    "v_cvt_pk_bf16_f32 v31, v46, v47\n"
    "global_store_dwordx4 v113, v[28:31], s[92:93] sc1\n"
    ".Lb_en81_%=:\n"
    "s_endpgm\n"
    ".Lb_fb79_%=:\n"
    "v_mov_b32 v2, 0xff800000\n"
    "s_mov_b32 s28, 65536\n"
    "s_mov_b32 s29, 79872\n"
    "s_mov_b32 s4, 63\n"
    ".Lb_fl115_%=:\n"
    "buffer_load_dwordx4 v[208:211], v71, s[16:19], s28 offen offset:0 sc1\n"
    "buffer_load_dwordx4 v[212:215], v71, s[16:19], s28 offen offset:1024 sc1\n"
    "s_mov_b64 exec, s[44:45]\n"
    "buffer_load_dwordx4 v[216:219], v72, s[16:19], s28 offen sc1\n"
    "buffer_load_dwordx4 v[220:223], v72, s[16:19], s29 offen sc1\n"
    "s_mov_b64 exec, -1\n"
    "s_add_u32 s28, s28, 28672\n"
    "s_add_u32 s29, s29, 28672\n"
    "s_waitcnt vmcnt(0)\n"
    "v_mfma_f32_32x32x16_bf16 v[128:143], v[208:211], v[104:107], 0\n"
    "v_mfma_f32_32x32x16_bf16 v[128:143], v[212:215], v[108:111], v[128:143]\n"
    "s_nop 11\n"
    "v_max3_f32 v2, v2, v128, v129\n"
    "v_max3_f32 v2, v2, v130, v131\n"
    "v_max3_f32 v2, v2, v132, v133\n"
    "v_max3_f32 v2, v2, v134, v135\n"
    "v_max3_f32 v2, v2, v136, v137\n"
    "v_max3_f32 v2, v2, v138, v139\n"
    "v_max3_f32 v2, v2, v140, v141\n"
    "v_max3_f32 v2, v2, v142, v143\n"
    "s_sub_u32 s4, s4, 1\n"
    "s_cbranch_scc0 .Lb_fl115_%=\n"
    "v_mov_b32 v3, v2\n"
    "s_nop 1\n"
    "v_permlane32_swap_b32 v2, v3\n"
    "v_max_f32 v2, v2, v3\n"
    "s_mov_b32 s28, 65536\n"
    "s_mov_b32 s29, 79872\n"
    "s_mov_b32 s4, 63\n"
    "v_mov_b64 v[48:49], 0\n"
    "v_mov_b64 v[50:51], 0\n"
    "v_mov_b64 v[52:53], 0\n"
    "v_mov_b64 v[54:55], 0\n"
    "v_mov_b64 v[56:57], 0\n"
    "v_mov_b64 v[58:59], 0\n"
    "v_mov_b64 v[60:61], 0\n"
    "v_mov_b64 v[62:63], 0\n"
    ".Lb_fl116_%=:\n"
    "buffer_load_dwordx4 v[208:211], v71, s[16:19], s28 offen offset:0 sc1\n"
    "buffer_load_dwordx4 v[212:215], v71, s[16:19], s28 offen offset:1024 sc1\n"
    "s_mov_b64 exec, s[44:45]\n"
    "buffer_load_dwordx4 v[216:219], v72, s[16:19], s28 offen sc1\n"
    "buffer_load_dwordx4 v[220:223], v72, s[16:19], s29 offen sc1\n"
    "s_mov_b64 exec, -1\n"
    "s_add_u32 s28, s28, 28672\n"
    "s_add_u32 s29, s29, 28672\n"
    "s_waitcnt vmcnt(0)\n"
    "v_mfma_f32_32x32x16_bf16 v[128:143], v[208:211], v[104:107], 0\n"
    "v_mfma_f32_32x32x16_bf16 v[128:143], v[212:215], v[108:111], v[128:143]\n"
    "s_nop 11\n"
    "v_sub_f32 v128, v128, v2\n"
    "v_sub_f32 v129, v129, v2\n"
    "v_sub_f32 v130, v130, v2\n"
    "v_sub_f32 v131, v131, v2\n"
    "v_sub_f32 v132, v132, v2\n"
    "v_sub_f32 v133, v133, v2\n"
    "v_sub_f32 v134, v134, v2\n"
    "v_sub_f32 v135, v135, v2\n"
    "v_sub_f32 v136, v136, v2\n"
    "v_sub_f32 v137, v137, v2\n"
    "v_sub_f32 v138, v138, v2\n"
    "v_sub_f32 v139, v139, v2\n"
    "v_sub_f32 v140, v140, v2\n"
    "v_sub_f32 v141, v141, v2\n"
    "v_sub_f32 v142, v142, v2\n"
    "v_sub_f32 v143, v143, v2\n"
    "v_exp_f32 v128, v128\n"
    "v_exp_f32 v129, v129\n"
    "v_exp_f32 v130, v130\n"
    "v_exp_f32 v131, v131\n"
    "v_exp_f32 v132, v132\n"
    "v_exp_f32 v133, v133\n"
    "v_exp_f32 v134, v134\n"
    "v_exp_f32 v135, v135\n"
    "v_exp_f32 v136, v136\n"
    "v_exp_f32 v137, v137\n"
    "v_exp_f32 v138, v138\n"
    "v_exp_f32 v139, v139\n"
    "v_exp_f32 v140, v140\n"
    "v_exp_f32 v141, v141\n"
    "v_exp_f32 v142, v142\n"
    "v_exp_f32 v143, v143\n"
    "v_smfmac_f32_32x32x32_bf16 v[48:63], v[216:219], v[128:135], v78\n"
    "s_nop 0\n"
    "v_smfmac_f32_32x32x32_bf16 v[48:63], v[220:223], v[136:143], v78\n"
    "s_sub_u32 s4, s4, 1\n"
    "s_cbranch_scc0 .Lb_fl116_%=\n"
    "s_nop 9\n"
    "v_mov_b32 v0, v60\n"
    "v_mov_b32 v1, v60\n"
    "s_nop 1\n"
    "v_permlane32_swap_b32 v0, v1\n"
    "v_rcp_f32 v0, v1\n"
    "v_mov_b32 v77, 1\n"
    "v_mul_f32 v48, v0, v48\n"
    "v_mul_f32 v49, v0, v49\n"
    "v_mul_f32 v50, v0, v50\n"
    "v_mul_f32 v51, v0, v51\n"
    "v_mul_f32 v52, v0, v52\n"
    "v_mul_f32 v53, v0, v53\n"
    "v_mul_f32 v54, v0, v54\n"
    "v_mul_f32 v55, v0, v55\n"
    "v_mul_f32 v56, v0, v56\n"
    "v_mul_f32 v57, v0, v57\n"
    "v_mul_f32 v58, v0, v58\n"
    "v_mul_f32 v59, v0, v59\n"
    "v_mul_f32 v60, v0, v60\n"
    "v_mul_f32 v61, v0, v61\n"
    "v_mul_f32 v62, v0, v62\n"
    "v_mul_f32 v63, v0, v63\n"
    "v_cvt_pk_bf16_f32 v8, v48, v49\n"
    "v_min_u32 v8, -2, v8\n"
    "v_cvt_pk_bf16_f32 v9, v50, v51\n"
    "v_min_u32 v9, -2, v9\n"
    "v_cvt_pk_bf16_f32 v10, v52, v53\n"
    "v_min_u32 v10, -2, v10\n"
    "v_cvt_pk_bf16_f32 v11, v54, v55\n"
    "v_min_u32 v11, -2, v11\n"
    "buffer_store_dwordx4 v[8:11], v74, s[16:19], s34 offen sc1\n"
    "v_cvt_pk_bf16_f32 v12, v56, v57\n"
    "v_min_u32 v12, -2, v12\n"
    "v_cvt_pk_bf16_f32 v13, v58, v59\n"
    "v_min_u32 v13, -2, v13\n"
    "v_cvt_pk_bf16_f32 v14, v60, v61\n"
    "v_min_u32 v14, -2, v14\n"
    "v_cvt_pk_bf16_f32 v15, v62, v63\n"
    "v_min_u32 v15, -2, v15\n"
    "s_branch .Lb_bk80_%=\n"
    ".Lb_sl28_%=:\n"
    "s_mov_b32 s4, 4194304\n"
    ".Lb_sp29_%=:\n"
    "ds_read_b32 v76, v75 offset:64\n"
    "s_waitcnt lgkmcnt(0)\n"
    "v_cmp_gt_u32 vcc, 2, v76\n"
    "s_sub_u32 s4, s4, 1\n"
    "s_cbranch_scc1 .Lb_ok27_%=\n"
    "s_cbranch_vccnz .Lb_sp29_%=\n"
    "s_branch .Lb_ok27_%=\n"
    ".Lb_sl31_%=:\n"
    "s_mov_b32 s4, 4194304\n"
    ".Lb_sp32_%=:\n"
    "ds_read_b32 v76, v75 offset:68\n"
    "s_waitcnt lgkmcnt(0)\n"
    "v_cmp_gt_u32 vcc, 2, v76\n"
    "s_sub_u32 s4, s4, 1\n"
    "s_cbranch_scc1 .Lb_ok30_%=\n"
    "s_cbranch_vccnz .Lb_sp32_%=\n"
    "s_branch .Lb_ok30_%=\n"
    ".Lb_sl34_%=:\n"
    "s_mov_b32 s4, 4194304\n"
    ".Lb_sp35_%=:\n"
    "ds_read_b32 v76, v75 offset:72\n"
    "s_waitcnt lgkmcnt(0)\n"
    "v_cmp_gt_u32 vcc, 2, v76\n"
    "s_sub_u32 s4, s4, 1\n"
    "s_cbranch_scc1 .Lb_ok33_%=\n"
    "s_cbranch_vccnz .Lb_sp35_%=\n"
    "s_branch .Lb_ok33_%=\n"
    ".Lb_sl37_%=:\n"
    "s_mov_b32 s4, 4194304\n"
    ".Lb_sp38_%=:\n"
    "ds_read_b32 v76, v75 offset:76\n"
    "s_waitcnt lgkmcnt(0)\n"
    "v_cmp_gt_u32 vcc, 2, v76\n"
    "s_sub_u32 s4, s4, 1\n"
    "s_cbranch_scc1 .Lb_ok36_%=\n"
    "s_cbranch_vccnz .Lb_sp38_%=\n"
    "s_branch .Lb_ok36_%=\n"
    ".Lb_sl40_%=:\n"
    "s_mov_b32 s4, 4194304\n"
    ".Lb_sp41_%=:\n"
    "ds_read_b32 v76, v75 offset:80\n"
    "s_waitcnt lgkmcnt(0)\n"
    "v_cmp_gt_u32 vcc, 2, v76\n"
    "s_sub_u32 s4, s4, 1\n"
    "s_cbranch_scc1 .Lb_ok39_%=\n"
    "s_cbranch_vccnz .Lb_sp41_%=\n"
    "s_branch .Lb_ok39_%=\n"
    ".Lb_sl43_%=:\n"
    "s_mov_b32 s4, 4194304\n"
    ".Lb_sp44_%=:\n"
    "ds_read_b32 v76, v75 offset:84\n"
    "s_waitcnt lgkmcnt(0)\n"
    "v_cmp_gt_u32 vcc, 2, v76\n"
    "s_sub_u32 s4, s4, 1\n"
    "s_cbranch_scc1 .Lb_ok42_%=\n"
    "s_cbranch_vccnz .Lb_sp44_%=\n"
    "s_branch .Lb_ok42_%=\n"
    ".Lb_sl46_%=:\n"
    "s_mov_b32 s4, 4194304\n"
    ".Lb_sp47_%=:\n"
    "ds_read_b32 v76, v75 offset:88\n"
    "s_waitcnt lgkmcnt(0)\n"
    "v_cmp_gt_u32 vcc, 2, v76\n"
    "s_sub_u32 s4, s4, 1\n"
    "s_cbranch_scc1 .Lb_ok45_%=\n"
    "s_cbranch_vccnz .Lb_sp47_%=\n"
    "s_branch .Lb_ok45_%=\n"
    ".Lb_sl52_%=:\n"
    "s_mov_b32 s4, 4194304\n"
    ".Lb_sp53_%=:\n"
    "s_sleep 1\n"
    "ds_read_b32 v77, v75 offset:112\n"
    "s_waitcnt lgkmcnt(0)\n"
    "v_cmp_gt_u32 vcc, 1, v77\n"
    "s_sub_u32 s4, s4, 1\n"
    "s_cbranch_scc1 .Lb_ok51_%=\n"
    "s_cbranch_vccnz .Lb_sp53_%=\n"
    "s_branch .Lb_ok51_%=\n"
    ".Lb_sl55_%=:\n"
    "s_mov_b32 s4, 4194304\n"
    ".Lb_sp56_%=:\n"
    "s_sleep 1\n"
    "ds_read_b32 v77, v75 offset:112\n"
    "s_waitcnt lgkmcnt(0)\n"
    "v_cmp_gt_u32 vcc, 1, v77\n"
    "s_sub_u32 s4, s4, 1\n"
    "s_cbranch_scc1 .Lb_ok54_%=\n"
    "s_cbranch_vccnz .Lb_sp56_%=\n"
    "s_branch .Lb_ok54_%=\n"
    ".Lb_sl58_%=:\n"
    "s_mov_b32 s4, 4194304\n"
    ".Lb_sp59_%=:\n"
    "ds_read_b32 v76, v75 offset:92\n"
    "s_waitcnt lgkmcnt(0)\n"
    "v_cmp_gt_u32 vcc, 4, v76\n"
    "s_sub_u32 s4, s4, 1\n"
    "s_cbranch_scc1 .Lb_ok57_%=\n"
    "s_cbranch_vccnz .Lb_sp59_%=\n"
    "s_branch .Lb_ok57_%=\n"
    ".Lb_sl62_%=:\n"
    "s_mov_b32 s4, 4194304\n"
    ".Lb_sp63_%=:\n"
    "ds_read_b32 v76, v75 offset:96\n"
    "s_waitcnt lgkmcnt(0)\n"
    "v_cmp_gt_u32 vcc, 2, v76\n"
    "s_sub_u32 s4, s4, 1\n"
    "s_cbranch_scc1 .Lb_ok61_%=\n"
    "s_cbranch_vccnz .Lb_sp63_%=\n"
    "s_branch .Lb_ok61_%=\n"
    ".Lb_sl65_%=:\n"
    "s_mov_b32 s4, 262144\n"
    ".Lb_sp66_%=:\n"
    "s_sleep 4\n"
    "buffer_load_dword v77, v73, s[16:19], 0 offen offset:0 sc1\n"
    "s_waitcnt vmcnt(0)\n"
    "v_cmp_ne_u32 vcc, 1, v77\n"
    "s_sub_u32 s4, s4, 1\n"
    "s_cbranch_scc1 .Lb_ok64_%=\n"
    "s_cbranch_vccnz .Lb_sp66_%=\n"
    "s_branch .Lb_ok64_%=\n"
    ".Lb_sl68_%=:\n"
    "s_mov_b32 s4, 4194304\n"
    ".Lb_sp69_%=:\n"
    "ds_read_b32 v76, v75 offset:104\n"
    "s_waitcnt lgkmcnt(0)\n"
    "v_cmp_gt_u32 vcc, 4, v76\n"
    "s_sub_u32 s4, s4, 1\n"
    "s_cbranch_scc1 .Lb_ok67_%=\n"
    "s_cbranch_vccnz .Lb_sp69_%=\n"
    "s_branch .Lb_ok67_%=\n"
    ".Lb_sl71_%=:\n"
    "s_mov_b32 s4, 262144\n"
    ".Lb_sp72_%=:\n"
    "s_sleep 2\n"
    "buffer_load_dword v77, v73, s[16:19], 0 offen offset:0 sc1\n"
    "s_waitcnt vmcnt(0)\n"
    "v_cmp_ne_u32 vcc, 1, v77\n"
    "s_sub_u32 s4, s4, 1\n"
    "s_cbranch_scc1 .Lb_ok70_%=\n"
    "s_cbranch_vccnz .Lb_sp72_%=\n"
    "s_branch .Lb_ok70_%=\n"
    ".Lb_sl74_%=:\n"
    "s_mov_b32 s4, 262144\n"
    ".Lb_sp75_%=:\n"
    "s_sleep 2\n"
    "buffer_load_dword v77, v73, s[16:19], 0 offen offset:4 sc1\n"
    "s_waitcnt vmcnt(0)\n"
    "v_cmp_ne_u32 vcc, 1, v77\n"
    "s_sub_u32 s4, s4, 1\n"
    "s_cbranch_scc1 .Lb_ok73_%=\n"
    "s_cbranch_vccnz .Lb_sp75_%=\n"
    "s_branch .Lb_ok73_%=\n"
    ".Lb_sl77_%=:\n"
    "s_mov_b32 s4, 4194304\n"
    ".Lb_sp78_%=:\n"
    "ds_read_b32 v76, v75 offset:104\n"
    "s_waitcnt lgkmcnt(0)\n"
    "v_cmp_gt_u32 vcc, 8, v76\n"
    "s_sub_u32 s4, s4, 1\n"
    "s_cbranch_scc1 .Lb_ok76_%=\n"
    "s_cbranch_vccnz .Lb_sp78_%=\n"
    "s_branch .Lb_ok76_%=\n"
    ".Lb_sl83_%=:\n"
    "s_mov_b32 s4, 4194304\n"
    ".Lb_sp84_%=:\n"
    "ds_read_b32 v76, v75 offset:116\n"
    "s_waitcnt lgkmcnt(0)\n"
    "v_cmp_gt_u32 vcc, 4, v76\n"
    "s_sub_u32 s4, s4, 1\n"
    "s_cbranch_scc1 .Lb_ok82_%=\n"
    "s_cbranch_vccnz .Lb_sp84_%=\n"
    "s_branch .Lb_ok82_%=\n"
    ".Lb_pl88_%=:\n"
    "s_sub_u32 s4, s4, 1\n"
    "s_cbranch_scc1 .Lb_pa90_%=\n"
    "s_sleep 1\n"
    "buffer_load_dword v124, off, s[16:19], s35 offset:2044 sc1\n"
    "buffer_load_dword v125, off, s[16:19], s35 offset:3836 sc1\n"
    "buffer_load_dword v126, off, s[16:19], s27 offset:1532 sc1\n"
    "buffer_load_dword v127, off, s[16:19], s27 offset:3324 sc1\n"
    "s_waitcnt vmcnt(0)\n"
    "v_cmp_eq_u32 vcc, -1, v124\n"
    "s_nop 0\n"
    "s_andn2_b64 exec, s[38:39], vcc\n"
    "s_and_b64 s[38:39], s[38:39], vcc\n"
    "s_cbranch_execz .Lb_sk95_%=\n"
    "s_add_u32 m0, s98, 0\n"
    "s_nop 0\n"
    "buffer_load_dwordx4 v74, s[16:19], s35 offen offset:0 sc1 lds\n"
    "buffer_load_dwordx4 v74, s[16:19], s35 offen offset:1024 sc1 lds\n"
    ".Lb_sk95_%=:\n"
    "s_mov_b64 exec, -1\n"
    "v_cmp_eq_u32 vcc, -1, v125\n"
    "s_nop 0\n"
    "s_andn2_b64 exec, s[40:41], vcc\n"
    "s_and_b64 s[40:41], s[40:41], vcc\n"
    "s_cbranch_execz .Lb_sk96_%=\n"
    "s_add_u32 m0, s98, 0\n"
    "s_nop 0\n"
    "buffer_load_dwordx4 v74, s[16:19], s35 offen offset:2048 sc1 lds\n"
    "buffer_load_dwordx4 v74, s[16:19], s35 offen offset:3072 sc1 lds\n"
    ".Lb_sk96_%=:\n"
    "s_mov_b64 exec, -1\n"
    "v_cmp_eq_u32 vcc, -1, v126\n"
    "s_nop 0\n"
    "s_andn2_b64 exec, s[42:43], vcc\n"
    "s_and_b64 s[42:43], s[42:43], vcc\n"
    "s_cbranch_execz .Lb_sk97_%=\n"
    "s_add_u32 m0, s98, 4096\n"
    "s_nop 0\n"
    "buffer_load_dwordx4 v74, s[16:19], s27 offen offset:0 sc1 lds\n"
    "buffer_load_dwordx4 v74, s[16:19], s27 offen offset:1024 sc1 lds\n"
    ".Lb_sk97_%=:\n"
    "s_mov_b64 exec, -1\n"
    "v_cmp_eq_u32 vcc, -1, v127\n"
    "s_nop 0\n"
    "s_andn2_b64 exec, s[44:45], vcc\n"
    "s_and_b64 s[44:45], s[44:45], vcc\n"
    "s_cbranch_execz .Lb_sk98_%=\n"
    "s_add_u32 m0, s98, 4096\n"
    "s_nop 0\n"
    "buffer_load_dwordx4 v74, s[16:19], s27 offen offset:2048 sc1 lds\n"
    "buffer_load_dwordx4 v74, s[16:19], s27 offen offset:3072 sc1 lds\n"
    ".Lb_sk98_%=:\n"
    "s_mov_b64 exec, -1\n"
    "s_or_b64 vcc, s[38:39], s[40:41]\n"
    "s_or_b64 vcc, vcc, s[42:43]\n"
    "s_or_b64 vcc, vcc, s[44:45]\n"
    "s_cbranch_scc1 .Lb_pl88_%=\n"
    "s_branch .Lb_pd89_%=\n"
    ".Lb_pa90_%=:\n"
    "s_mov_b64 exec, s[38:39]\n"
    "s_cbranch_execz .Lb_sk99_%=\n"
    "s_add_u32 m0, s98, 0\n"
    "s_nop 0\n"
    "buffer_load_dwordx4 v74, s[16:19], s35 offen offset:0 sc1 lds\n"
    "buffer_load_dwordx4 v74, s[16:19], s35 offen offset:1024 sc1 lds\n"
    ".Lb_sk99_%=:\n"
    "s_mov_b64 exec, s[40:41]\n"
    "s_cbranch_execz .Lb_sk100_%=\n"
    "s_add_u32 m0, s98, 0\n"
    "s_nop 0\n"
    "buffer_load_dwordx4 v74, s[16:19], s35 offen offset:2048 sc1 lds\n"
    "buffer_load_dwordx4 v74, s[16:19], s35 offen offset:3072 sc1 lds\n"
    ".Lb_sk100_%=:\n"
    "s_mov_b64 exec, s[42:43]\n"
    "s_cbranch_execz .Lb_sk101_%=\n"
    "s_add_u32 m0, s98, 4096\n"
    "s_nop 0\n"
    "buffer_load_dwordx4 v74, s[16:19], s27 offen offset:0 sc1 lds\n"
    "buffer_load_dwordx4 v74, s[16:19], s27 offen offset:1024 sc1 lds\n"
    ".Lb_sk101_%=:\n"
    "s_mov_b64 exec, s[44:45]\n"
    "s_cbranch_execz .Lb_sk102_%=\n"
    "s_add_u32 m0, s98, 4096\n"
    "s_nop 0\n"
    "buffer_load_dwordx4 v74, s[16:19], s27 offen offset:2048 sc1 lds\n"
    "buffer_load_dwordx4 v74, s[16:19], s27 offen offset:3072 sc1 lds\n"
    ".Lb_sk102_%=:\n"
    "s_mov_b64 exec, -1\n"
    "s_branch .Lb_pd89_%=\n"
    ".Lb_sl87_%=:\n"
    "s_sub_u32 s4, s4, 1\n"
    "s_cbranch_scc1 .Lb_ok86_%=\n"
    "s_sleep 1\n"
    "s_mov_b64 exec, s[38:39]\n"
    "s_cbranch_execz .Lb_sk103_%=\n"
    "s_add_u32 m0, s98, 0\n"
    "s_nop 0\n"
    "buffer_load_dwordx4 v74, s[16:19], s35 offen offset:0 sc1 lds\n"
    "buffer_load_dwordx4 v74, s[16:19], s35 offen offset:1024 sc1 lds\n"
    ".Lb_sk103_%=:\n"
    "s_mov_b64 exec, s[40:41]\n"
    "s_cbranch_execz .Lb_sk104_%=\n"
    "s_add_u32 m0, s98, 0\n"
    "s_nop 0\n"
    "buffer_load_dwordx4 v74, s[16:19], s35 offen offset:2048 sc1 lds\n"
    "buffer_load_dwordx4 v74, s[16:19], s35 offen offset:3072 sc1 lds\n"
    ".Lb_sk104_%=:\n"
    "s_mov_b64 exec, s[42:43]\n"
    "s_cbranch_execz .Lb_sk105_%=\n"
    "s_add_u32 m0, s98, 4096\n"
    "s_nop 0\n"
    "buffer_load_dwordx4 v74, s[16:19], s27 offen offset:0 sc1 lds\n"
    "buffer_load_dwordx4 v74, s[16:19], s27 offen offset:1024 sc1 lds\n"
    ".Lb_sk105_%=:\n"
    "s_mov_b64 exec, s[44:45]\n"
    "s_cbranch_execz .Lb_sk106_%=\n"
    "s_add_u32 m0, s98, 4096\n"
    "s_nop 0\n"
    "buffer_load_dwordx4 v74, s[16:19], s27 offen offset:2048 sc1 lds\n"
    "buffer_load_dwordx4 v74, s[16:19], s27 offen offset:3072 sc1 lds\n"
    ".Lb_sk106_%=:\n"
    "s_mov_b64 exec, -1\n"
    "s_waitcnt vmcnt(0)\n"
    "s_branch .Lb_so85_%=\n"
    ".Lb_sl109_%=:\n"
    "s_sub_u32 s4, s4, 1\n"
    "s_cbranch_scc1 .Lb_ok108_%=\n"
    "s_sleep 1\n"
    "s_mov_b64 exec, s[38:39]\n"
    "s_cbranch_execz .Lb_sk110_%=\n"
    "buffer_load_dwordx4 v[48:51], v114, s[16:19], s30 offen sc1\n"
    ".Lb_sk110_%=:\n"
    "s_mov_b64 exec, s[40:41]\n"
    "s_cbranch_execz .Lb_sk111_%=\n"
    "buffer_load_dwordx4 v[52:55], v114, s[16:19], s20 offen sc1\n"
    ".Lb_sk111_%=:\n"
    "s_mov_b64 exec, -1\n"
    "s_waitcnt vmcnt(0)\n"
    "s_branch .Lb_so107_%=\n"
    ".Lb_sl113_%=:\n"
    "s_mov_b32 s4, 4194304\n"
    ".Lb_sp114_%=:\n"
    "ds_read_b32 v76, v75 offset:120\n"
    "s_waitcnt lgkmcnt(0)\n"
    "v_cmp_gt_u32 vcc, 4, v76\n"
    "s_sub_u32 s4, s4, 1\n"
    "s_cbranch_scc1 .Lb_ok112_%=\n"
    "s_cbranch_vccnz .Lb_sp114_%=\n"
    "s_branch .Lb_ok112_%=\n"
    ".La2_%=:\n"
    "v_mov_b32 v3, 0\n"
    "v_mov_b32 v4, 1\n"
    "s_cmp_eq_u32 s2, 7\n"
    "s_cbranch_scc1 .La_ld4_%=\n"
    ".La_nl5_%=:\n"
    "s_setprio 1\n"
    "v_lshlrev_b32 v2, 4, v1\n"
    "s_and_b32 s42, s5, 1\n"
    "s_mul_i32 s43, s5, 8704\n"
    "s_add_u32 s43, s43, 18688\n"
    "s_lshl_b32 s92, s2, 7\n"
    "s_and_b32 s52, s2, 1\n"
    "s_andn2_b32 s53, 7, s7\n"
    "s_lshr_b32 s48, s2, 6\n"
    "s_lshl_b32 s49, s2, 3\n"
    "s_and_b32 s49, s49, 48\n"
    "s_lshl2_add_u32 s54, s48, s49\n"
    "s_and_b32 s55, s53, 3\n"
    "s_mov_b32 s38, 401408\n"
    "s_mov_b32 s39, 0x20000\n"
    "s_sub_u32 s48, s53, s52\n"
    "s_add_u32 s48, s48, 7\n"
    "s_and_b32 s48, s48, 7\n"
    "s_lshl3_add_u32 s48, s52, s48\n"
    "s_add_u32 s49, s54, s5\n"
    "s_lshl4_add_u32 s48, s49, s48\n"
    "s_lshl_b32 s48, s48, 11\n"
    "s_add_u32 s88, s48, 3735552\n"
    "s_lshl_b32 s48, s6, 7\n"
    "s_lshl2_add_u32 s48, s7, s48\n"
    "s_add_u32 s48, s48, s5\n"
    "s_lshr_b32 s48, s48, 1\n"
    "s_mul_i32 s48, s48, 4224\n"
    "s_lshl_b32 s49, s42, 11\n"
    "s_add_u32 s49, s49, s48\n"
    "s_add_u32 s89, s49, 7725056\n"
    "s_lshl3_add_u32 s49, s42, s48\n"
    "s_add_u32 s91, s49, 5832692\n"
    "s_lshl2_add_u32 s48, s6, s5\n"
    "s_mul_i32 s48, s48, 67584\n"
    "s_lshl_b32 s49, s7, 7\n"
    "s_add_u32 s48, s48, s49\n"
    "s_add_u32 s90, s48, 5832704\n"
    "s_lshl_b32 s48, s5, 11\n"
    "s_add_u32 s95, s48, 18688\n"
    "s_mul_i32 s48, s5, 8320\n"
    "s_add_u32 s96, s48, 18736\n"
    "s_mul_i32 s48, s5, 14336\n"
    "s_add_u32 s93, s48, 102656\n"
    "s_mul_i32 s48, s5, 50176\n"
    "s_lshl_b32 s49, s55, 8\n"
    "s_add_u32 s94, s48, s49\n"
    "v_lshrrev_b32 v32, 3, v1\n"
    "v_and_b32 v33, 7, v1\n"
    "v_lshrrev_b32 v34, 4, v1\n"
    "v_bfe_u32 v35, v1, 2, 2\n"
    "v_and_b32 v36, 3, v1\n"
    "v_and_b32 v37, 15, v1\n"
    "v_lshrrev_b32 v38, 5, v1\n"
    "v_and_b32 v39, 31, v1\n"
    "s_lshl2_add_u32 s48, s7, s5\n"
    "s_mul_i32 s48, s48, 14336\n"
    "s_min_u32 s49, s6, 6\n"
    "s_lshl_b32 s49, s49, 7\n"
    "s_add_u32 s48, s48, s49\n"
    "v_and_b32 v40, 3, v32\n"
    "v_lshlrev_b32 v40, 1, v40\n"
    "v_xor_b32 v40, v40, v33\n"
    "v_mul_u32_u24 v41, 896, v32\n"
    "v_lshl_add_u32 v40, v40, 4, v41\n"
    "v_add_u32 v9, s48, v40\n"
    "v_add_u32 v10, 6144, v9\n"
    "v_xor_b32 v40, s5, v35\n"
    "v_lshlrev_b32 v41, 3, v36\n"
    "v_lshl_or_b32 v41, v40, 5, v41\n"
    "v_lshl_or_b32 v42, v34, 4, v35\n"
    "v_lshl_or_b32 v41, v42, 7, v41\n"
    "v_add_u32 v11, 18688, v41\n"
    "s_mul_i32 s48, s5, 2304\n"
    "s_add_u32 s48, s48, 26880\n"
    "v_mul_u32_u24 v41, 144, v37\n"
    "v_lshl_add_u32 v41, v34, 5, v41\n"
    "v_add_u32 v12, s48, v41\n"
    "v_mul_u32_u24 v41, 144, v32\n"
    "v_lshl_add_u32 v41, v33, 4, v41\n"
    "v_add_u32 v13, s48, v41\n"
    "v_mul_u32_u24 v41, 4224, v32\n"
    "v_lshl_add_u32 v41, v33, 4, v41\n"
    "s_xor_b32 s48, s44, 1\n"
    "s_lshl_b32 s48, s48, 30\n"
    "v_or_b32 v14, s48, v41\n"
    "v_or_b32 v0, s48, v2\n"
    "s_add_u32 s48, s6, 264\n"
    "s_lshr_b32 s49, s54, 2\n"
    "s_add_u32 s49, s49, 272\n"
    "v_mov_b32 v41, s48\n"
    "v_mov_b32 v42, s49\n"
    "s_mov_b32 vcc_lo, 0xaaaaaaaa\n"
    "s_mov_b32 vcc_hi, 0xaaaaaaaa\n"
    "v_cndmask_b32 v41, v41, v42, vcc\n"
    "v_lshlrev_b32 v15, 7, v41\n"
    "v_cndmask_b32 v212, 1, 2, vcc\n"
    "s_cmp_eq_u32 s55, 3\n"
    "s_cselect_b32 s48, 7, 15\n"
    "s_cselect_b32 s49, 2, 4\n"
    "s_cmp_lt_u32 s5, s49\n"
    "s_cselect_b32 s97, 1, 0\n"
    "v_lshlrev_b32 v41, 2, v34\n"
    "v_xor_b32 v41, v41, v37\n"
    "v_and_b32 v41, s48, v41\n"
    "v_mul_u32_u24 v42, 896, v34\n"
    "v_lshl_add_u32 v16, v41, 4, v42\n"
    "v_add_u32 v17, 2560, v16\n"
    "v_add_u32 v18, 5120, v16\n"
    "v_add_u32 v19, 7680, v16\n"
    "v_bfrev_b32 v41, v1\n"
    "v_lshrrev_b32 v41, 30, v41\n"
    "v_and_b32 v42, 1, v34\n"
    "v_lshlrev_b32 v41, 3, v41\n"
    "v_lshl_or_b32 v41, v42, 5, v41\n"
    "v_lshl_or_b32 v41, v40, 6, v41\n"
    "v_lshl_or_b32 v42, v38, 3, v35\n"
    "v_lshl_or_b32 v41, v42, 8, v41\n"
    "v_add_u32 v20, 102656, v41\n"
    "v_lshlrev_b32 v41, 10, v38\n"
    "v_sub_u32 v21, v20, v41\n"
    "s_lshr_b32 s48, s5, 1\n"
    "s_mul_i32 s48, s48, 16640\n"
    "s_lshl_b32 s49, s42, 8\n"
    "s_add_u32 s48, s48, s49\n"
    "s_add_u32 s48, s48, 18688\n"
    "v_mul_u32_u24 v41, 4160, v36\n"
    "v_add_u32 v42, v34, v35\n"
    "v_lshl_add_u32 v41, v42, 4, v41\n"
    "v_add_u32 v30, s48, v41\n"
    "s_and_b32 s48, s5, 2\n"
    "s_mul_i32 s48, s48, 4576\n"
    "s_sub_u32 s48, s48, s49\n"
    "s_add_u32 s48, s48, 560\n"
    "v_bfe_u32 v41, v1, 3, 1\n"
    "v_mul_u32_u24 v41, 4576, v41\n"
    "v_lshlrev_b32 v42, 2, v33\n"
    "v_sub_u32 v42, v42, v34\n"
    "v_lshl_add_u32 v41, v42, 4, v41\n"
    "v_add_u32 v31, s48, v41\n"
    "s_lshl_b32 s48, s5, 12\n"
    "s_add_u32 s49, s48, 86272\n"
    "v_add_u32 v22, s49, v2\n"
    "s_xor_b32 s48, s48, 4096\n"
    "s_add_u32 s48, s48, 86272\n"
    "v_add_u32 v23, s48, v2\n"
    "v_add_u32 v24, 53504, v2\n"
    "s_mul_i32 s48, s5, 2560\n"
    "s_add_u32 s48, s48, 26880\n"
    "v_mul_u32_u24 v41, 80, v39\n"
    "v_lshl_add_u32 v41, v38, 4, v41\n"
    "v_add_u32 v25, s48, v41\n"
    "v_lshrrev_b32 v42, 2, v1\n"
    "v_mul_u32_u24 v41, 80, v42\n"
    "v_lshl_add_u32 v41, v36, 4, v41\n"
    "v_add_u32 v26, s48, v41\n"
    "s_lshl1_add_u32 s48, s52, s54\n"
    "s_lshr_b32 s49, s53, 2\n"
    "s_add_u32 s48, s48, s49\n"
    "s_mul_i32 s48, s48, 57344\n"
    "s_lshl2_add_u32 s49, s55, s5\n"
    "s_lshl_b32 s49, s49, 6\n"
    "s_add_u32 s48, s48, s49\n"
    "s_add_u32 s48, s48, 896\n"
    "v_mul_u32_u24 v41, 1792, v42\n"
    "v_lshl_add_u32 v41, v36, 4, v41\n"
    "v_add_u32 v27, s48, v41\n"
    "v_add_u32 v28, 28672, v27\n"
    "s_lshl_b32 s48, s42, 1\n"
    "s_sub_u32 s48, 14, s48\n"
    "v_sub_u32 v41, s48, v38\n"
    "v_min_u32 v41, v41, v39\n"
    "v_mul_u32_u24 v42, 4160, v38\n"
    "v_lshl_add_u32 v41, v41, 2, v42\n"
    "s_sub_u32 s48, s96, 48\n"
    "v_add_u32 v8, s48, v41\n"
    "s_lshl2_add_u32 s48, s7, s5\n"
    "s_min_u32 s49, s48, 111\n"
    "s_mul_i32 s49, s49, 10752\n"
    "s_and_b32 s51, s2, 1\n"
    "s_mul_i32 s51, s51, 384\n"
    "s_add_u32 s98, s49, s51\n"
    "v_bfe_u32 v32, v1, 2, 2\n"
    "v_mul_u32_u24 v32, 2688, v32\n"
    "v_and_b32 v33, 3, v1\n"
    "v_lshl_add_u32 v213, v33, 7, v32\n"
    "s_lshl3_add_u32 s48, s48, s6\n"
    "s_min_u32 s48, s48, 783\n"
    "s_lshl_b32 s99, s48, 9\n"
    "v_lshlrev_b32 v214, 7, v33\n"
    "s_cmp_eq_u32 s44, 0\n"
    "s_cbranch_scc1 .La_nk3_%=\n"
    "v_add_u32 v240, s43, v2\n"
    "s_xor_b32 s48, s5, 1\n"
    "s_mul_i32 s48, s48, 8704\n"
    "s_add_u32 s48, s48, 18688\n"
    "v_add_u32 v241, s48, v2\n"
    "s_and_b32 s48, s5, 2\n"
    "s_lshl_b32 s48, s48, 1\n"
    "v_mov_b32 v242, s48\n"
    "v_and_b32 v32, 31, v1\n"
    "v_lshrrev_b32 v33, 5, v1\n"
    "s_mov_b32 s52, 0\n"
    "s_mov_b32 s53, -1\n"
    "s_add_u32 s51, s43, 4096\n"
    "v_mul_u32_u24 v34, 272, v32\n"
    "v_lshl_add_u32 v34, v33, 4, v34\n"
    "v_add_u32 v223, s43, v34\n"
    "v_lshlrev_b32 v34, 4, v32\n"
    "v_lshl_add_u32 v35, v33, 3, v34\n"
    "v_add_u32 v243, s51, v35\n"
    "v_lshlrev_b32 v36, 3, v32\n"
    "v_add_u32 v36, 512, v36\n"
    "v_lshlrev_b32 v37, 2, v32\n"
    "v_add_u32 v37, 768, v37\n"
    "v_cndmask_b32 v37, v36, v37, s[52:53]\n"
    "v_add_u32 v244, s51, v37\n"
    "v_mul_u32_u24 v35, 124, v33\n"
    "v_add_u32 v245, v244, v35\n"
    "v_lshlrev_b32 v35, 1, v32\n"
    "v_lshl_add_u32 v35, v33, 8, v35\n"
    "v_add_u32 v246, s51, v35\n"
    "v_cndmask_b32 v34, v34, v36, s[52:53]\n"
    "v_add_u32 v247, s51, v34\n"
    "v_lshlrev_b32 v35, 3, v33\n"
    "v_sub_u32 v248, v247, v35\n"
    "v_lshl_add_u32 v249, v1, 2, s51\n"
    "v_lshl_add_u32 v250, v1, 1, s51\n"
    "s_mul_i32 s48, s5, 4576\n"
    "s_add_u32 s48, s48, 256\n"
    "v_add_u32 v251, s48, v2\n"
    "s_cmp_lg_u32 s42, 0\n"
    "s_mov_b32 s29, 50464004\n"
    "s_cmov_b32 s29, 84150022\n"
    "s_mov_b32 s30, 84150022\n"
    "s_cmov_b32 s30, 117833984\n"
    "s_mov_b32 s31, 16777986\n"
    "s_cmov_b32 s31, 50464004\n"
    "s_mov_b32 s32, 50464004\n"
    "s_cmov_b32 s32, 84149508\n"
    "s_mov_b32 s33, 16777472\n"
    "s_cmov_b32 s33, 16777986\n"
    "s_cselect_b32 s55, -1, -2\n"
    "s_mov_b32 s54, 0\n"
    "s_mov_b32 s49, 16777472\n"
    "s_cmov_b32 s49, 16778500\n"
    "s_mov_b32 s48, 16778500\n"
    "s_cmov_b32 s48, 84149508\n"
    "v_mov_b32 v34, s29\n"
    "v_mov_b32 v35, s30\n"
    "v_cndmask_b32 v252, v34, v35, s[52:53]\n"
    "v_mov_b32 v35, 16777472\n"
    "v_cndmask_b32 v253, v34, v35, s[52:53]\n"
    "v_mov_b32 v35, s31\n"
    "v_cndmask_b32 v254, v34, v35, s[52:53]\n"
    "v_mov_b32 v35, 0x05040504\n"
    "v_cndmask_b32 v255, v34, v35, s[54:55]\n"
    "v_mov_b32 v34, s49\n"
    "v_mov_b32 v35, s48\n"
    "s_mov_b32 vcc_lo, 0xaaaaaaaa\n"
    "s_mov_b32 vcc_hi, 0xaaaaaaaa\n"
    "v_cndmask_b32 v215, v34, v35, vcc\n"
    "s_mov_b32 s29, 16777472\n"
    "s_mov_b32 s30, 0x03020302\n"
    "s_mov_b32 s31, 0x05040504\n"
    "s_mov_b32 s22, 0x3fb8aa3b\n"
    "s_mov_b32 s24, 0x41000000\n"
    "s_mov_b32 s26, 0x43800000\n"
    "s_mov_b32 s48, 0xc3000000\n"
    "v_mov_b32 v52, 0x43000000\n"
    "s_waitcnt vmcnt(6)\n"
    "v_med3_f32 v48, v222, s48, v52\n"
    "v_cmp_u_f32 vcc, v222, v222\n"
    "v_mul_f32 v49, s22, v48\n"
    "v_fma_f32 v50, v48, s22, -v49\n"
    "v_rndne_f32 v51, v49\n"
    "v_fmamk_f32 v50, v48, 0x32a5705f, v50\n"
    "v_sub_f32 v49, v49, v51\n"
    "v_add_f32 v49, v49, v50\n"
    "v_exp_f32 v49, v49\n"
    "v_cvt_i32_f32 v51, v51\n"
    "v_ldexp_f32 v49, v49, v51\n"
    "v_cndmask_b32 v49, v49, v222, vcc\n"
    "v_cvt_pk_bf16_f32 v49, v49, v49\n"
    "v_lshlrev_b32 v222, 16, v49\n"
    "s_waitcnt vmcnt(0)\n"
    "v_lshlrev_b32 v138, 16, v216\n"
    "v_lshlrev_b32 v139, 16, v217\n"
    "v_lshlrev_b32 v140, 16, v218\n"
    "v_lshlrev_b32 v141, 16, v219\n"
    "v_lshlrev_b32 v142, 16, v220\n"
    "v_lshlrev_b32 v143, 16, v221\n"
    "v_mul_f32 v138, v222, v138\n"
    "v_mul_f32 v139, v222, v139\n"
    "v_mul_f32 v140, v222, v140\n"
    "v_mul_f32 v141, v222, v141\n"
    "v_mul_f32 v138, s22, v138\n"
    "v_mul_f32 v139, 0.15915494, v139\n"
    "v_mul_f32 v160, v142, v140\n"
    "v_mul_f32 v161, v143, v140\n"
    "v_mul_f32 v146, s26, v138\n"
    "v_mul_f32 v147, s26, v139\n"
    "v_mul_f32 v144, s24, v138\n"
    "v_mul_f32 v145, s24, v139\n"
    "v_fma_f32 v160, -v143, v141, v160\n"
    "v_fma_f32 v161, -v142, v141, -v161\n"
    "v_fract_f32 v139, v139\n"
    "v_fract_f32 v147, v147\n"
    "v_fract_f32 v145, v145\n"
    "v_exp_f32 v138, v138\n"
    "v_cos_f32 v148, v139\n"
    "v_sin_f32 v139, v139\n"
    "v_exp_f32 v146, v146\n"
    "v_cos_f32 v154, v147\n"
    "v_sin_f32 v147, v147\n"
    "v_exp_f32 v144, v144\n"
    "v_cos_f32 v151, v145\n"
    "v_sin_f32 v145, v145\n"
    "v_mul_f32 v148, v138, v148\n"
    "v_mul_f32 v150, v138, v139\n"
    "v_mul_f32 v149, -v138, v139\n"
    "v_mul_f32 v154, v146, v154\n"
    "v_mul_f32 v156, v146, v147\n"
    "v_mul_f32 v155, -v146, v147\n"
    "v_mul_f32 v151, v144, v151\n"
    "v_mul_f32 v152, v144, v145\n"
    "v_mul_f32 v153, -v144, v145\n"
    "v_mul_f32 v162, v160, v148\n"
    "v_mul_f32 v163, v161, v148\n"
    "v_mul_f32 v176, v160, v154\n"
    "v_mul_f32 v177, v161, v154\n"
    "v_mul_f32 v202, v151, v151\n"
    "v_mul_f32 v203, v152, v151\n"
    "v_cvt_pk_bf16_f32 v32, v160, v161\n"
    "v_cvt_pk_bf16_f32 v54, v151, v152\n"
    "v_mov_b32 v53, 0x3f80\n"
    "v_fmac_f32 v162, v161, v150\n"
    "v_fmac_f32 v163, v160, v149\n"
    "v_fmac_f32 v176, v161, v156\n"
    "v_fmac_f32 v177, v160, v155\n"
    "v_fmac_f32 v202, v152, v153\n"
    "v_fmac_f32 v203, v151, v152\n"
    "v_mul_f32 v164, v162, v148\n"
    "v_mul_f32 v165, v163, v148\n"
    "v_mul_f32 v178, v176, v154\n"
    "v_mul_f32 v179, v177, v154\n"
    "v_mul_f32 v204, v202, v151\n"
    "v_mul_f32 v205, v203, v151\n"
    "v_cvt_pk_bf16_f32 v33, v162, v163\n"
    "v_cvt_pk_bf16_f32 v40, v176, v177\n"
    "v_cvt_pk_bf16_f32 v55, v202, v203\n"
    "v_fmac_f32 v164, v163, v150\n"
    "v_fmac_f32 v165, v162, v149\n"
    "v_fmac_f32 v178, v177, v156\n"
    "v_fmac_f32 v179, v176, v155\n"
    "v_fmac_f32 v204, v203, v153\n"
    "v_fmac_f32 v205, v202, v152\n"
    "v_mul_f32 v166, v164, v148\n"
    "v_mul_f32 v167, v165, v148\n"
    "v_mul_f32 v180, v178, v154\n"
    "v_mul_f32 v181, v179, v154\n"
    "v_mul_f32 v192, v164, v154\n"
    "v_mul_f32 v193, v165, v154\n"
    "v_cvt_pk_bf16_f32 v34, v164, v165\n"
    "v_cvt_pk_bf16_f32 v44, v178, v179\n"
    "v_cvt_pk_bf16_f32 v56, v204, v205\n"
    "v_fmac_f32 v166, v165, v150\n"
    "v_fmac_f32 v167, v164, v149\n"
    "v_fmac_f32 v180, v179, v156\n"
    "v_fmac_f32 v181, v178, v155\n"
    "v_fmac_f32 v192, v165, v156\n"
    "v_fmac_f32 v193, v164, v155\n"
    "v_mul_f32 v168, v166, v148\n"
    "v_mul_f32 v169, v167, v148\n"
    "v_mul_f32 v182, v180, v154\n"
    "v_mul_f32 v183, v181, v154\n"
    "v_mul_f32 v157, v204, v151\n"
    "v_mul_f32 v158, v205, v151\n"
    "v_cvt_pk_bf16_f32 v35, v166, v167\n"
    "v_cvt_pk_bf16_f32 v46, v180, v181\n"
    "v_cvt_pk_bf16_f32 v41, v192, v193\n"
    "v_fmac_f32 v168, v167, v150\n"
    "v_fmac_f32 v169, v166, v149\n"
    "v_fmac_f32 v182, v181, v156\n"
    "v_fmac_f32 v183, v180, v155\n"
    "v_fmac_f32 v157, v205, v153\n"
    "v_fmac_f32 v158, v204, v152\n"
    "v_mul_f32 v170, v168, v148\n"
    "v_mul_f32 v171, v169, v148\n"
    "v_mul_f32 v184, v182, v154\n"
    "v_mul_f32 v185, v183, v154\n"
    "v_mul_f32 v194, v168, v154\n"
    "v_mul_f32 v195, v169, v154\n"
    "v_cvt_pk_bf16_f32 v36, v168, v169\n"
    "v_cvt_pk_bf16_f32 v48, v182, v183\n"
    "v_fmac_f32 v170, v169, v150\n"
    "v_fmac_f32 v171, v168, v149\n"
    "v_fmac_f32 v184, v183, v156\n"
    "v_fmac_f32 v185, v182, v155\n"
    "v_fmac_f32 v194, v169, v156\n"
    "v_fmac_f32 v195, v168, v155\n"
    "v_mul_f32 v172, v170, v148\n"
    "v_mul_f32 v173, v171, v148\n"
    "v_mul_f32 v186, v184, v154\n"
    "v_mul_f32 v187, v185, v154\n"
    "v_mul_f32 v198, v194, v154\n"
    "v_mul_f32 v199, v195, v154\n"
    "v_cvt_pk_bf16_f32 v37, v170, v171\n"
    "v_cvt_pk_bf16_f32 v49, v184, v185\n"
    "v_cvt_pk_bf16_f32 v42, v194, v195\n"
    "v_fmac_f32 v172, v171, v150\n"
    "v_fmac_f32 v173, v170, v149\n"
    "v_fmac_f32 v186, v185, v156\n"
    "v_fmac_f32 v187, v184, v155\n"
    "v_fmac_f32 v198, v195, v156\n"
    "v_fmac_f32 v199, v194, v155\n"
    "v_mul_f32 v174, v172, v148\n"
    "v_mul_f32 v175, v173, v148\n"
    "v_mul_f32 v188, v186, v154\n"
    "v_mul_f32 v189, v187, v154\n"
    "v_mul_f32 v196, v172, v154\n"
    "v_mul_f32 v197, v173, v154\n"
    "v_mul_f32 v200, v198, v154\n"
    "v_mul_f32 v201, v199, v154\n"
    "v_cvt_pk_bf16_f32 v38, v172, v173\n"
    "v_cvt_pk_bf16_f32 v50, v186, v187\n"
    "v_cvt_pk_bf16_f32 v45, v198, v199\n"
    "v_fmac_f32 v174, v173, v150\n"
    "v_fmac_f32 v175, v172, v149\n"
    "v_fmac_f32 v188, v187, v156\n"
    "v_fmac_f32 v189, v186, v155\n"
    "v_fmac_f32 v196, v173, v156\n"
    "v_fmac_f32 v197, v172, v155\n"
    "v_fmac_f32 v200, v199, v156\n"
    "v_fmac_f32 v201, v198, v155\n"
    "v_xor_b32 v159, 0x80000000, v158\n"
    "v_mul_f32 v190, v188, v154\n"
    "v_mul_f32 v191, v189, v154\n"
    "v_mul_f32 v160, v151, v157\n"
    "v_mul_f32 v161, v152, v157\n"
    "v_mul_f32 v162, v202, v157\n"
    "v_mul_f32 v163, v203, v157\n"
    "v_mul_f32 v164, v204, v157\n"
    "v_mul_f32 v165, v205, v157\n"
    "v_cvt_pk_bf16_f32 v39, v174, v175\n"
    "v_cvt_pk_bf16_f32 v51, v188, v189\n"
    "v_cvt_pk_bf16_f32 v43, v196, v197\n"
    "v_cvt_pk_bf16_f32 v47, v200, v201\n"
    "v_fmac_f32 v190, v189, v156\n"
    "v_fmac_f32 v191, v188, v155\n"
    "v_fmac_f32 v160, v152, v159\n"
    "v_fmac_f32 v161, v151, v158\n"
    "v_fmac_f32 v162, v203, v159\n"
    "v_fmac_f32 v163, v202, v158\n"
    "v_fmac_f32 v164, v205, v159\n"
    "v_fmac_f32 v165, v204, v158\n"
    "v_cvt_pk_bf16_f32 v52, v190, v191\n"
    "v_mul_f32 v166, v157, v157\n"
    "v_mul_f32 v167, v158, v157\n"
    "v_mul_f32 v168, v160, v157\n"
    "v_mul_f32 v169, v161, v157\n"
    "v_mul_f32 v170, v162, v157\n"
    "v_mul_f32 v171, v163, v157\n"
    "v_mul_f32 v176, v164, v157\n"
    "v_mul_f32 v177, v165, v157\n"
    "v_cvt_pk_bf16_f32 v57, v157, v158\n"
    "v_cvt_pk_bf16_f32 v58, v160, v161\n"
    "v_cvt_pk_bf16_f32 v59, v162, v163\n"
    "v_cvt_pk_bf16_f32 v60, v164, v165\n"
    "v_fmac_f32 v166, v158, v159\n"
    "v_fmac_f32 v167, v157, v158\n"
    "v_fmac_f32 v168, v161, v159\n"
    "v_fmac_f32 v169, v160, v158\n"
    "v_fmac_f32 v170, v163, v159\n"
    "v_fmac_f32 v171, v162, v158\n"
    "v_fmac_f32 v176, v165, v159\n"
    "v_fmac_f32 v177, v164, v158\n"
    "v_mul_f32 v178, v166, v157\n"
    "v_mul_f32 v179, v167, v157\n"
    "v_mul_f32 v160, v168, v157\n"
    "v_mul_f32 v161, v169, v157\n"
    "v_mul_f32 v162, v170, v157\n"
    "v_mul_f32 v163, v171, v157\n"
    "v_mul_f32 v164, v176, v157\n"
    "v_mul_f32 v165, v177, v157\n"
    "v_cvt_pk_bf16_f32 v61, v166, v167\n"
    "v_cvt_pk_bf16_f32 v62, v168, v169\n"
    "v_cvt_pk_bf16_f32 v63, v170, v171\n"
    "v_cvt_pk_bf16_f32 v64, v176, v177\n"
    "v_fmac_f32 v178, v167, v159\n"
    "v_fmac_f32 v179, v166, v158\n"
    "v_fmac_f32 v160, v169, v159\n"
    "v_fmac_f32 v161, v168, v158\n"
    "v_fmac_f32 v162, v171, v159\n"
    "v_fmac_f32 v163, v170, v158\n"
    "v_fmac_f32 v164, v177, v159\n"
    "v_fmac_f32 v165, v176, v158\n"
    "v_mul_f32 v166, v178, v157\n"
    "v_mul_f32 v167, v179, v157\n"
    "v_mul_f32 v168, v160, v157\n"
    "v_mul_f32 v169, v161, v157\n"
    "v_mul_f32 v170, v162, v157\n"
    "v_mul_f32 v171, v163, v157\n"
    "v_mul_f32 v176, v164, v157\n"
    "v_mul_f32 v177, v165, v157\n"
    "v_cvt_pk_bf16_f32 v65, v178, v179\n"
    "v_cvt_pk_bf16_f32 v66, v160, v161\n"
    "v_cvt_pk_bf16_f32 v67, v162, v163\n"
    "v_cvt_pk_bf16_f32 v68, v164, v165\n"
    "v_fmac_f32 v166, v179, v159\n"
    "v_fmac_f32 v167, v178, v158\n"
    "v_fmac_f32 v168, v161, v159\n"
    "v_fmac_f32 v169, v160, v158\n"
    "v_fmac_f32 v170, v163, v159\n"
    "v_fmac_f32 v171, v162, v158\n"
    "v_fmac_f32 v176, v165, v159\n"
    "v_fmac_f32 v177, v164, v158\n"
    "v_mul_f32 v178, v166, v157\n"
    "v_mul_f32 v179, v167, v157\n"
    "v_mul_f32 v160, v168, v157\n"
    "v_mul_f32 v161, v169, v157\n"
    "v_mul_f32 v162, v170, v157\n"
    "v_mul_f32 v163, v171, v157\n"
    "v_mul_f32 v164, v176, v157\n"
    "v_mul_f32 v165, v177, v157\n"
    "v_cvt_pk_bf16_f32 v69, v166, v167\n"
    "v_cvt_pk_bf16_f32 v70, v168, v169\n"
    "v_cvt_pk_bf16_f32 v71, v170, v171\n"
    "v_cvt_pk_bf16_f32 v72, v176, v177\n"
    "v_fmac_f32 v178, v167, v159\n"
    "v_fmac_f32 v179, v166, v158\n"
    "v_fmac_f32 v160, v169, v159\n"
    "v_fmac_f32 v161, v168, v158\n"
    "v_fmac_f32 v162, v171, v159\n"
    "v_fmac_f32 v163, v170, v158\n"
    "v_fmac_f32 v164, v177, v159\n"
    "v_fmac_f32 v165, v176, v158\n"
    "v_mul_f32 v166, v178, v157\n"
    "v_mul_f32 v167, v179, v157\n"
    "v_mul_f32 v168, v160, v157\n"
    "v_mul_f32 v169, v161, v157\n"
    "v_mul_f32 v170, v162, v157\n"
    "v_mul_f32 v171, v163, v157\n"
    "v_mul_f32 v176, v164, v157\n"
    "v_mul_f32 v177, v165, v157\n"
    "v_cvt_pk_bf16_f32 v73, v178, v179\n"
    "v_cvt_pk_bf16_f32 v74, v160, v161\n"
    "v_cvt_pk_bf16_f32 v75, v162, v163\n"
    "v_cvt_pk_bf16_f32 v76, v164, v165\n"
    "v_fmac_f32 v166, v179, v159\n"
    "v_fmac_f32 v167, v178, v158\n"
    "v_fmac_f32 v168, v161, v159\n"
    "v_fmac_f32 v169, v160, v158\n"
    "v_fmac_f32 v170, v163, v159\n"
    "v_fmac_f32 v171, v162, v158\n"
    "v_fmac_f32 v176, v165, v159\n"
    "v_fmac_f32 v177, v164, v158\n"
    "v_mul_f32 v178, v166, v157\n"
    "v_mul_f32 v179, v167, v157\n"
    "v_mul_f32 v160, v168, v157\n"
    "v_mul_f32 v161, v169, v157\n"
    "v_mul_f32 v162, v170, v157\n"
    "v_mul_f32 v163, v171, v157\n"
    "v_mul_f32 v164, v176, v157\n"
    "v_mul_f32 v165, v177, v157\n"
    "v_cvt_pk_bf16_f32 v77, v166, v167\n"
    "v_cvt_pk_bf16_f32 v78, v168, v169\n"
    "v_cvt_pk_bf16_f32 v79, v170, v171\n"
    "v_cvt_pk_bf16_f32 v80, v176, v177\n"
    "v_fmac_f32 v178, v167, v159\n"
    "v_fmac_f32 v179, v166, v158\n"
    "v_fmac_f32 v160, v169, v159\n"
    "v_fmac_f32 v161, v168, v158\n"
    "v_fmac_f32 v162, v171, v159\n"
    "v_fmac_f32 v163, v170, v158\n"
    "v_fmac_f32 v164, v177, v159\n"
    "v_fmac_f32 v165, v176, v158\n"
    "v_cvt_pk_bf16_f32 v81, v178, v179\n"
    "v_cvt_pk_bf16_f32 v82, v160, v161\n"
    "v_cvt_pk_bf16_f32 v83, v162, v163\n"
    "v_cvt_pk_bf16_f32 v84, v164, v165\n"
    "s_setprio 3\n"
    "s_cmp_eq_u32 s5, 0\n"
    "s_cbranch_scc0 .La_nk3_%=a\n"
    "buffer_load_dword v6, off, s[16:19], s50 sc1\n"
    ".La_nk3_%=a:\n"
    "s_mov_b32 m0, 160000\n"
    "s_mov_b64 exec, 0xffff\n"
    "buffer_load_dword v213, s[12:15], s98 offen lds\n"
    "s_mov_b64 exec, 15\n"
    "buffer_load_dword v214, s[36:39], s99 offen lds\n"
    "s_mov_b64 exec, -1\n"
    "s_setprio 1\n"
    "v_and_b32 v138, 0xffff0000, v216\n"
    "v_and_b32 v139, 0xffff0000, v217\n"
    "v_and_b32 v140, 0xffff0000, v218\n"
    "v_and_b32 v141, 0xffff0000, v219\n"
    "v_and_b32 v142, 0xffff0000, v220\n"
    "v_and_b32 v143, 0xffff0000, v221\n"
    "v_mul_f32 v138, v222, v138\n"
    "v_mul_f32 v139, v222, v139\n"
    "v_mul_f32 v140, v222, v140\n"
    "v_mul_f32 v141, v222, v141\n"
    "v_mul_f32 v138, s22, v138\n"
    "v_mul_f32 v139, 0.15915494, v139\n"
    "v_mul_f32 v160, v142, v140\n"
    "v_mul_f32 v161, v143, v140\n"
    "v_mul_f32 v146, s26, v138\n"
    "v_mul_f32 v147, s26, v139\n"
    "v_mul_f32 v144, s24, v138\n"
    "v_mul_f32 v145, s24, v139\n"
    "v_fma_f32 v160, -v143, v141, v160\n"
    "v_fma_f32 v161, -v142, v141, -v161\n"
    "v_fract_f32 v139, v139\n"
    "v_fract_f32 v147, v147\n"
    "v_fract_f32 v145, v145\n"
    "v_exp_f32 v138, v138\n"
    "v_cos_f32 v148, v139\n"
    "v_sin_f32 v139, v139\n"
    "v_exp_f32 v146, v146\n"
    "v_cos_f32 v154, v147\n"
    "v_sin_f32 v147, v147\n"
    "v_exp_f32 v144, v144\n"
    "v_cos_f32 v151, v145\n"
    "v_sin_f32 v145, v145\n"
    "v_mul_f32 v148, v138, v148\n"
    "v_mul_f32 v150, v138, v139\n"
    "v_mul_f32 v149, -v138, v139\n"
    "v_mul_f32 v154, v146, v154\n"
    "v_mul_f32 v156, v146, v147\n"
    "v_mul_f32 v155, -v146, v147\n"
    "v_mul_f32 v151, v144, v151\n"
    "v_mul_f32 v152, v144, v145\n"
    "v_mul_f32 v153, -v144, v145\n"
    "v_mul_f32 v162, v160, v148\n"
    "v_mul_f32 v163, v161, v148\n"
    "v_mul_f32 v176, v160, v154\n"
    "v_mul_f32 v177, v161, v154\n"
    "v_mul_f32 v202, v151, v151\n"
    "v_mul_f32 v203, v152, v151\n"
    "v_cvt_pk_bf16_f32 v85, v160, v161\n"
    "v_cvt_pk_bf16_f32 v107, v151, v152\n"
    "v_mov_b32 v106, 0x3f80\n"
    "v_fmac_f32 v162, v161, v150\n"
    "v_fmac_f32 v163, v160, v149\n"
    "v_fmac_f32 v176, v161, v156\n"
    "v_fmac_f32 v177, v160, v155\n"
    "v_fmac_f32 v202, v152, v153\n"
    "v_fmac_f32 v203, v151, v152\n"
    "v_mul_f32 v164, v162, v148\n"
    "v_mul_f32 v165, v163, v148\n"
    "v_mul_f32 v178, v176, v154\n"
    "v_mul_f32 v179, v177, v154\n"
    "v_mul_f32 v204, v202, v151\n"
    "v_mul_f32 v205, v203, v151\n"
    "v_cvt_pk_bf16_f32 v86, v162, v163\n"
    "v_cvt_pk_bf16_f32 v93, v176, v177\n"
    "v_cvt_pk_bf16_f32 v108, v202, v203\n"
    "v_fmac_f32 v164, v163, v150\n"
    "v_fmac_f32 v165, v162, v149\n"
    "v_fmac_f32 v178, v177, v156\n"
    "v_fmac_f32 v179, v176, v155\n"
    "v_fmac_f32 v204, v203, v153\n"
    "v_fmac_f32 v205, v202, v152\n"
    "v_mul_f32 v166, v164, v148\n"
    "v_mul_f32 v167, v165, v148\n"
    "v_mul_f32 v180, v178, v154\n"
    "v_mul_f32 v181, v179, v154\n"
    "v_mul_f32 v192, v164, v154\n"
    "v_mul_f32 v193, v165, v154\n"
    "v_cvt_pk_bf16_f32 v87, v164, v165\n"
    "v_cvt_pk_bf16_f32 v97, v178, v179\n"
    "v_cvt_pk_bf16_f32 v109, v204, v205\n"
    "v_fmac_f32 v166, v165, v150\n"
    "v_fmac_f32 v167, v164, v149\n"
    "v_fmac_f32 v180, v179, v156\n"
    "v_fmac_f32 v181, v178, v155\n"
    "v_fmac_f32 v192, v165, v156\n"
    "v_fmac_f32 v193, v164, v155\n"
    "v_mul_f32 v168, v166, v148\n"
    "v_mul_f32 v169, v167, v148\n"
    "v_mul_f32 v182, v180, v154\n"
    "v_mul_f32 v183, v181, v154\n"
    "v_mul_f32 v157, v204, v151\n"
    "v_mul_f32 v158, v205, v151\n"
    "v_cvt_pk_bf16_f32 v88, v166, v167\n"
    "v_cvt_pk_bf16_f32 v99, v180, v181\n"
    "v_cvt_pk_bf16_f32 v94, v192, v193\n"
    "v_fmac_f32 v168, v167, v150\n"
    "v_fmac_f32 v169, v166, v149\n"
    "v_fmac_f32 v182, v181, v156\n"
    "v_fmac_f32 v183, v180, v155\n"
    "v_fmac_f32 v157, v205, v153\n"
    "v_fmac_f32 v158, v204, v152\n"
    "v_mul_f32 v170, v168, v148\n"
    "v_mul_f32 v171, v169, v148\n"
    "v_mul_f32 v184, v182, v154\n"
    "v_mul_f32 v185, v183, v154\n"
    "v_mul_f32 v194, v168, v154\n"
    "v_mul_f32 v195, v169, v154\n"
    "v_cvt_pk_bf16_f32 v89, v168, v169\n"
    "v_cvt_pk_bf16_f32 v101, v182, v183\n"
    "v_fmac_f32 v170, v169, v150\n"
    "v_fmac_f32 v171, v168, v149\n"
    "v_fmac_f32 v184, v183, v156\n"
    "v_fmac_f32 v185, v182, v155\n"
    "v_fmac_f32 v194, v169, v156\n"
    "v_fmac_f32 v195, v168, v155\n"
    "v_mul_f32 v172, v170, v148\n"
    "v_mul_f32 v173, v171, v148\n"
    "v_mul_f32 v186, v184, v154\n"
    "v_mul_f32 v187, v185, v154\n"
    "v_mul_f32 v198, v194, v154\n"
    "v_mul_f32 v199, v195, v154\n"
    "v_cvt_pk_bf16_f32 v90, v170, v171\n"
    "v_cvt_pk_bf16_f32 v102, v184, v185\n"
    "v_cvt_pk_bf16_f32 v95, v194, v195\n"
    "v_fmac_f32 v172, v171, v150\n"
    "v_fmac_f32 v173, v170, v149\n"
    "v_fmac_f32 v186, v185, v156\n"
    "v_fmac_f32 v187, v184, v155\n"
    "v_fmac_f32 v198, v195, v156\n"
    "v_fmac_f32 v199, v194, v155\n"
    "v_mul_f32 v174, v172, v148\n"
    "v_mul_f32 v175, v173, v148\n"
    "v_mul_f32 v188, v186, v154\n"
    "v_mul_f32 v189, v187, v154\n"
    "v_mul_f32 v196, v172, v154\n"
    "v_mul_f32 v197, v173, v154\n"
    "v_mul_f32 v200, v198, v154\n"
    "v_mul_f32 v201, v199, v154\n"
    "v_cvt_pk_bf16_f32 v91, v172, v173\n"
    "v_cvt_pk_bf16_f32 v103, v186, v187\n"
    "v_cvt_pk_bf16_f32 v98, v198, v199\n"
    "v_fmac_f32 v174, v173, v150\n"
    "v_fmac_f32 v175, v172, v149\n"
    "v_fmac_f32 v188, v187, v156\n"
    "v_fmac_f32 v189, v186, v155\n"
    "v_fmac_f32 v196, v173, v156\n"
    "v_fmac_f32 v197, v172, v155\n"
    "v_fmac_f32 v200, v199, v156\n"
    "v_fmac_f32 v201, v198, v155\n"
    "v_xor_b32 v159, 0x80000000, v158\n"
    "v_mul_f32 v190, v188, v154\n"
    "v_mul_f32 v191, v189, v154\n"
    "v_mul_f32 v160, v151, v157\n"
    "v_mul_f32 v161, v152, v157\n"
    "v_mul_f32 v162, v202, v157\n"
    "v_mul_f32 v163, v203, v157\n"
    "v_mul_f32 v164, v204, v157\n"
    "v_mul_f32 v165, v205, v157\n"
    "v_cvt_pk_bf16_f32 v92, v174, v175\n"
    "v_cvt_pk_bf16_f32 v104, v188, v189\n"
    "v_cvt_pk_bf16_f32 v96, v196, v197\n"
    "v_cvt_pk_bf16_f32 v100, v200, v201\n"
    "v_fmac_f32 v190, v189, v156\n"
    "v_fmac_f32 v191, v188, v155\n"
    "v_fmac_f32 v160, v152, v159\n"
    "v_fmac_f32 v161, v151, v158\n"
    "v_fmac_f32 v162, v203, v159\n"
    "v_fmac_f32 v163, v202, v158\n"
    "v_fmac_f32 v164, v205, v159\n"
    "v_fmac_f32 v165, v204, v158\n"
    "v_cvt_pk_bf16_f32 v105, v190, v191\n"
    "v_mul_f32 v166, v157, v157\n"
    "v_mul_f32 v167, v158, v157\n"
    "v_mul_f32 v168, v160, v157\n"
    "v_mul_f32 v169, v161, v157\n"
    "v_mul_f32 v170, v162, v157\n"
    "v_mul_f32 v171, v163, v157\n"
    "v_mul_f32 v176, v164, v157\n"
    "v_mul_f32 v177, v165, v157\n"
    "v_cvt_pk_bf16_f32 v110, v157, v158\n"
    "v_cvt_pk_bf16_f32 v111, v160, v161\n"
    "v_cvt_pk_bf16_f32 v112, v162, v163\n"
    "v_cvt_pk_bf16_f32 v113, v164, v165\n"
    "v_fmac_f32 v166, v158, v159\n"
    "v_fmac_f32 v167, v157, v158\n"
    "v_fmac_f32 v168, v161, v159\n"
    "v_fmac_f32 v169, v160, v158\n"
    "v_fmac_f32 v170, v163, v159\n"
    "v_fmac_f32 v171, v162, v158\n"
    "v_fmac_f32 v176, v165, v159\n"
    "v_fmac_f32 v177, v164, v158\n"
    "v_mul_f32 v178, v166, v157\n"
    "v_mul_f32 v179, v167, v157\n"
    "v_mul_f32 v160, v168, v157\n"
    "v_mul_f32 v161, v169, v157\n"
    "v_mul_f32 v162, v170, v157\n"
    "v_mul_f32 v163, v171, v157\n"
    "v_mul_f32 v164, v176, v157\n"
    "v_mul_f32 v165, v177, v157\n"
    "v_cvt_pk_bf16_f32 v114, v166, v167\n"
    "v_cvt_pk_bf16_f32 v115, v168, v169\n"
    "v_cvt_pk_bf16_f32 v116, v170, v171\n"
    "v_cvt_pk_bf16_f32 v117, v176, v177\n"
    "v_fmac_f32 v178, v167, v159\n"
    "v_fmac_f32 v179, v166, v158\n"
    "v_fmac_f32 v160, v169, v159\n"
    "v_fmac_f32 v161, v168, v158\n"
    "v_fmac_f32 v162, v171, v159\n"
    "v_fmac_f32 v163, v170, v158\n"
    "v_fmac_f32 v164, v177, v159\n"
    "v_fmac_f32 v165, v176, v158\n"
    "v_mul_f32 v166, v178, v157\n"
    "v_mul_f32 v167, v179, v157\n"
    "v_mul_f32 v168, v160, v157\n"
    "v_mul_f32 v169, v161, v157\n"
    "v_mul_f32 v170, v162, v157\n"
    "v_mul_f32 v171, v163, v157\n"
    "v_mul_f32 v176, v164, v157\n"
    "v_mul_f32 v177, v165, v157\n"
    "v_cvt_pk_bf16_f32 v118, v178, v179\n"
    "v_cvt_pk_bf16_f32 v119, v160, v161\n"
    "v_cvt_pk_bf16_f32 v120, v162, v163\n"
    "v_cvt_pk_bf16_f32 v121, v164, v165\n"
    "v_fmac_f32 v166, v179, v159\n"
    "v_fmac_f32 v167, v178, v158\n"
    "v_fmac_f32 v168, v161, v159\n"
    "v_fmac_f32 v169, v160, v158\n"
    "v_fmac_f32 v170, v163, v159\n"
    "v_fmac_f32 v171, v162, v158\n"
    "v_fmac_f32 v176, v165, v159\n"
    "v_fmac_f32 v177, v164, v158\n"
    "v_mul_f32 v178, v166, v157\n"
    "v_mul_f32 v179, v167, v157\n"
    "v_mul_f32 v160, v168, v157\n"
    "v_mul_f32 v161, v169, v157\n"
    "v_mul_f32 v162, v170, v157\n"
    "v_mul_f32 v163, v171, v157\n"
    "v_mul_f32 v164, v176, v157\n"
    "v_mul_f32 v165, v177, v157\n"
    "v_cvt_pk_bf16_f32 v122, v166, v167\n"
    "v_cvt_pk_bf16_f32 v123, v168, v169\n"
    "v_cvt_pk_bf16_f32 v124, v170, v171\n"
    "v_cvt_pk_bf16_f32 v125, v176, v177\n"
    "v_fmac_f32 v178, v167, v159\n"
    "v_fmac_f32 v179, v166, v158\n"
    "v_fmac_f32 v160, v169, v159\n"
    "v_fmac_f32 v161, v168, v158\n"
    "v_fmac_f32 v162, v171, v159\n"
    "v_fmac_f32 v163, v170, v158\n"
    "v_fmac_f32 v164, v177, v159\n"
    "v_fmac_f32 v165, v176, v158\n"
    "v_mul_f32 v166, v178, v157\n"
    "v_mul_f32 v167, v179, v157\n"
    "v_mul_f32 v168, v160, v157\n"
    "v_mul_f32 v169, v161, v157\n"
    "v_mul_f32 v170, v162, v157\n"
    "v_mul_f32 v171, v163, v157\n"
    "v_mul_f32 v176, v164, v157\n"
    "v_mul_f32 v177, v165, v157\n"
    "v_cvt_pk_bf16_f32 v126, v178, v179\n"
    "v_cvt_pk_bf16_f32 v127, v160, v161\n"
    "v_cvt_pk_bf16_f32 v128, v162, v163\n"
    "v_cvt_pk_bf16_f32 v129, v164, v165\n"
    "v_fmac_f32 v166, v179, v159\n"
    "v_fmac_f32 v167, v178, v158\n"
    "v_fmac_f32 v168, v161, v159\n"
    "v_fmac_f32 v169, v160, v158\n"
    "v_fmac_f32 v170, v163, v159\n"
    "v_fmac_f32 v171, v162, v158\n"
    "v_fmac_f32 v176, v165, v159\n"
    "v_fmac_f32 v177, v164, v158\n"
    "v_mul_f32 v178, v166, v157\n"
    "v_mul_f32 v179, v167, v157\n"
    "v_mul_f32 v160, v168, v157\n"
    "v_mul_f32 v161, v169, v157\n"
    "v_mul_f32 v162, v170, v157\n"
    "v_mul_f32 v163, v171, v157\n"
    "v_mul_f32 v164, v176, v157\n"
    "v_mul_f32 v165, v177, v157\n"
    "v_cvt_pk_bf16_f32 v130, v166, v167\n"
    "v_cvt_pk_bf16_f32 v131, v168, v169\n"
    "v_cvt_pk_bf16_f32 v132, v170, v171\n"
    "v_cvt_pk_bf16_f32 v133, v176, v177\n"
    "v_fmac_f32 v178, v167, v159\n"
    "v_fmac_f32 v179, v166, v158\n"
    "v_fmac_f32 v160, v169, v159\n"
    "v_fmac_f32 v161, v168, v158\n"
    "v_fmac_f32 v162, v171, v159\n"
    "v_fmac_f32 v163, v170, v158\n"
    "v_fmac_f32 v164, v177, v159\n"
    "v_fmac_f32 v165, v176, v158\n"
    "v_cvt_pk_bf16_f32 v134, v178, v179\n"
    "v_cvt_pk_bf16_f32 v135, v160, v161\n"
    "v_cvt_pk_bf16_f32 v136, v162, v163\n"
    "v_cvt_pk_bf16_f32 v137, v164, v165\n"
    ".La_go6_%=:\n"
    "s_cmp_eq_u32 s5, 0\n"
    "s_cbranch_scc0 .La_ak7_%=\n"
    "s_setprio 3\n"
    "s_waitcnt vmcnt(0)\n"
    "v_cmp_eq_u32 vcc, v7, v6\n"
    "s_nop 0\n"
    "s_cbranch_vccnz .La_sl9_%=\n"
    ".La_ok8_%=:\n"
    "s_mov_b64 exec, 1\n"
    "ds_add_u32 v3, v4 offset:112\n"
    "s_mov_b64 exec, -1\n"
    "s_mov_b64 exec, 1\n"
    "s_and_b32 s48, s92, 0x380\n"
    "buffer_atomic_add v4, off, s[16:19], s48 offset:64 sc1\n"
    "s_mov_b64 exec, -1\n"
    ".La_ak7_%=:\n"
    "s_setprio 1\n"
    "s_cmp_eq_u32 s44, 0\n"
    "s_cbranch_scc1 .La_ne11_%=\n"
    "s_mov_b32 m0, s43\n"
    "s_nop 0\n"
    "ds_write_addtid_b32 v32 offset:0\n"
    "ds_write_addtid_b32 v33 offset:272\n"
    "ds_write_addtid_b32 v34 offset:544\n"
    "ds_write_addtid_b32 v35 offset:816\n"
    "ds_write_addtid_b32 v36 offset:1088\n"
    "ds_write_addtid_b32 v37 offset:1360\n"
    "ds_write_addtid_b32 v38 offset:1632\n"
    "ds_write_addtid_b32 v39 offset:1904\n"
    "ds_write_addtid_b32 v40 offset:2176\n"
    "ds_write_addtid_b32 v41 offset:2448\n"
    "ds_write_addtid_b32 v42 offset:2720\n"
    "ds_write_addtid_b32 v43 offset:2992\n"
    "ds_write_addtid_b32 v44 offset:3264\n"
    "ds_write_addtid_b32 v45 offset:3536\n"
    "ds_write_addtid_b32 v46 offset:3808\n"
    "ds_write_addtid_b32 v47 offset:4080\n"
    "ds_write_addtid_b32 v48 offset:4352\n"
    "ds_write_addtid_b32 v49 offset:4624\n"
    "ds_write_addtid_b32 v50 offset:4896\n"
    "ds_write_addtid_b32 v51 offset:5168\n"
    "ds_write_addtid_b32 v52 offset:5440\n"
    "ds_read_b128 v[140:143], v223 offset:0\n"
    "ds_read_b128 v[144:147], v223 offset:32\n"
    "ds_read_b128 v[148:151], v223 offset:64\n"
    "ds_read_b128 v[152:155], v223 offset:96\n"
    "ds_read_b128 v[156:159], v223 offset:128\n"
    "ds_read_b128 v[160:163], v223 offset:160\n"
    "ds_read_b128 v[164:167], v223 offset:192\n"
    "ds_read_b128 v[168:171], v223 offset:224\n"
    "ds_write_addtid_b32 v53 offset:0\n"
    "ds_write_addtid_b32 v54 offset:272\n"
    "ds_write_addtid_b32 v55 offset:544\n"
    "ds_write_addtid_b32 v56 offset:816\n"
    "ds_write_addtid_b32 v57 offset:1088\n"
    "ds_write_addtid_b32 v58 offset:1360\n"
    "ds_write_addtid_b32 v59 offset:1632\n"
    "ds_write_addtid_b32 v60 offset:1904\n"
    "ds_write_addtid_b32 v61 offset:2176\n"
    "ds_write_addtid_b32 v62 offset:2448\n"
    "ds_write_addtid_b32 v63 offset:2720\n"
    "ds_write_addtid_b32 v64 offset:2992\n"
    "ds_write_addtid_b32 v65 offset:3264\n"
    "ds_write_addtid_b32 v66 offset:3536\n"
    "ds_write_addtid_b32 v67 offset:3808\n"
    "ds_write_addtid_b32 v68 offset:4080\n"
    "ds_write_addtid_b32 v69 offset:4352\n"
    "ds_write_addtid_b32 v70 offset:4624\n"
    "ds_write_addtid_b32 v71 offset:4896\n"
    "ds_write_addtid_b32 v72 offset:5168\n"
    "ds_write_addtid_b32 v73 offset:5440\n"
    "ds_write_addtid_b32 v74 offset:5712\n"
    "ds_write_addtid_b32 v75 offset:5984\n"
    "ds_write_addtid_b32 v76 offset:6256\n"
    "ds_write_addtid_b32 v77 offset:6528\n"
    "ds_write_addtid_b32 v78 offset:6800\n"
    "ds_write_addtid_b32 v79 offset:7072\n"
    "ds_write_addtid_b32 v80 offset:7344\n"
    "ds_write_addtid_b32 v81 offset:7616\n"
    "ds_write_addtid_b32 v82 offset:7888\n"
    "ds_write_addtid_b32 v83 offset:8160\n"
    "ds_write_addtid_b32 v84 offset:8432\n"
    "ds_read_b128 v[172:175], v223 offset:0\n"
    "ds_read_b128 v[176:179], v223 offset:32\n"
    "ds_read_b128 v[180:183], v223 offset:64\n"
    "ds_read_b128 v[184:187], v223 offset:96\n"
    "ds_read_b128 v[188:191], v223 offset:128\n"
    "ds_read_b128 v[192:195], v223 offset:160\n"
    "ds_read_b128 v[196:199], v223 offset:192\n"
    "ds_read_b128 v[200:203], v223 offset:224\n"
    "ds_write_addtid_b32 v85 offset:0\n"
    "ds_write_addtid_b32 v86 offset:272\n"
    "ds_write_addtid_b32 v87 offset:544\n"
    "ds_write_addtid_b32 v88 offset:816\n"
    "ds_write_addtid_b32 v89 offset:1088\n"
    "ds_write_addtid_b32 v90 offset:1360\n"
    "s_waitcnt lgkmcnt(13)\n"
    "v_mfma_f32_32x32x16_bf16 v[224:239], v[140:143], v[172:175], 0\n"
    "ds_write_addtid_b32 v91 offset:1632\n"
    "ds_write_addtid_b32 v92 offset:1904\n"
    "s_waitcnt lgkmcnt(14)\n"
    "v_mfma_f32_32x32x16_bf16 v[224:239], v[144:147], v[176:179], v[224:239]\n"
    "ds_write_addtid_b32 v93 offset:2176\n"
    "ds_write_addtid_b32 v94 offset:2448\n"
    "s_waitcnt lgkmcnt(14)\n"
    "v_mfma_f32_32x32x16_bf16 v[224:239], v[148:151], v[180:183], v[224:239]\n"
    "ds_write_addtid_b32 v95 offset:2720\n"
    "ds_write_addtid_b32 v96 offset:2992\n"
    "v_mfma_f32_32x32x16_bf16 v[224:239], v[152:155], v[184:187], v[224:239]\n"
    "ds_write_addtid_b32 v97 offset:3264\n"
    "ds_write_addtid_b32 v98 offset:3536\n"
    "s_waitcnt lgkmcnt(14)\n"
    "v_mfma_f32_32x32x16_bf16 v[224:239], v[156:159], v[188:191], v[224:239]\n"
    "ds_write_addtid_b32 v99 offset:3808\n"
    "ds_write_addtid_b32 v100 offset:4080\n"
    "v_mfma_f32_32x32x16_bf16 v[224:239], v[160:163], v[192:195], v[224:239]\n"
    "ds_write_addtid_b32 v101 offset:4352\n"
    "ds_write_addtid_b32 v102 offset:4624\n"
    "v_mfma_f32_32x32x16_bf16 v[224:239], v[164:167], v[196:199], v[224:239]\n"
    "ds_write_addtid_b32 v103 offset:4896\n"
    "ds_write_addtid_b32 v104 offset:5168\n"
    "v_mfma_f32_32x32x16_bf16 v[224:239], v[168:171], v[200:203], v[224:239]\n"
    "ds_write_addtid_b32 v105 offset:5440\n"
    "ds_read_b128 v[140:143], v223 offset:0\n"
    "ds_read_b128 v[144:147], v223 offset:32\n"
    "ds_read_b128 v[148:151], v223 offset:64\n"
    "ds_read_b128 v[152:155], v223 offset:96\n"
    "ds_read_b128 v[156:159], v223 offset:128\n"
    "ds_read_b128 v[160:163], v223 offset:160\n"
    "ds_read_b128 v[164:167], v223 offset:192\n"
    "ds_read_b128 v[168:171], v223 offset:224\n"
    "ds_write_addtid_b32 v106 offset:0\n"
    "ds_write_addtid_b32 v107 offset:272\n"
    "ds_write_addtid_b32 v108 offset:544\n"
    "ds_write_addtid_b32 v109 offset:816\n"
    "ds_write_addtid_b32 v110 offset:1088\n"
    "ds_write_addtid_b32 v111 offset:1360\n"
    "ds_write_addtid_b32 v112 offset:1632\n"
    "ds_write_addtid_b32 v113 offset:1904\n"
    "ds_write_addtid_b32 v114 offset:2176\n"
    "ds_write_addtid_b32 v115 offset:2448\n"
    "ds_write_addtid_b32 v116 offset:2720\n"
    "ds_write_addtid_b32 v117 offset:2992\n"
    "ds_write_addtid_b32 v118 offset:3264\n"
    "ds_write_addtid_b32 v119 offset:3536\n"
    "ds_write_addtid_b32 v120 offset:3808\n"
    "ds_write_addtid_b32 v121 offset:4080\n"
    "ds_write_addtid_b32 v122 offset:4352\n"
    "ds_write_addtid_b32 v123 offset:4624\n"
    "ds_write_addtid_b32 v124 offset:4896\n"
    "ds_write_addtid_b32 v125 offset:5168\n"
    "ds_write_addtid_b32 v126 offset:5440\n"
    "ds_write_addtid_b32 v127 offset:5712\n"
    "ds_write_addtid_b32 v128 offset:5984\n"
    "ds_write_addtid_b32 v129 offset:6256\n"
    "ds_write_addtid_b32 v130 offset:6528\n"
    "ds_write_addtid_b32 v131 offset:6800\n"
    "ds_write_addtid_b32 v132 offset:7072\n"
    "ds_write_addtid_b32 v133 offset:7344\n"
    "ds_write_addtid_b32 v134 offset:7616\n"
    "ds_write_addtid_b32 v135 offset:7888\n"
    "ds_write_addtid_b32 v136 offset:8160\n"
    "ds_write_addtid_b32 v137 offset:8432\n"
    "ds_read_b128 v[172:175], v223 offset:0\n"
    "ds_read_b128 v[176:179], v223 offset:32\n"
    "ds_read_b128 v[180:183], v223 offset:64\n"
    "ds_read_b128 v[184:187], v223 offset:96\n"
    "ds_read_b128 v[188:191], v223 offset:128\n"
    "ds_read_b128 v[192:195], v223 offset:160\n"
    "ds_read_b128 v[196:199], v223 offset:192\n"
    "ds_read_b128 v[200:203], v223 offset:224\n"
    "s_waitcnt lgkmcnt(7)\n"
    "v_mfma_f32_32x32x16_bf16 v[224:239], v[140:143], v[172:175], v[224:239]\n"
    "s_waitcnt lgkmcnt(6)\n"
    "v_mfma_f32_32x32x16_bf16 v[224:239], v[144:147], v[176:179], v[224:239]\n"
    "s_waitcnt lgkmcnt(5)\n"
    "v_mfma_f32_32x32x16_bf16 v[224:239], v[148:151], v[180:183], v[224:239]\n"
    "s_waitcnt lgkmcnt(4)\n"
    "v_mfma_f32_32x32x16_bf16 v[224:239], v[152:155], v[184:187], v[224:239]\n"
    "s_waitcnt lgkmcnt(3)\n"
    "v_mfma_f32_32x32x16_bf16 v[224:239], v[156:159], v[188:191], v[224:239]\n"
    "s_waitcnt lgkmcnt(2)\n"
    "v_mfma_f32_32x32x16_bf16 v[224:239], v[160:163], v[192:195], v[224:239]\n"
    "s_waitcnt lgkmcnt(1)\n"
    "v_mfma_f32_32x32x16_bf16 v[224:239], v[164:167], v[196:199], v[224:239]\n"
    "s_waitcnt lgkmcnt(0)\n"
    "v_mfma_f32_32x32x16_bf16 v[224:239], v[168:171], v[200:203], v[224:239]\n"
    ".La_ne11_%=:\n"
    "v_mov_b64 v[64:65], -1\n"
    "v_mov_b64 v[66:67], -1\n"
    "v_mov_b64 v[32:33], 0\n"
    "v_mov_b64 v[34:35], 0\n"
    "v_mov_b64 v[36:37], 0\n"
    "v_mov_b64 v[38:39], 0\n"
    "v_mov_b64 v[40:41], 0\n"
    "v_mov_b64 v[42:43], 0\n"
    "v_mov_b64 v[44:45], 0\n"
    "v_mov_b64 v[46:47], 0\n"
    "v_mov_b64 v[48:49], 0\n"
    "v_mov_b64 v[50:51], 0\n"
    "v_mov_b64 v[52:53], 0\n"
    "v_mov_b64 v[54:55], 0\n"
    "v_mov_b64 v[56:57], 0\n"
    "v_mov_b64 v[58:59], 0\n"
    "v_mov_b64 v[60:61], 0\n"
    "v_mov_b64 v[62:63], 0\n"
    "s_cmp_eq_u32 s44, 0\n"
    "s_cbranch_scc1 .La_ne12_%=\n"
    "ds_write_b128 v251, v[32:35]\n"
    "ds_write_b128 v240, v[224:227] offset:0\n"
    "ds_write_b128 v240, v[228:231] offset:1024\n"
    "ds_write_b128 v240, v[232:235] offset:2048\n"
    "ds_write_b32 v249, v32\n"
    "s_mov_b64 exec, 1\n"
    "ds_add_u32 v242, v4 offset:132\n"
    "s_mov_b64 exec, -1\n"
    "ds_read_b32 v5, v242 offset:132\n"
    "s_waitcnt lgkmcnt(0)\n"
    "v_cmp_gt_u32 vcc, 2, v5\n"
    "s_nop 0\n"
    "s_cbranch_vccnz .La_sl14_%=\n"
    ".La_ok13_%=:\n"
    "ds_read_b128 v[68:71], v241 offset:0\n"
    "ds_read_b128 v[72:75], v241 offset:1024\n"
    "ds_read_b128 v[76:79], v241 offset:2048\n"
    "s_waitcnt lgkmcnt(2)\n"
    "v_add_f32 v224, v224, v68\n"
    "v_add_f32 v225, v225, v69\n"
    "v_add_f32 v226, v226, v70\n"
    "v_add_f32 v227, v227, v71\n"
    "s_waitcnt lgkmcnt(1)\n"
    "v_add_f32 v228, v228, v72\n"
    "v_add_f32 v229, v229, v73\n"
    "v_add_f32 v230, v230, v74\n"
    "v_add_f32 v231, v231, v75\n"
    "s_waitcnt lgkmcnt(0)\n"
    "v_add_f32 v232, v232, v76\n"
    "v_add_f32 v233, v233, v77\n"
    "v_add_f32 v234, v234, v78\n"
    "v_add_f32 v235, v235, v79\n"
    "v_cvt_pk_bf16_f32 v68, v224, v225\n"
    "v_cvt_pk_bf16_f32 v69, v226, v227\n"
    "v_cvt_pk_bf16_f32 v70, v228, v229\n"
    "v_cvt_pk_bf16_f32 v71, v230, v231\n"
    "v_cvt_pk_bf16_f32 v72, v232, v233\n"
    "v_cvt_pk_bf16_f32 v73, v234, v235\n"
    "ds_write_b64 v243, v[68:69] offset:16\n"
    "ds_write_b32 v244, v70 offset:16\n"
    "ds_write_b32 v245, v71 offset:20\n"
    "ds_write_b16 v246, v72 offset:1040\n"
    "ds_write_b16_d16_hi v246, v72 offset:1104\n"
    "ds_write_b16 v246, v73 offset:1168\n"
    "ds_write_b16_d16_hi v246, v73 offset:1232\n"
    "ds_read2_b32 v[76:77], v247 offset0:3 offset1:4\n"
    "ds_read2_b32 v[78:79], v247 offset0:5 offset1:6\n"
    "ds_read_b32 v80, v248 offset:28\n"
    "ds_read2_b32 v[82:83], v249 offset0:196 offset1:197\n"
    "ds_read_u16 v84, v250 offset:1040\n"
    "ds_read_u16 v85, v250 offset:1042\n"
    "ds_read_u16 v86, v250 offset:1168\n"
    "ds_read_u16 v87, v250 offset:1170\n"
    "s_waitcnt lgkmcnt(0)\n"
    "v_perm_b32 v88, v80, v79, v252\n"
    "v_perm_b32 v89, v79, v78, v253\n"
    "v_perm_b32 v90, v78, v77, v254\n"
    "v_perm_b32 v91, v77, v76, v255\n"
    "v_perm_b32 v92, v83, v82, s32\n"
    "v_perm_b32 v93, v83, v82, s30\n"
    "v_perm_b32 v94, v83, v82, s33\n"
    "v_perm_b32 v95, v83, v82, s29\n"
    "v_perm_b32 v96, v85, v84, s31\n"
    "v_perm_b32 v97, v85, v84, v215\n"
    "v_perm_b32 v98, v85, v84, s29\n"
    "v_mov_b32 v99, v98\n"
    "v_perm_b32 v100, v87, v86, s31\n"
    "v_perm_b32 v101, v87, v86, v215\n"
    "v_perm_b32 v102, v87, v86, s29\n"
    "v_mov_b32 v103, v102\n"
    "ds_write_b128 v251, v[88:91] offset:448\n"
    "ds_write_b128 v251, v[92:95] offset:1472\n"
    "ds_write_b128 v251, v[96:99] offset:2496\n"
    "ds_write_b128 v251, v[100:103] offset:3520\n"
    ".La_ne12_%=:\n"
    "s_mov_b64 exec, 1\n"
    "ds_add_u32 v3, v4 offset:140\n"
    "s_mov_b64 exec, -1\n"
    "s_lshl3_add_u32 s48, s2, s3\n"
    "s_cmp_eq_u32 s48, 61\n"
    "s_cbranch_scc1 .La_l316_%=\n"
    ".La_l417_%=:\n"
    "s_and_b32 s48, s5, 2\n"
    "s_lshl_b32 s48, s48, 1\n"
    "v_mov_b32 v7, s48\n"
    "ds_read_b32 v5, v3 offset:104\n"
    "s_waitcnt lgkmcnt(0)\n"
    "v_cmp_gt_u32 vcc, 8, v5\n"
    "s_nop 0\n"
    "s_cbranch_vccnz .La_sl20_%=\n"
    ".La_ok19_%=:\n"
    "s_waitcnt vmcnt(0)\n"
    "ds_read_b32 v5, v3 offset:140\n"
    "s_waitcnt lgkmcnt(0)\n"
    "v_cmp_gt_u32 vcc, 4, v5\n"
    "s_nop 0\n"
    "s_cbranch_vccnz .La_sl23_%=\n"
    ".La_ok22_%=:\n"
    "s_mov_b32 m0, s95\n"
    "s_add_u32 s49, s90, 33792\n"
    "global_load_lds_dwordx4 v9, s[20:21]\n"
    "global_load_lds_dwordx4 v10, s[20:21] offset:1024\n"
    "s_mov_b32 s51, 4160\n"
    "buffer_store_dwordx4 v[64:67], v2, s[16:19], s88 offen offset:0 sc1\n"
    "s_waitcnt vmcnt(2)\n"
    "buffer_store_dwordx4 v[64:67], v2, s[16:19], s88 offen offset:1024 sc1\n"
    "s_waitcnt vmcnt(2)\n"
    "s_mov_b64 exec, 1\n"
    "ds_add_u32 v3, v4 offset:124\n"
    "s_mov_b64 exec, -1\n"
    "ds_read_b32 v5, v3 offset:124\n"
    "s_waitcnt lgkmcnt(0)\n"
    "v_cmp_gt_u32 vcc, 4, v5\n"
    "s_nop 0\n"
    "s_cbranch_vccnz .La_sl26_%=\n"
    ".La_ok25_%=:\n"
    "ds_read_b64_tr_b16 v[68:69], v11 offset:0\n"
    "ds_read_b64_tr_b16 v[70:71], v11 offset:512\n"
    "ds_read_b64_tr_b16 v[72:73], v11 offset:1024\n"
    "ds_read_b64_tr_b16 v[74:75], v11 offset:1536\n"
    "s_waitcnt lgkmcnt(2)\n"
    "ds_write_b128 v12, v[68:71]\n"
    "s_waitcnt lgkmcnt(1)\n"
    "ds_write_b128 v12, v[72:75] offset:16\n"
    "ds_read_b128 v[76:79], v13\n"
    "ds_read_b128 v[80:83], v13 offset:1152\n"
    "buffer_store_dwordx4 v[64:67], v0, s[16:19], s89 offen offset:0 sc1\n"
    "s_waitcnt vmcnt(2)\n"
    "buffer_store_dwordx4 v[64:67], v0, s[16:19], s89 offen offset:1024 sc1\n"
    "s_waitcnt vmcnt(2)\n"
    "s_waitcnt lgkmcnt(1)\n"
    "buffer_store_dwordx4 v[76:79], v14, s[16:19], s90 offen sc1\n"
    "s_waitcnt vmcnt(2)\n"
    "s_waitcnt lgkmcnt(0)\n"
    "buffer_store_dwordx4 v[80:83], v14, s[16:19], s49 offen sc1\n"
    "s_add_u32 m0, s93, 0\n"
    "s_add_u32 s48, s94, 0\n"
    "s_waitcnt vmcnt(2)\n"
    "buffer_load_dwordx4 v16, s[36:39], s48 offen offset:0 lds\n"
    "s_waitcnt vmcnt(2)\n"
    "buffer_load_dwordx4 v17, s[36:39], s48 offen offset:1024 lds\n"
    "s_waitcnt vmcnt(2)\n"
    "s_mov_b64 exec, 1\n"
    "ds_add_rtn_u32 v5, v3, v4 offset:128\n"
    "s_waitcnt lgkmcnt(0)\n"
    "v_cmp_eq_u32 vcc, 3, v5\n"
    "s_nop 0\n"
    "s_cbranch_vccz .La_n228_%=\n"
    "s_mov_b64 exec, 3\n"
    "v_mov_b32 v5, v212\n"
    "buffer_atomic_add v5, v15, s[16:19], 0 offen offset:64 sc0 sc1\n"
    "s_waitcnt vmcnt(0)\n"
    "v_add_u32 v5, v5, v212\n"
    "v_cmp_eq_u32 vcc, 32, v5\n"
    "s_nop 0\n"
    "s_cbranch_vccz .La_n228_%=\n"
    "s_mov_b64 exec, vcc\n"
    "buffer_store_dword v4, v15, s[16:19], s51 offen sc1\n"
    ".La_n228_%=:\n"
    "s_mov_b64 exec, -1\n"
    "buffer_load_dwordx4 v18, s[36:39], s48 offen offset:2048 lds\n"
    "s_waitcnt vmcnt(2)\n"
    "buffer_load_dwordx4 v19, s[36:39], s48 offen offset:3072 lds\n"
    "s_add_u32 m0, s93, 4096\n"
    "s_add_u32 s48, s94, 14336\n"
    "s_waitcnt vmcnt(2)\n"
    "buffer_load_dwordx4 v16, s[36:39], s48 offen offset:0 lds\n"
    "s_waitcnt vmcnt(2)\n"
    "buffer_load_dwordx4 v17, s[36:39], s48 offen offset:1024 lds\n"
    "s_waitcnt vmcnt(2)\n"
    "buffer_load_dwordx4 v18, s[36:39], s48 offen offset:2048 lds\n"
    "s_waitcnt vmcnt(2)\n"
    "buffer_load_dwordx4 v19, s[36:39], s48 offen offset:3072 lds\n"
    "s_add_u32 m0, s93, 8192\n"
    "s_add_u32 s48, s94, 28672\n"
    "s_waitcnt vmcnt(2)\n"
    "buffer_load_dwordx4 v16, s[36:39], s48 offen offset:0 lds\n"
    "s_waitcnt vmcnt(2)\n"
    "buffer_load_dwordx4 v17, s[36:39], s48 offen offset:1024 lds\n"
    "s_waitcnt vmcnt(2)\n"
    "buffer_load_dwordx4 v18, s[36:39], s48 offen offset:2048 lds\n"
    "s_waitcnt vmcnt(2)\n"
    "buffer_load_dwordx4 v19, s[36:39], s48 offen offset:3072 lds\n"
    "s_add_u32 m0, s93, 12288\n"
    "s_add_u32 s48, s94, 43008\n"
    "s_waitcnt vmcnt(2)\n"
    "buffer_load_dwordx4 v16, s[36:39], s48 offen offset:0 lds\n"
    "s_waitcnt vmcnt(2)\n"
    "buffer_load_dwordx4 v17, s[36:39], s48 offen offset:1024 lds\n"
    "s_waitcnt vmcnt(0)\n"
    "s_mov_b64 exec, 1\n"
    "ds_add_u32 v3, v4 offset:144\n"
    "s_mov_b64 exec, -1\n"
    "ds_read_b32 v5, v3 offset:144\n"
    "s_waitcnt lgkmcnt(0)\n"
    "v_cmp_gt_u32 vcc, 4, v5\n"
    "s_nop 0\n"
    "s_cbranch_vccnz .La_sl30_%=\n"
    ".La_ok29_%=:\n"
    "buffer_load_dword v6, v15, s[16:19], s51 offen sc1\n"
    "ds_read_b64_tr_b16 v[128:129], v20 offset:0\n"
    "ds_read_b64_tr_b16 v[130:131], v20 offset:1024\n"
    "ds_read_b64_tr_b16 v[132:133], v20 offset:4096\n"
    "ds_read_b64_tr_b16 v[134:135], v21 offset:5120\n"
    "ds_read_b64_tr_b16 v[136:137], v20 offset:7168\n"
    "ds_read_b64_tr_b16 v[138:139], v20 offset:8192\n"
    "ds_read_b64_tr_b16 v[140:141], v20 offset:11264\n"
    "ds_read_b64_tr_b16 v[142:143], v21 offset:12288\n"
    "ds_read_b64_tr_b16 v[144:145], v20 offset:14336\n"
    "ds_read_b64_tr_b16 v[146:147], v20 offset:15360\n"
    "ds_read_b64_tr_b16 v[148:149], v20 offset:18432\n"
    "ds_read_b64_tr_b16 v[150:151], v21 offset:19456\n"
    "ds_read_b64_tr_b16 v[152:153], v20 offset:21504\n"
    "ds_read_b64_tr_b16 v[154:155], v20 offset:22528\n"
    "ds_read_b64_tr_b16 v[156:157], v20 offset:25600\n"
    "ds_read_b64_tr_b16 v[158:159], v21 offset:26624\n"
    "ds_read_b64_tr_b16 v[160:161], v20 offset:28672\n"
    "ds_read_b64_tr_b16 v[162:163], v20 offset:29696\n"
    "ds_read_b64_tr_b16 v[164:165], v20 offset:32768\n"
    "ds_read_b64_tr_b16 v[166:167], v21 offset:33792\n"
    "ds_read_b64_tr_b16 v[168:169], v20 offset:35840\n"
    "ds_read_b64_tr_b16 v[170:171], v20 offset:36864\n"
    "ds_read_b64_tr_b16 v[172:173], v20 offset:39936\n"
    "ds_read_b64_tr_b16 v[174:175], v21 offset:40960\n"
    "ds_read_b64_tr_b16 v[176:177], v20 offset:43008\n"
    "ds_read_b64_tr_b16 v[178:179], v20 offset:44032\n"
    "ds_read_b64_tr_b16 v[180:181], v20 offset:47104\n"
    "ds_read_b64_tr_b16 v[182:183], v21 offset:48128\n"
    "ds_read_b64_tr_b16 v[184:185], v20 offset:50176\n"
    "ds_read_b64_tr_b16 v[186:187], v20 offset:51200\n"
    "ds_read_b64_tr_b16 v[188:189], v20 offset:54272\n"
    "ds_read_b64_tr_b16 v[190:191], v21 offset:55296\n"
    "s_mov_b64 exec, 1\n"
    "ds_add_u32 v3, v4 offset:144\n"
    "s_mov_b64 exec, -1\n"
    "s_waitcnt vmcnt(0)\n"
    "v_cmp_ne_u32 vcc, 1, v6\n"
    "s_nop 0\n"
    "s_cbranch_vccnz .La_sl33_%=\n"
    ".La_ok32_%=:\n"
    "s_add_u32 m0, s96, 0\n"
    "s_add_u32 s48, s91, 0\n"
    "buffer_load_dwordx4 v0, s[16:19], s48 offen offset:0 sc1 lds\n"
    "buffer_load_dwordx4 v0, s[16:19], s48 offen offset:1024 sc1 lds\n"
    "buffer_load_dwordx4 v0, s[16:19], s48 offen offset:2048 sc1 lds\n"
    "buffer_load_dwordx4 v0, s[16:19], s48 offen offset:3072 sc1 lds\n"
    "s_add_u32 m0, s96, 4160\n"
    "s_add_u32 s48, s91, 4\n"
    "s_waitcnt vmcnt(3)\n"
    "buffer_load_dwordx4 v0, s[16:19], s48 offen offset:0 sc1 lds\n"
    "s_waitcnt vmcnt(3)\n"
    "buffer_load_dwordx4 v0, s[16:19], s48 offen offset:1024 sc1 lds\n"
    "s_waitcnt vmcnt(3)\n"
    "buffer_load_dwordx4 v0, s[16:19], s48 offen offset:2048 sc1 lds\n"
    "s_waitcnt vmcnt(3)\n"
    "buffer_load_dwordx4 v0, s[16:19], s48 offen offset:3072 sc1 lds\n"
    "ds_read_b32 v5, v3 offset:144\n"
    "s_waitcnt lgkmcnt(0)\n"
    "v_cmp_gt_u32 vcc, 8, v5\n"
    "s_nop 0\n"
    "s_cbranch_vccnz .La_sl36_%=\n"
    ".La_ok35_%=:\n"
    "s_add_u32 m0, s93, 0\n"
    "s_add_u32 s48, s94, 200704\n"
    "buffer_load_dwordx4 v16, s[36:39], s48 offen offset:0 lds\n"
    "buffer_load_dwordx4 v17, s[36:39], s48 offen offset:1024 lds\n"
    "s_waitcnt vmcnt(5)\n"
    "buffer_load_dwordx4 v18, s[36:39], s48 offen offset:2048 lds\n"
    "s_waitcnt vmcnt(5)\n"
    "buffer_load_dwordx4 v19, s[36:39], s48 offen offset:3072 lds\n"
    "s_add_u32 m0, s93, 4096\n"
    "s_add_u32 s48, s94, 215040\n"
    "s_waitcnt vmcnt(5)\n"
    "buffer_load_dwordx4 v16, s[36:39], s48 offen offset:0 lds\n"
    "s_waitcnt vmcnt(5)\n"
    "buffer_load_dwordx4 v17, s[36:39], s48 offen offset:1024 lds\n"
    "s_waitcnt vmcnt(5)\n"
    "buffer_load_dwordx4 v18, s[36:39], s48 offen offset:2048 lds\n"
    "ds_write_b32 v8, v3\n"
    "s_mov_b64 exec, 1\n"
    "ds_add_u32 v3, v4 offset:148\n"
    "s_mov_b64 exec, -1\n"
    "s_waitcnt vmcnt(5)\n"
    "buffer_load_dwordx4 v19, s[36:39], s48 offen offset:3072 lds\n"
    "s_add_u32 m0, s93, 8192\n"
    "s_add_u32 s48, s94, 229376\n"
    "s_waitcnt vmcnt(5)\n"
    "buffer_load_dwordx4 v16, s[36:39], s48 offen offset:0 lds\n"
    "s_waitcnt vmcnt(5)\n"
    "buffer_load_dwordx4 v17, s[36:39], s48 offen offset:1024 lds\n"
    "s_waitcnt vmcnt(5)\n"
    "buffer_load_dwordx4 v18, s[36:39], s48 offen offset:2048 lds\n"
    "s_waitcnt vmcnt(5)\n"
    "buffer_load_dwordx4 v19, s[36:39], s48 offen offset:3072 lds\n"
    "s_add_u32 m0, s93, 12288\n"
    "s_add_u32 s48, s94, 243712\n"
    "s_waitcnt vmcnt(5)\n"
    "buffer_load_dwordx4 v16, s[36:39], s48 offen offset:0 lds\n"
    "s_waitcnt vmcnt(5)\n"
    "buffer_load_dwordx4 v17, s[36:39], s48 offen offset:1024 lds\n"
    "ds_read_b32 v5, v3 offset:148\n"
    "s_waitcnt lgkmcnt(0)\n"
    "v_cmp_gt_u32 vcc, 4, v5\n"
    "s_nop 0\n"
    "s_cbranch_vccnz .La_sl39_%=\n"
    ".La_ok38_%=:\n"
    "ds_read_b128 v[192:195], v30 offset:0\n"
    "ds_read_b128 v[224:227], v31 offset:192\n"
    "s_waitcnt lgkmcnt(1)\n"
    "ds_read_b128 v[228:231], v31 offset:704\n"
    "s_waitcnt lgkmcnt(1)\n"
    "ds_read_b128 v[232:235], v31 offset:1216\n"
    "s_waitcnt lgkmcnt(1)\n"
    "ds_read_b128 v[236:239], v31 offset:1728\n"
    "s_waitcnt lgkmcnt(1)\n"
    "ds_read_b128 v[240:243], v31 offset:2240\n"
    "s_waitcnt lgkmcnt(1)\n"
    "ds_read_b128 v[244:247], v31 offset:2752\n"
    "s_waitcnt lgkmcnt(1)\n"
    "ds_read_b128 v[248:251], v31 offset:3264\n"
    "s_waitcnt lgkmcnt(1)\n"
    "ds_read_b128 v[252:255], v31 offset:3776\n"
    "s_waitcnt lgkmcnt(1)\n"
    "ds_read_b128 v[196:199], v30 offset:512\n"
    "s_waitcnt lgkmcnt(1)\n"
    "ds_read_b128 v[200:203], v30 offset:1024\n"
    "s_waitcnt lgkmcnt(1)\n"
    "ds_read_b128 v[204:207], v30 offset:1536\n"
    "s_waitcnt lgkmcnt(1)\n"
    "ds_read_b128 v[208:211], v30 offset:2048\n"
    "s_waitcnt lgkmcnt(1)\n"
    "ds_read_b128 v[212:215], v30 offset:2560\n"
    "s_waitcnt lgkmcnt(1)\n"
    "ds_read_b128 v[216:219], v30 offset:3072\n"
    "s_waitcnt lgkmcnt(1)\n"
    "ds_read_b128 v[220:223], v30 offset:3584\n"
    "s_waitcnt lgkmcnt(0)\n"
    "s_waitcnt vmcnt(0)\n"
    "s_barrier\n"
    "s_cmp_eq_u32 s44, 0\n"
    "s_cbranch_scc1 .La_n442_%=\n"
    "v_mfma_f32_16x16x32_bf16 v[32:35], v[192:195], v[224:227], v[32:35]\n"
    "v_mfma_f32_16x16x32_bf16 v[36:39], v[192:195], v[228:231], v[36:39]\n"
    "ds_read_b128 v[64:67], v30 offset:64\n"
    "v_mfma_f32_16x16x32_bf16 v[40:43], v[192:195], v[232:235], v[40:43]\n"
    "ds_read_b128 v[96:99], v31 offset:128\n"
    "v_mfma_f32_16x16x32_bf16 v[44:47], v[192:195], v[236:239], v[44:47]\n"
    "ds_read_b128 v[100:103], v31 offset:640\n"
    "v_mfma_f32_16x16x32_bf16 v[48:51], v[192:195], v[240:243], v[48:51]\n"
    "ds_read_b128 v[104:107], v31 offset:1152\n"
    "v_mfma_f32_16x16x32_bf16 v[52:55], v[192:195], v[244:247], v[52:55]\n"
    "ds_read_b128 v[108:111], v31 offset:1664\n"
    "v_mfma_f32_16x16x32_bf16 v[56:59], v[192:195], v[248:251], v[56:59]\n"
    "ds_read_b128 v[112:115], v31 offset:2176\n"
    "v_mfma_f32_16x16x32_bf16 v[60:63], v[192:195], v[252:255], v[60:63]\n"
    "ds_read_b128 v[116:119], v31 offset:2688\n"
    "v_mfma_f32_16x16x32_bf16 v[36:39], v[196:199], v[224:227], v[36:39]\n"
    "ds_read_b128 v[120:123], v31 offset:3200\n"
    "v_mfma_f32_16x16x32_bf16 v[40:43], v[196:199], v[228:231], v[40:43]\n"
    "ds_read_b128 v[124:127], v31 offset:3712\n"
    "v_mfma_f32_16x16x32_bf16 v[44:47], v[196:199], v[232:235], v[44:47]\n"
    "ds_read_b128 v[68:71], v30 offset:576\n"
    "v_mfma_f32_16x16x32_bf16 v[48:51], v[196:199], v[236:239], v[48:51]\n"
    "ds_read_b128 v[72:75], v30 offset:1088\n"
    "v_mfma_f32_16x16x32_bf16 v[52:55], v[196:199], v[240:243], v[52:55]\n"
    "ds_read_b128 v[76:79], v30 offset:1600\n"
    "v_mfma_f32_16x16x32_bf16 v[56:59], v[196:199], v[244:247], v[56:59]\n"
    "ds_read_b128 v[80:83], v30 offset:2112\n"
    "v_mfma_f32_16x16x32_bf16 v[60:63], v[196:199], v[248:251], v[60:63]\n"
    "ds_read_b128 v[84:87], v30 offset:2624\n"
    "v_mfma_f32_16x16x32_bf16 v[40:43], v[200:203], v[224:227], v[40:43]\n"
    "ds_read_b128 v[88:91], v30 offset:3136\n"
    "v_mfma_f32_16x16x32_bf16 v[44:47], v[200:203], v[228:231], v[44:47]\n"
    "ds_read_b128 v[92:95], v30 offset:3648\n"
    "v_mfma_f32_16x16x32_bf16 v[48:51], v[200:203], v[232:235], v[48:51]\n"
    "v_mfma_f32_16x16x32_bf16 v[52:55], v[200:203], v[236:239], v[52:55]\n"
    "v_mfma_f32_16x16x32_bf16 v[56:59], v[200:203], v[240:243], v[56:59]\n"
    "v_mfma_f32_16x16x32_bf16 v[60:63], v[200:203], v[244:247], v[60:63]\n"
    "v_mfma_f32_16x16x32_bf16 v[44:47], v[204:207], v[224:227], v[44:47]\n"
    "v_mfma_f32_16x16x32_bf16 v[48:51], v[204:207], v[228:231], v[48:51]\n"
    "v_mfma_f32_16x16x32_bf16 v[52:55], v[204:207], v[232:235], v[52:55]\n"
    "v_mfma_f32_16x16x32_bf16 v[56:59], v[204:207], v[236:239], v[56:59]\n"
    "v_mfma_f32_16x16x32_bf16 v[60:63], v[204:207], v[240:243], v[60:63]\n"
    "v_mfma_f32_16x16x32_bf16 v[48:51], v[208:211], v[224:227], v[48:51]\n"
    "v_mfma_f32_16x16x32_bf16 v[52:55], v[208:211], v[228:231], v[52:55]\n"
    "v_mfma_f32_16x16x32_bf16 v[56:59], v[208:211], v[232:235], v[56:59]\n"
    "v_mfma_f32_16x16x32_bf16 v[60:63], v[208:211], v[236:239], v[60:63]\n"
    "v_mfma_f32_16x16x32_bf16 v[52:55], v[212:215], v[224:227], v[52:55]\n"
    "v_mfma_f32_16x16x32_bf16 v[56:59], v[212:215], v[228:231], v[56:59]\n"
    "v_mfma_f32_16x16x32_bf16 v[60:63], v[212:215], v[232:235], v[60:63]\n"
    "v_mfma_f32_16x16x32_bf16 v[56:59], v[216:219], v[224:227], v[56:59]\n"
    "s_waitcnt lgkmcnt(0)\n"
    "v_mfma_f32_16x16x32_bf16 v[60:63], v[216:219], v[228:231], v[60:63]\n"
    "v_mfma_f32_16x16x32_bf16 v[60:63], v[220:223], v[224:227], v[60:63]\n"
    "v_mfma_f32_16x16x32_bf16 v[32:35], v[64:67], v[96:99], v[32:35]\n"
    "v_mfma_f32_16x16x32_bf16 v[36:39], v[64:67], v[100:103], v[36:39]\n"
    "ds_read_b128 v[192:195], v30 offset:128\n"
    "v_mfma_f32_16x16x32_bf16 v[40:43], v[64:67], v[104:107], v[40:43]\n"
    "ds_read_b128 v[224:227], v31 offset:64\n"
    "v_mfma_f32_16x16x32_bf16 v[44:47], v[64:67], v[108:111], v[44:47]\n"
    "ds_read_b128 v[228:231], v31 offset:576\n"
    "v_mfma_f32_16x16x32_bf16 v[48:51], v[64:67], v[112:115], v[48:51]\n"
    "ds_read_b128 v[232:235], v31 offset:1088\n"
    "v_mfma_f32_16x16x32_bf16 v[52:55], v[64:67], v[116:119], v[52:55]\n"
    "ds_read_b128 v[236:239], v31 offset:1600\n"
    "v_mfma_f32_16x16x32_bf16 v[56:59], v[64:67], v[120:123], v[56:59]\n"
    "ds_read_b128 v[240:243], v31 offset:2112\n"
    "v_mfma_f32_16x16x32_bf16 v[60:63], v[64:67], v[124:127], v[60:63]\n"
    "ds_read_b128 v[244:247], v31 offset:2624\n"
    "v_mfma_f32_16x16x32_bf16 v[36:39], v[68:71], v[96:99], v[36:39]\n"
    "ds_read_b128 v[248:251], v31 offset:3136\n"
    "v_mfma_f32_16x16x32_bf16 v[40:43], v[68:71], v[100:103], v[40:43]\n"
    "ds_read_b128 v[252:255], v31 offset:3648\n"
    "v_mfma_f32_16x16x32_bf16 v[44:47], v[68:71], v[104:107], v[44:47]\n"
    "ds_read_b128 v[196:199], v30 offset:640\n"
    "v_mfma_f32_16x16x32_bf16 v[48:51], v[68:71], v[108:111], v[48:51]\n"
    "ds_read_b128 v[200:203], v30 offset:1152\n"
    "v_mfma_f32_16x16x32_bf16 v[52:55], v[68:71], v[112:115], v[52:55]\n"
    "ds_read_b128 v[204:207], v30 offset:1664\n"
    "v_mfma_f32_16x16x32_bf16 v[56:59], v[68:71], v[116:119], v[56:59]\n"
    "ds_read_b128 v[208:211], v30 offset:2176\n"
    "v_mfma_f32_16x16x32_bf16 v[60:63], v[68:71], v[120:123], v[60:63]\n"
    "ds_read_b128 v[212:215], v30 offset:2688\n"
    "v_mfma_f32_16x16x32_bf16 v[40:43], v[72:75], v[96:99], v[40:43]\n"
    "ds_read_b128 v[216:219], v30 offset:3200\n"
    "v_mfma_f32_16x16x32_bf16 v[44:47], v[72:75], v[100:103], v[44:47]\n"
    "ds_read_b128 v[220:223], v30 offset:3712\n"
    "v_mfma_f32_16x16x32_bf16 v[48:51], v[72:75], v[104:107], v[48:51]\n"
    "v_mfma_f32_16x16x32_bf16 v[52:55], v[72:75], v[108:111], v[52:55]\n"
    "v_mfma_f32_16x16x32_bf16 v[56:59], v[72:75], v[112:115], v[56:59]\n"
    "v_mfma_f32_16x16x32_bf16 v[60:63], v[72:75], v[116:119], v[60:63]\n"
    "v_mfma_f32_16x16x32_bf16 v[44:47], v[76:79], v[96:99], v[44:47]\n"
    "v_mfma_f32_16x16x32_bf16 v[48:51], v[76:79], v[100:103], v[48:51]\n"
    "v_mfma_f32_16x16x32_bf16 v[52:55], v[76:79], v[104:107], v[52:55]\n"
    "v_mfma_f32_16x16x32_bf16 v[56:59], v[76:79], v[108:111], v[56:59]\n"
    "v_mfma_f32_16x16x32_bf16 v[60:63], v[76:79], v[112:115], v[60:63]\n"
    "v_mfma_f32_16x16x32_bf16 v[48:51], v[80:83], v[96:99], v[48:51]\n"
    "v_mfma_f32_16x16x32_bf16 v[52:55], v[80:83], v[100:103], v[52:55]\n"
    "v_mfma_f32_16x16x32_bf16 v[56:59], v[80:83], v[104:107], v[56:59]\n"
    "v_mfma_f32_16x16x32_bf16 v[60:63], v[80:83], v[108:111], v[60:63]\n"
    "v_mfma_f32_16x16x32_bf16 v[52:55], v[84:87], v[96:99], v[52:55]\n"
    "v_mfma_f32_16x16x32_bf16 v[56:59], v[84:87], v[100:103], v[56:59]\n"
    "v_mfma_f32_16x16x32_bf16 v[60:63], v[84:87], v[104:107], v[60:63]\n"
    "v_mfma_f32_16x16x32_bf16 v[56:59], v[88:91], v[96:99], v[56:59]\n"
    "s_waitcnt lgkmcnt(0)\n"
    "v_mfma_f32_16x16x32_bf16 v[60:63], v[88:91], v[100:103], v[60:63]\n"
    "v_mfma_f32_16x16x32_bf16 v[60:63], v[92:95], v[96:99], v[60:63]\n"
    "v_mfma_f32_16x16x32_bf16 v[32:35], v[192:195], v[224:227], v[32:35]\n"
    "v_mfma_f32_16x16x32_bf16 v[36:39], v[192:195], v[228:231], v[36:39]\n"
    "ds_read_b128 v[64:67], v30 offset:192\n"
    "v_mfma_f32_16x16x32_bf16 v[40:43], v[192:195], v[232:235], v[40:43]\n"
    "ds_read_b128 v[96:99], v31 offset:0\n"
    "v_mfma_f32_16x16x32_bf16 v[44:47], v[192:195], v[236:239], v[44:47]\n"
    "ds_read_b128 v[100:103], v31 offset:512\n"
    "v_mfma_f32_16x16x32_bf16 v[48:51], v[192:195], v[240:243], v[48:51]\n"
    "ds_read_b128 v[104:107], v31 offset:1024\n"
    "v_mfma_f32_16x16x32_bf16 v[52:55], v[192:195], v[244:247], v[52:55]\n"
    "ds_read_b128 v[108:111], v31 offset:1536\n"
    "v_mfma_f32_16x16x32_bf16 v[56:59], v[192:195], v[248:251], v[56:59]\n"
    "ds_read_b128 v[112:115], v31 offset:2048\n"
    "v_mfma_f32_16x16x32_bf16 v[60:63], v[192:195], v[252:255], v[60:63]\n"
    "ds_read_b128 v[116:119], v31 offset:2560\n"
    "v_mfma_f32_16x16x32_bf16 v[36:39], v[196:199], v[224:227], v[36:39]\n"
    "ds_read_b128 v[120:123], v31 offset:3072\n"
    "v_mfma_f32_16x16x32_bf16 v[40:43], v[196:199], v[228:231], v[40:43]\n"
    "ds_read_b128 v[124:127], v31 offset:3584\n"
    "v_mfma_f32_16x16x32_bf16 v[44:47], v[196:199], v[232:235], v[44:47]\n"
    "ds_read_b128 v[68:71], v30 offset:704\n"
    "v_mfma_f32_16x16x32_bf16 v[48:51], v[196:199], v[236:239], v[48:51]\n"
    "ds_read_b128 v[72:75], v30 offset:1216\n"
    "v_mfma_f32_16x16x32_bf16 v[52:55], v[196:199], v[240:243], v[52:55]\n"
    "ds_read_b128 v[76:79], v30 offset:1728\n"
    "v_mfma_f32_16x16x32_bf16 v[56:59], v[196:199], v[244:247], v[56:59]\n"
    "ds_read_b128 v[80:83], v30 offset:2240\n"
    "v_mfma_f32_16x16x32_bf16 v[60:63], v[196:199], v[248:251], v[60:63]\n"
    "ds_read_b128 v[84:87], v30 offset:2752\n"
    "v_mfma_f32_16x16x32_bf16 v[40:43], v[200:203], v[224:227], v[40:43]\n"
    "ds_read_b128 v[88:91], v30 offset:3264\n"
    "v_mfma_f32_16x16x32_bf16 v[44:47], v[200:203], v[228:231], v[44:47]\n"
    "ds_read_b128 v[92:95], v30 offset:3776\n"
    "v_mfma_f32_16x16x32_bf16 v[48:51], v[200:203], v[232:235], v[48:51]\n"
    "v_mfma_f32_16x16x32_bf16 v[52:55], v[200:203], v[236:239], v[52:55]\n"
    "v_mfma_f32_16x16x32_bf16 v[56:59], v[200:203], v[240:243], v[56:59]\n"
    "v_mfma_f32_16x16x32_bf16 v[60:63], v[200:203], v[244:247], v[60:63]\n"
    "v_mfma_f32_16x16x32_bf16 v[44:47], v[204:207], v[224:227], v[44:47]\n"
    "v_mfma_f32_16x16x32_bf16 v[48:51], v[204:207], v[228:231], v[48:51]\n"
    "v_mfma_f32_16x16x32_bf16 v[52:55], v[204:207], v[232:235], v[52:55]\n"
    "v_mfma_f32_16x16x32_bf16 v[56:59], v[204:207], v[236:239], v[56:59]\n"
    "v_mfma_f32_16x16x32_bf16 v[60:63], v[204:207], v[240:243], v[60:63]\n"
    "v_mfma_f32_16x16x32_bf16 v[48:51], v[208:211], v[224:227], v[48:51]\n"
    "v_mfma_f32_16x16x32_bf16 v[52:55], v[208:211], v[228:231], v[52:55]\n"
    "v_mfma_f32_16x16x32_bf16 v[56:59], v[208:211], v[232:235], v[56:59]\n"
    "v_mfma_f32_16x16x32_bf16 v[60:63], v[208:211], v[236:239], v[60:63]\n"
    "v_mfma_f32_16x16x32_bf16 v[52:55], v[212:215], v[224:227], v[52:55]\n"
    "v_mfma_f32_16x16x32_bf16 v[56:59], v[212:215], v[228:231], v[56:59]\n"
    "v_mfma_f32_16x16x32_bf16 v[60:63], v[212:215], v[232:235], v[60:63]\n"
    "v_mfma_f32_16x16x32_bf16 v[56:59], v[216:219], v[224:227], v[56:59]\n"
    "s_waitcnt lgkmcnt(0)\n"
    "v_mfma_f32_16x16x32_bf16 v[60:63], v[216:219], v[228:231], v[60:63]\n"
    "v_mfma_f32_16x16x32_bf16 v[60:63], v[220:223], v[224:227], v[60:63]\n"
    "ds_read_b32 v5, v3 offset:116\n"
    "s_cmp_lg_u32 s42, 0\n"
    "s_cbranch_scc1 .La_p143_%=\n"
    "v_mfma_f32_16x16x32_bf16 v[60:63], v[64:67], v[124:127], v[60:63]\n"
    "v_mfma_f32_16x16x32_bf16 v[60:63], v[68:71], v[120:123], v[60:63]\n"
    "v_mfma_f32_16x16x32_bf16 v[60:63], v[72:75], v[116:119], v[60:63]\n"
    "v_mfma_f32_16x16x32_bf16 v[60:63], v[76:79], v[112:115], v[60:63]\n"
    "v_mfma_f32_16x16x32_bf16 v[60:63], v[80:83], v[108:111], v[60:63]\n"
    "v_mfma_f32_16x16x32_bf16 v[60:63], v[84:87], v[104:107], v[60:63]\n"
    "s_waitcnt lgkmcnt(0)\n"
    "v_cmp_gt_u32 vcc, 4, v5\n"
    "s_nop 0\n"
    "s_cbranch_vccnz .La_sl46_%=\n"
    ".La_ok45_%=:\n"
    "v_mfma_f32_16x16x32_bf16 v[60:63], v[88:91], v[100:103], v[60:63]\n"
    "v_mfma_f32_16x16x32_bf16 v[60:63], v[92:95], v[96:99], v[60:63]\n"
    "v_mfma_f32_16x16x32_bf16 v[56:59], v[64:67], v[120:123], v[56:59]\n"
    "v_mfma_f32_16x16x32_bf16 v[56:59], v[68:71], v[116:119], v[56:59]\n"
    "v_mfma_f32_16x16x32_bf16 v[56:59], v[72:75], v[112:115], v[56:59]\n"
    "v_mfma_f32_16x16x32_bf16 v[56:59], v[76:79], v[108:111], v[56:59]\n"
    "v_mfma_f32_16x16x32_bf16 v[56:59], v[80:83], v[104:107], v[56:59]\n"
    "v_mfma_f32_16x16x32_bf16 v[56:59], v[84:87], v[100:103], v[56:59]\n"
    "v_mfma_f32_16x16x32_bf16 v[56:59], v[88:91], v[96:99], v[56:59]\n"
    "v_mfma_f32_16x16x32_bf16 v[52:55], v[64:67], v[116:119], v[52:55]\n"
    "ds_write_b128 v22, v[60:63] offset:3072\n"
    "v_mfma_f32_16x16x32_bf16 v[52:55], v[68:71], v[112:115], v[52:55]\n"
    "v_mfma_f32_16x16x32_bf16 v[52:55], v[72:75], v[108:111], v[52:55]\n"
    "v_mfma_f32_16x16x32_bf16 v[52:55], v[76:79], v[104:107], v[52:55]\n"
    "v_mfma_f32_16x16x32_bf16 v[52:55], v[80:83], v[100:103], v[52:55]\n"
    "v_mfma_f32_16x16x32_bf16 v[52:55], v[84:87], v[96:99], v[52:55]\n"
    "v_mfma_f32_16x16x32_bf16 v[48:51], v[64:67], v[112:115], v[48:51]\n"
    "v_mfma_f32_16x16x32_bf16 v[48:51], v[68:71], v[108:111], v[48:51]\n"
    "ds_write_b128 v22, v[56:59] offset:2048\n"
    "v_mfma_f32_16x16x32_bf16 v[48:51], v[72:75], v[104:107], v[48:51]\n"
    "v_mfma_f32_16x16x32_bf16 v[48:51], v[76:79], v[100:103], v[48:51]\n"
    "v_mfma_f32_16x16x32_bf16 v[48:51], v[80:83], v[96:99], v[48:51]\n"
    "v_mfma_f32_16x16x32_bf16 v[44:47], v[64:67], v[108:111], v[44:47]\n"
    "v_mfma_f32_16x16x32_bf16 v[44:47], v[68:71], v[104:107], v[44:47]\n"
    "v_mfma_f32_16x16x32_bf16 v[44:47], v[72:75], v[100:103], v[44:47]\n"
    "ds_write_b128 v22, v[52:55] offset:1024\n"
    "v_mfma_f32_16x16x32_bf16 v[44:47], v[76:79], v[96:99], v[44:47]\n"
    "v_mfma_f32_16x16x32_bf16 v[40:43], v[64:67], v[104:107], v[40:43]\n"
    "v_mfma_f32_16x16x32_bf16 v[40:43], v[68:71], v[100:103], v[40:43]\n"
    "v_mfma_f32_16x16x32_bf16 v[40:43], v[72:75], v[96:99], v[40:43]\n"
    "v_mfma_f32_16x16x32_bf16 v[36:39], v[64:67], v[100:103], v[36:39]\n"
    "ds_write_b128 v22, v[48:51] offset:0\n"
    "s_mov_b64 exec, 1\n"
    "ds_add_u32 v7, v4 offset:152\n"
    "s_mov_b64 exec, -1\n"
    "v_mfma_f32_16x16x32_bf16 v[36:39], v[68:71], v[96:99], v[36:39]\n"
    "v_mfma_f32_16x16x32_bf16 v[32:35], v[64:67], v[96:99], v[32:35]\n"
    "ds_read_b32 v5, v7 offset:152\n"
    "s_waitcnt lgkmcnt(0)\n"
    "v_cmp_gt_u32 vcc, 2, v5\n"
    "s_nop 0\n"
    "s_cbranch_vccnz .La_sl49_%=\n"
    ".La_ok48_%=:\n"
    "ds_read_b128 v[64:67], v23 offset:0\n"
    "ds_read_b128 v[68:71], v23 offset:1024\n"
    "ds_read_b128 v[72:75], v23 offset:2048\n"
    "ds_read_b128 v[76:79], v23 offset:3072\n"
    "s_waitcnt lgkmcnt(3)\n"
    "v_add_f32 v64, v64, v32\n"
    "v_add_f32 v65, v65, v33\n"
    "v_add_f32 v66, v66, v34\n"
    "v_add_f32 v67, v67, v35\n"
    "s_waitcnt lgkmcnt(2)\n"
    "v_add_f32 v68, v68, v36\n"
    "v_add_f32 v69, v69, v37\n"
    "v_add_f32 v70, v70, v38\n"
    "v_add_f32 v71, v71, v39\n"
    "v_cvt_pk_bf16_f32 v80, v64, v65\n"
    "v_cvt_pk_bf16_f32 v81, v66, v67\n"
    "v_cvt_pk_bf16_f32 v82, v68, v69\n"
    "v_cvt_pk_bf16_f32 v83, v70, v71\n"
    "v_min_u32 v80, -2, v80\n"
    "v_min_u32 v81, -2, v81\n"
    "v_min_u32 v82, -2, v82\n"
    "v_min_u32 v83, -2, v83\n"
    "buffer_store_dwordx4 v[80:83], v2, s[16:19], s89 offen offset:0 sc1\n"
    "s_waitcnt lgkmcnt(1)\n"
    "v_add_f32 v72, v72, v40\n"
    "v_add_f32 v73, v73, v41\n"
    "v_add_f32 v74, v74, v42\n"
    "v_add_f32 v75, v75, v43\n"
    "s_waitcnt lgkmcnt(0)\n"
    "v_add_f32 v76, v76, v44\n"
    "v_add_f32 v77, v77, v45\n"
    "v_add_f32 v78, v78, v46\n"
    "v_add_f32 v79, v79, v47\n"
    "v_cvt_pk_bf16_f32 v84, v72, v73\n"
    "v_cvt_pk_bf16_f32 v85, v74, v75\n"
    "v_cvt_pk_bf16_f32 v86, v76, v77\n"
    "v_cvt_pk_bf16_f32 v87, v78, v79\n"
    "v_min_u32 v84, -2, v84\n"
    "v_min_u32 v85, -2, v85\n"
    "v_min_u32 v86, -2, v86\n"
    "v_min_u32 v87, -2, v87\n"
    "buffer_store_dwordx4 v[84:87], v2, s[16:19], s89 offen offset:1024 sc1\n"
    "s_branch .La_pj44_%=\n"
    ".La_p143_%=:\n"
    "v_mfma_f32_16x16x32_bf16 v[32:35], v[64:67], v[96:99], v[32:35]\n"
    "v_mfma_f32_16x16x32_bf16 v[36:39], v[64:67], v[100:103], v[36:39]\n"
    "v_mfma_f32_16x16x32_bf16 v[36:39], v[68:71], v[96:99], v[36:39]\n"
    "v_mfma_f32_16x16x32_bf16 v[40:43], v[64:67], v[104:107], v[40:43]\n"
    "v_mfma_f32_16x16x32_bf16 v[40:43], v[68:71], v[100:103], v[40:43]\n"
    "v_mfma_f32_16x16x32_bf16 v[40:43], v[72:75], v[96:99], v[40:43]\n"
    "s_waitcnt lgkmcnt(0)\n"
    "v_cmp_gt_u32 vcc, 4, v5\n"
    "s_nop 0\n"
    "s_cbranch_vccnz .La_sl52_%=\n"
    ".La_ok51_%=:\n"
    "v_mfma_f32_16x16x32_bf16 v[44:47], v[64:67], v[108:111], v[44:47]\n"
    "v_mfma_f32_16x16x32_bf16 v[44:47], v[68:71], v[104:107], v[44:47]\n"
    "v_mfma_f32_16x16x32_bf16 v[44:47], v[72:75], v[100:103], v[44:47]\n"
    "ds_write_b128 v22, v[32:35] offset:0\n"
    "v_mfma_f32_16x16x32_bf16 v[44:47], v[76:79], v[96:99], v[44:47]\n"
    "v_mfma_f32_16x16x32_bf16 v[48:51], v[64:67], v[112:115], v[48:51]\n"
    "ds_write_b128 v22, v[36:39] offset:1024\n"
    "v_mfma_f32_16x16x32_bf16 v[48:51], v[68:71], v[108:111], v[48:51]\n"
    "v_mfma_f32_16x16x32_bf16 v[48:51], v[72:75], v[104:107], v[48:51]\n"
    "v_mfma_f32_16x16x32_bf16 v[48:51], v[76:79], v[100:103], v[48:51]\n"
    "ds_write_b128 v22, v[40:43] offset:2048\n"
    "v_mfma_f32_16x16x32_bf16 v[48:51], v[80:83], v[96:99], v[48:51]\n"
    "v_mfma_f32_16x16x32_bf16 v[52:55], v[64:67], v[116:119], v[52:55]\n"
    "v_mfma_f32_16x16x32_bf16 v[52:55], v[68:71], v[112:115], v[52:55]\n"
    "v_mfma_f32_16x16x32_bf16 v[52:55], v[72:75], v[108:111], v[52:55]\n"
    "ds_write_b128 v22, v[44:47] offset:3072\n"
    "s_mov_b64 exec, 1\n"
    "ds_add_u32 v7, v4 offset:152\n"
    "s_mov_b64 exec, -1\n"
    "v_mfma_f32_16x16x32_bf16 v[52:55], v[76:79], v[104:107], v[52:55]\n"
    "v_mfma_f32_16x16x32_bf16 v[52:55], v[80:83], v[100:103], v[52:55]\n"
    "v_mfma_f32_16x16x32_bf16 v[52:55], v[84:87], v[96:99], v[52:55]\n"
    "v_mfma_f32_16x16x32_bf16 v[56:59], v[64:67], v[120:123], v[56:59]\n"
    "v_mfma_f32_16x16x32_bf16 v[56:59], v[68:71], v[116:119], v[56:59]\n"
    "v_mfma_f32_16x16x32_bf16 v[56:59], v[72:75], v[112:115], v[56:59]\n"
    "v_mfma_f32_16x16x32_bf16 v[56:59], v[76:79], v[108:111], v[56:59]\n"
    "v_mfma_f32_16x16x32_bf16 v[56:59], v[80:83], v[104:107], v[56:59]\n"
    "v_mfma_f32_16x16x32_bf16 v[56:59], v[84:87], v[100:103], v[56:59]\n"
    "v_mfma_f32_16x16x32_bf16 v[56:59], v[88:91], v[96:99], v[56:59]\n"
    "v_mfma_f32_16x16x32_bf16 v[60:63], v[64:67], v[124:127], v[60:63]\n"
    "v_mfma_f32_16x16x32_bf16 v[60:63], v[68:71], v[120:123], v[60:63]\n"
    "v_mfma_f32_16x16x32_bf16 v[60:63], v[72:75], v[116:119], v[60:63]\n"
    "v_mfma_f32_16x16x32_bf16 v[60:63], v[76:79], v[112:115], v[60:63]\n"
    "v_mfma_f32_16x16x32_bf16 v[60:63], v[80:83], v[108:111], v[60:63]\n"
    "v_mfma_f32_16x16x32_bf16 v[60:63], v[84:87], v[104:107], v[60:63]\n"
    "v_mfma_f32_16x16x32_bf16 v[60:63], v[88:91], v[100:103], v[60:63]\n"
    "v_mfma_f32_16x16x32_bf16 v[60:63], v[92:95], v[96:99], v[60:63]\n"
    "ds_read_b32 v5, v7 offset:152\n"
    "s_waitcnt lgkmcnt(0)\n"
    "v_cmp_gt_u32 vcc, 2, v5\n"
    "s_nop 0\n"
    "s_cbranch_vccnz .La_sl55_%=\n"
    ".La_ok54_%=:\n"
    "ds_read_b128 v[64:67], v23 offset:0\n"
    "ds_read_b128 v[68:71], v23 offset:1024\n"
    "ds_read_b128 v[72:75], v23 offset:2048\n"
    "ds_read_b128 v[76:79], v23 offset:3072\n"
    "s_waitcnt lgkmcnt(3)\n"
    "v_add_f32 v64, v64, v48\n"
    "v_add_f32 v65, v65, v49\n"
    "v_add_f32 v66, v66, v50\n"
    "v_add_f32 v67, v67, v51\n"
    "s_waitcnt lgkmcnt(2)\n"
    "v_add_f32 v68, v68, v52\n"
    "v_add_f32 v69, v69, v53\n"
    "v_add_f32 v70, v70, v54\n"
    "v_add_f32 v71, v71, v55\n"
    "v_cvt_pk_bf16_f32 v80, v64, v65\n"
    "v_cvt_pk_bf16_f32 v81, v66, v67\n"
    "v_cvt_pk_bf16_f32 v82, v68, v69\n"
    "v_cvt_pk_bf16_f32 v83, v70, v71\n"
    "v_min_u32 v80, -2, v80\n"
    "v_min_u32 v81, -2, v81\n"
    "v_min_u32 v82, -2, v82\n"
    "v_min_u32 v83, -2, v83\n"
    "buffer_store_dwordx4 v[80:83], v2, s[16:19], s89 offen offset:0 sc1\n"
    "s_waitcnt lgkmcnt(1)\n"
    "v_add_f32 v72, v72, v56\n"
    "v_add_f32 v73, v73, v57\n"
    "v_add_f32 v74, v74, v58\n"
    "v_add_f32 v75, v75, v59\n"
    "s_waitcnt lgkmcnt(0)\n"
    "v_add_f32 v76, v76, v60\n"
    "v_add_f32 v77, v77, v61\n"
    "v_add_f32 v78, v78, v62\n"
    "v_add_f32 v79, v79, v63\n"
    "v_cvt_pk_bf16_f32 v84, v72, v73\n"
    "v_cvt_pk_bf16_f32 v85, v74, v75\n"
    "v_cvt_pk_bf16_f32 v86, v76, v77\n"
    "v_cvt_pk_bf16_f32 v87, v78, v79\n"
    "v_min_u32 v84, -2, v84\n"
    "v_min_u32 v85, -2, v85\n"
    "v_min_u32 v86, -2, v86\n"
    "v_min_u32 v87, -2, v87\n"
    "buffer_store_dwordx4 v[84:87], v2, s[16:19], s89 offen offset:1024 sc1\n"
    ".La_pj44_%=:\n"
    ".La_n442_%=:\n"
    "ds_read_b64_tr_b16 v[192:193], v20 offset:0\n"
    "ds_read_b64_tr_b16 v[194:195], v20 offset:1024\n"
    "ds_read_b64_tr_b16 v[196:197], v20 offset:4096\n"
    "ds_read_b64_tr_b16 v[198:199], v21 offset:5120\n"
    "ds_read_b64_tr_b16 v[200:201], v20 offset:7168\n"
    "ds_read_b64_tr_b16 v[202:203], v20 offset:8192\n"
    "ds_read_b64_tr_b16 v[204:205], v20 offset:11264\n"
    "ds_read_b64_tr_b16 v[206:207], v21 offset:12288\n"
    "ds_read_b64_tr_b16 v[208:209], v20 offset:14336\n"
    "ds_read_b64_tr_b16 v[210:211], v20 offset:15360\n"
    "ds_read_b64_tr_b16 v[212:213], v20 offset:18432\n"
    "ds_read_b64_tr_b16 v[214:215], v21 offset:19456\n"
    "ds_read_b64_tr_b16 v[216:217], v20 offset:21504\n"
    "ds_read_b64_tr_b16 v[218:219], v20 offset:22528\n"
    "ds_read_b64_tr_b16 v[220:221], v20 offset:25600\n"
    "ds_read_b64_tr_b16 v[222:223], v21 offset:26624\n"
    "ds_read_b64_tr_b16 v[224:225], v20 offset:28672\n"
    "ds_read_b64_tr_b16 v[226:227], v20 offset:29696\n"
    "ds_read_b64_tr_b16 v[228:229], v20 offset:32768\n"
    "ds_read_b64_tr_b16 v[230:231], v21 offset:33792\n"
    "ds_read_b64_tr_b16 v[232:233], v20 offset:35840\n"
    "ds_read_b64_tr_b16 v[234:235], v20 offset:36864\n"
    "ds_read_b64_tr_b16 v[236:237], v20 offset:39936\n"
    "ds_read_b64_tr_b16 v[238:239], v21 offset:40960\n"
    "ds_read_b64_tr_b16 v[240:241], v20 offset:43008\n"
    "ds_read_b64_tr_b16 v[242:243], v20 offset:44032\n"
    "ds_read_b64_tr_b16 v[244:245], v20 offset:47104\n"
    "ds_read_b64_tr_b16 v[246:247], v21 offset:48128\n"
    "ds_read_b64_tr_b16 v[248:249], v20 offset:50176\n"
    "ds_read_b64_tr_b16 v[250:251], v20 offset:51200\n"
    "ds_read_b64_tr_b16 v[252:253], v20 offset:54272\n"
    "ds_read_b64_tr_b16 v[254:255], v21 offset:55296\n"
    "s_barrier\n"
    "ds_read_b128 v[96:99], v24 offset:0\n"
    "ds_read_b128 v[100:103], v24 offset:1024\n"
    "ds_read_b128 v[104:107], v24 offset:2048\n"
    "ds_read_b128 v[108:111], v24 offset:3072\n"
    "ds_read_b128 v[112:115], v24 offset:4096\n"
    "ds_read_b128 v[116:119], v24 offset:5120\n"
    "s_waitcnt lgkmcnt(5)\n"
    "v_mfma_f32_32x32x16_bf16 v[32:47], v[128:131], v[96:99], 0\n"
    "ds_read_b128 v[120:123], v24 offset:6144\n"
    "s_waitcnt lgkmcnt(5)\n"
    "v_mfma_f32_32x32x16_bf16 v[32:47], v[132:135], v[100:103], v[32:47]\n"
    "ds_read_b128 v[124:127], v24 offset:7168\n"
    "s_waitcnt lgkmcnt(5)\n"
    "v_mfma_f32_32x32x16_bf16 v[32:47], v[136:139], v[104:107], v[32:47]\n"
    "ds_read_b128 v[96:99], v24 offset:8192\n"
    "s_waitcnt lgkmcnt(5)\n"
    "v_mfma_f32_32x32x16_bf16 v[32:47], v[140:143], v[108:111], v[32:47]\n"
    "ds_read_b128 v[100:103], v24 offset:9216\n"
    "s_waitcnt lgkmcnt(5)\n"
    "v_mfma_f32_32x32x16_bf16 v[32:47], v[144:147], v[112:115], v[32:47]\n"
    "ds_read_b128 v[104:107], v24 offset:10240\n"
    "s_waitcnt lgkmcnt(5)\n"
    "v_mfma_f32_32x32x16_bf16 v[32:47], v[148:151], v[116:119], v[32:47]\n"
    "ds_read_b128 v[108:111], v24 offset:11264\n"
    "s_waitcnt lgkmcnt(5)\n"
    "v_mfma_f32_32x32x16_bf16 v[32:47], v[152:155], v[120:123], v[32:47]\n"
    "ds_read_b128 v[112:115], v24 offset:12288\n"
    "s_waitcnt lgkmcnt(5)\n"
    "v_mfma_f32_32x32x16_bf16 v[32:47], v[156:159], v[124:127], v[32:47]\n"
    "ds_read_b128 v[116:119], v24 offset:13312\n"
    "s_waitcnt lgkmcnt(5)\n"
    "v_mfma_f32_32x32x16_bf16 v[32:47], v[160:163], v[96:99], v[32:47]\n"
    "ds_read_b128 v[120:123], v24 offset:14336\n"
    "s_waitcnt lgkmcnt(5)\n"
    "v_mfma_f32_32x32x16_bf16 v[32:47], v[164:167], v[100:103], v[32:47]\n"
    "ds_read_b128 v[124:127], v24 offset:15360\n"
    "s_waitcnt lgkmcnt(5)\n"
    "v_mfma_f32_32x32x16_bf16 v[32:47], v[168:171], v[104:107], v[32:47]\n"
    "ds_read_b128 v[96:99], v24 offset:16384\n"
    "s_waitcnt lgkmcnt(5)\n"
    "v_mfma_f32_32x32x16_bf16 v[32:47], v[172:175], v[108:111], v[32:47]\n"
    "ds_read_b128 v[100:103], v24 offset:17408\n"
    "s_waitcnt lgkmcnt(5)\n"
    "v_mfma_f32_32x32x16_bf16 v[32:47], v[176:179], v[112:115], v[32:47]\n"
    "ds_read_b128 v[104:107], v24 offset:18432\n"
    "s_waitcnt lgkmcnt(5)\n"
    "v_mfma_f32_32x32x16_bf16 v[32:47], v[180:183], v[116:119], v[32:47]\n"
    "ds_read_b128 v[108:111], v24 offset:19456\n"
    "s_waitcnt lgkmcnt(5)\n"
    "v_mfma_f32_32x32x16_bf16 v[32:47], v[184:187], v[120:123], v[32:47]\n"
    "ds_read_b128 v[112:115], v24 offset:20480\n"
    "s_waitcnt lgkmcnt(5)\n"
    "v_mfma_f32_32x32x16_bf16 v[32:47], v[188:191], v[124:127], v[32:47]\n"
    "ds_read_b128 v[116:119], v24 offset:21504\n"
    "s_waitcnt lgkmcnt(5)\n"
    "v_mfma_f32_32x32x16_bf16 v[32:47], v[192:195], v[96:99], v[32:47]\n"
    "ds_read_b128 v[120:123], v24 offset:22528\n"
    "s_waitcnt lgkmcnt(5)\n"
    "v_mfma_f32_32x32x16_bf16 v[32:47], v[196:199], v[100:103], v[32:47]\n"
    "ds_read_b128 v[124:127], v24 offset:23552\n"
    "s_waitcnt lgkmcnt(5)\n"
    "v_mfma_f32_32x32x16_bf16 v[32:47], v[200:203], v[104:107], v[32:47]\n"
    "ds_read_b128 v[96:99], v24 offset:24576\n"
    "s_waitcnt lgkmcnt(5)\n"
    "v_mfma_f32_32x32x16_bf16 v[32:47], v[204:207], v[108:111], v[32:47]\n"
    "ds_read_b128 v[100:103], v24 offset:25600\n"
    "s_waitcnt lgkmcnt(5)\n"
    "v_mfma_f32_32x32x16_bf16 v[32:47], v[208:211], v[112:115], v[32:47]\n"
    "ds_read_b128 v[104:107], v24 offset:26624\n"
    "s_waitcnt lgkmcnt(5)\n"
    "v_mfma_f32_32x32x16_bf16 v[32:47], v[212:215], v[116:119], v[32:47]\n"
    "ds_read_b128 v[108:111], v24 offset:27648\n"
    "s_waitcnt lgkmcnt(5)\n"
    "v_mfma_f32_32x32x16_bf16 v[32:47], v[216:219], v[120:123], v[32:47]\n"
    "ds_read_b128 v[112:115], v24 offset:28672\n"
    "s_waitcnt lgkmcnt(5)\n"
    "v_mfma_f32_32x32x16_bf16 v[32:47], v[220:223], v[124:127], v[32:47]\n"
    "ds_read_b128 v[116:119], v24 offset:29696\n"
    "s_waitcnt lgkmcnt(5)\n"
    "v_mfma_f32_32x32x16_bf16 v[32:47], v[224:227], v[96:99], v[32:47]\n"
    "ds_read_b128 v[120:123], v24 offset:30720\n"
    "s_waitcnt lgkmcnt(5)\n"
    "v_mfma_f32_32x32x16_bf16 v[32:47], v[228:231], v[100:103], v[32:47]\n"
    "ds_read_b128 v[124:127], v24 offset:31744\n"
    "s_waitcnt lgkmcnt(5)\n"
    "v_mfma_f32_32x32x16_bf16 v[32:47], v[232:235], v[104:107], v[32:47]\n"
    "s_waitcnt lgkmcnt(4)\n"
    "v_mfma_f32_32x32x16_bf16 v[32:47], v[236:239], v[108:111], v[32:47]\n"
    "s_waitcnt lgkmcnt(3)\n"
    "v_mfma_f32_32x32x16_bf16 v[32:47], v[240:243], v[112:115], v[32:47]\n"
    "s_waitcnt lgkmcnt(2)\n"
    "v_mfma_f32_32x32x16_bf16 v[32:47], v[244:247], v[116:119], v[32:47]\n"
    "s_waitcnt lgkmcnt(1)\n"
    "v_mfma_f32_32x32x16_bf16 v[32:47], v[248:251], v[120:123], v[32:47]\n"
    "s_waitcnt lgkmcnt(0)\n"
    "v_mfma_f32_32x32x16_bf16 v[32:47], v[252:255], v[124:127], v[32:47]\n"
    "s_nop 11\n"
    "v_cvt_pk_bf16_f32 v48, v32, v33\n"
    "v_cvt_pk_bf16_f32 v49, v34, v35\n"
    "v_cvt_pk_bf16_f32 v50, v36, v37\n"
    "v_cvt_pk_bf16_f32 v51, v38, v39\n"
    "ds_write_b128 v25, v[48:51] offset:0\n"
    "v_cvt_pk_bf16_f32 v52, v40, v41\n"
    "v_cvt_pk_bf16_f32 v53, v42, v43\n"
    "v_cvt_pk_bf16_f32 v54, v44, v45\n"
    "v_cvt_pk_bf16_f32 v55, v46, v47\n"
    "ds_write_b128 v25, v[52:55] offset:32\n"
    "ds_read_b128 v[56:59], v26\n"
    "ds_read_b128 v[60:63], v26 offset:1280\n"
    "s_cmp_eq_u32 s97, 0\n"
    "s_cbranch_scc1 .La_en57_%=\n"
    "s_waitcnt lgkmcnt(1)\n"
    "global_store_dwordx4 v27, v[56:59], s[40:41] sc1\n"
    "s_waitcnt lgkmcnt(0)\n"
    "global_store_dwordx4 v28, v[60:63], s[40:41] sc1\n"
    ".La_en57_%=:\n"
    "s_endpgm\n"
    ".La_nk3_%=:\n"
    "s_sleep 24\n"
    "s_setprio 3\n"
    "s_cmp_eq_u32 s5, 0\n"
    "s_cbranch_scc0 .La_nk3_%=b\n"
    "buffer_load_dword v6, off, s[16:19], s50 sc1\n"
    ".La_nk3_%=b:\n"
    "s_mov_b32 m0, 160000\n"
    "s_mov_b64 exec, 0xffff\n"
    "buffer_load_dword v213, s[12:15], s98 offen lds\n"
    "s_mov_b64 exec, 15\n"
    "buffer_load_dword v214, s[36:39], s99 offen lds\n"
    "s_mov_b64 exec, -1\n"
    "s_branch .La_go6_%=\n"
    ".La_sl9_%=:\n"
    "s_mov_b32 s4, 262144\n"
    ".La_sp10_%=:\n"
    "s_sleep 4\n"
    "buffer_load_dword v6, off, s[16:19], s50 sc1\n"
    "s_waitcnt vmcnt(0)\n"
    "v_cmp_eq_u32 vcc, v7, v6\n"
    "s_sub_u32 s4, s4, 1\n"
    "s_cbranch_scc1 .La_ok8_%=\n"
    "s_cbranch_vccnz .La_sp10_%=\n"
    "s_branch .La_ok8_%=\n"
    ".La_sl14_%=:\n"
    "s_mov_b32 s4, 4194304\n"
    ".La_sp15_%=:\n"
    "ds_read_b32 v5, v242 offset:132\n"
    "s_waitcnt lgkmcnt(0)\n"
    "v_cmp_gt_u32 vcc, 2, v5\n"
    "s_sub_u32 s4, s4, 1\n"
    "s_cbranch_scc1 .La_ok13_%=\n"
    "s_cbranch_vccnz .La_sp15_%=\n"
    "s_branch .La_ok13_%=\n"
    ".La_ld4_%=:\n"
    "s_waitcnt vmcnt(0)\n"
    "v_add_u32 v32, 1, v7\n"
    "s_mov_b64 exec, 1\n"
    "ds_add_rtn_u32 v33, v3, v4 offset:160\n"
    "s_waitcnt lgkmcnt(0)\n"
    "v_cmp_eq_u32 vcc, 3, v33\n"
    "s_mov_b64 exec, -1\n"
    "s_cbranch_vccz .La_nl5_%=\n"
    "s_mov_b64 exec, 1\n"
    "buffer_store_dword v32, off, s[16:19], s50 sc1\n"
    "s_mov_b64 exec, -1\n"
    "s_branch .La_nl5_%=\n"
    ".La_l316_%=:\n"
    "v_add_u32 v72, 1, v7\n"
    "v_and_b32 v73, 7, v1\n"
    "v_lshlrev_b32 v73, 7, v73\n"
    "s_mov_b32 s4, 0x40000\n"
    ".La_ll18_%=:\n"
    "s_sleep 64\n"
    "s_sub_u32 s4, s4, 1\n"
    "s_cbranch_scc1 .La_l417_%=\n"
    "ds_read_b32 v5, v3 offset:112\n"
    "s_waitcnt lgkmcnt(0)\n"
    "v_cmp_gt_u32 vcc, 1, v5\n"
    "s_nop 0\n"
    "s_cbranch_vccnz .La_ll18_%=\n"
    "buffer_load_dword v68, v73, s[16:19], 0 offen offset:64 sc1\n"
    "s_waitcnt vmcnt(0)\n"
    "v_cmp_ne_u32 vcc, 32, v68\n"
    "v_add_u32 v72, 1, v72\n"
    "s_cbranch_vccz .La_l417_%=\n"
    "s_mov_b64 exec, 1\n"
    "buffer_store_dword v72, off, s[16:19], s50 sc1\n"
    "s_mov_b64 exec, -1\n"
    "s_branch .La_ll18_%=\n"
    ".La_sl20_%=:\n"
    "s_mov_b32 s4, 4194304\n"
    ".La_sp21_%=:\n"
    "s_sleep 4\n"
    "ds_read_b32 v5, v3 offset:104\n"
    "s_waitcnt lgkmcnt(0)\n"
    "v_cmp_gt_u32 vcc, 8, v5\n"
    "s_sub_u32 s4, s4, 1\n"
    "s_cbranch_scc1 .La_ok19_%=\n"
    "s_cbranch_vccnz .La_sp21_%=\n"
    "s_branch .La_ok19_%=\n"
    ".La_sl23_%=:\n"
    "s_mov_b32 s4, 4194304\n"
    ".La_sp24_%=:\n"
    "s_sleep 2\n"
    "ds_read_b32 v5, v3 offset:140\n"
    "s_waitcnt lgkmcnt(0)\n"
    "v_cmp_gt_u32 vcc, 4, v5\n"
    "s_sub_u32 s4, s4, 1\n"
    "s_cbranch_scc1 .La_ok22_%=\n"
    "s_cbranch_vccnz .La_sp24_%=\n"
    "s_branch .La_ok22_%=\n"
    ".La_sl26_%=:\n"
    "s_mov_b32 s4, 4194304\n"
    ".La_sp27_%=:\n"
    "s_sleep 1\n"
    "ds_read_b32 v5, v3 offset:124\n"
    "s_waitcnt lgkmcnt(0)\n"
    "v_cmp_gt_u32 vcc, 4, v5\n"
    "s_sub_u32 s4, s4, 1\n"
    "s_cbranch_scc1 .La_ok25_%=\n"
    "s_cbranch_vccnz .La_sp27_%=\n"
    "s_branch .La_ok25_%=\n"
    ".La_sl30_%=:\n"
    "s_mov_b32 s4, 4194304\n"
    ".La_sp31_%=:\n"
    "s_sleep 1\n"
    "ds_read_b32 v5, v3 offset:144\n"
    "s_waitcnt lgkmcnt(0)\n"
    "v_cmp_gt_u32 vcc, 4, v5\n"
    "s_sub_u32 s4, s4, 1\n"
    "s_cbranch_scc1 .La_ok29_%=\n"
    "s_cbranch_vccnz .La_sp31_%=\n"
    "s_branch .La_ok29_%=\n"
    ".La_sl33_%=:\n"
    "s_mov_b32 s4, 262144\n"
    ".La_sp34_%=:\n"
    "s_sleep 8\n"
    "buffer_load_dword v6, v15, s[16:19], s51 offen sc1\n"
    "s_waitcnt vmcnt(0)\n"
    "v_cmp_ne_u32 vcc, 1, v6\n"
    "s_sub_u32 s4, s4, 1\n"
    "s_cbranch_scc1 .La_ok32_%=\n"
    "s_cbranch_vccnz .La_sp34_%=\n"
    "s_branch .La_ok32_%=\n"
    ".La_sl36_%=:\n"
    "s_mov_b32 s4, 4194304\n"
    ".La_sp37_%=:\n"
    "s_sleep 1\n"
    "ds_read_b32 v5, v3 offset:144\n"
    "s_waitcnt lgkmcnt(0)\n"
    "v_cmp_gt_u32 vcc, 8, v5\n"
    "s_sub_u32 s4, s4, 1\n"
    "s_cbranch_scc1 .La_ok35_%=\n"
    "s_cbranch_vccnz .La_sp37_%=\n"
    "s_branch .La_ok35_%=\n"
    ".La_sl39_%=:\n"
    "s_mov_b32 s4, 4194304\n"
    ".La_sp40_%=:\n"
    "s_sleep 1\n"
    "ds_read_b32 v5, v3 offset:148\n"
    "s_waitcnt lgkmcnt(0)\n"
    "v_cmp_gt_u32 vcc, 4, v5\n"
    "s_sub_u32 s4, s4, 1\n"
    "s_cbranch_scc1 .La_ok38_%=\n"
    "s_cbranch_vccnz .La_sp40_%=\n"
    "s_branch .La_ok38_%=\n"
    ".La_sl46_%=:\n"
    "s_mov_b32 s4, 4194304\n"
    ".La_sp47_%=:\n"
    "ds_read_b32 v5, v3 offset:116\n"
    "s_waitcnt lgkmcnt(0)\n"
    "v_cmp_gt_u32 vcc, 4, v5\n"
    "s_sub_u32 s4, s4, 1\n"
    "s_cbranch_scc1 .La_ok45_%=\n"
    "s_cbranch_vccnz .La_sp47_%=\n"
    "s_branch .La_ok45_%=\n"
    ".La_sl49_%=:\n"
    "s_mov_b32 s4, 4194304\n"
    ".La_sp50_%=:\n"
    "ds_read_b32 v5, v7 offset:152\n"
    "s_waitcnt lgkmcnt(0)\n"
    "v_cmp_gt_u32 vcc, 2, v5\n"
    "s_sub_u32 s4, s4, 1\n"
    "s_cbranch_scc1 .La_ok48_%=\n"
    "s_cbranch_vccnz .La_sp50_%=\n"
    "s_branch .La_ok48_%=\n"
    ".La_sl52_%=:\n"
    "s_mov_b32 s4, 4194304\n"
    ".La_sp53_%=:\n"
    "ds_read_b32 v5, v3 offset:116\n"
    "s_waitcnt lgkmcnt(0)\n"
    "v_cmp_gt_u32 vcc, 4, v5\n"
    "s_sub_u32 s4, s4, 1\n"
    "s_cbranch_scc1 .La_ok51_%=\n"
    "s_cbranch_vccnz .La_sp53_%=\n"
    "s_branch .La_ok51_%=\n"
    ".La_sl55_%=:\n"
    "s_mov_b32 s4, 4194304\n"
    ".La_sp56_%=:\n"
    "ds_read_b32 v5, v7 offset:152\n"
    "s_waitcnt lgkmcnt(0)\n"
    "v_cmp_gt_u32 vcc, 2, v5\n"
    "s_sub_u32 s4, s4, 1\n"
    "s_cbranch_scc1 .La_ok54_%=\n"
    "s_cbranch_vccnz .La_sp56_%=\n"
    "s_branch .La_ok54_%=\n"
        :: "{s[0:1]}"(__builtin_amdgcn_kernarg_segment_ptr()), "{s2}"(blockIdx.x), "{v0}"(threadIdx.x)
        : "memory", "scc", "vcc", "s99", "v255");
}

extern "C" void kernel_launch(void* const* d_in, const int* in_sizes, int n_in,
                              void* d_out, int out_size, void* d_ws, size_t ws_size,
                              hipStream_t stream) {
    Params p;
    p.u = d_in[0]; p.are = d_in[1]; p.aim = d_in[2]; p.bre = d_in[3]; p.bim = d_in[4];
    p.cre = d_in[5]; p.cim = d_in[6]; p.logdt = d_in[7]; p.dskip = d_in[8];
    p.x = d_in[9]; p.wqkv = d_in[10]; p.wo = d_in[11];
    p.out = d_out; p.ws = d_ws;
    (void)hipFuncSetAttribute((const void*)fused_s4_mha, hipFuncAttributeMaxDynamicSharedMemorySize, 160256);
    fused_s4_mha<<<256, 512, 160256, stream>>>(p);
}
```
